```python
import torch

def _softmax(x, axis=-1):
    return torch.softmax(x, dim=axis)
def _gelu(x):
    return torch.nn.functional.gelu(x, approximate="tanh")
def _silu(x):
    return torch.nn.functional.silu(x)
def _sigmoid(x):
    return torch.sigmoid(x)
def _layernorm(x, axis=-1, eps=1e-5):
    m = x.mean(dim=axis, keepdim=True)
    v = x.var(dim=axis, keepdim=True, unbiased=False)
    return (x - m) / torch.sqrt(v + eps)
def _scatter_add(tgt, idx, src, axis=0):
    return tgt.clone().index_add_(axis, idx, src)
def _segment_sum(src, seg, n):
    out = torch.zeros((n,) + src.shape[1:], dtype=src.dtype)
    return out.index_add_(0, seg, src)
def _pad(x, pw):
    flat = tuple(v for pair in reversed(pw) for v in pair)
    return torch.nn.functional.pad(x, flat)

kernel_name = 'parallel_sum__ssm_conv_s5__glu_gated_swiglu__prompt'

def setup_inputs():

    _w = (lambda a: (lambda t: t.to(torch.bfloat16) if t.is_floating_point() else t)(torch.as_tensor(a)))
    def _A_setup():
        B, S, D = 1, 2048, 512
        P = D
        Lambda_re = -0.5 * (lambda _x: torch.exp(torch.as_tensor(_x)))((lambda *s: torch.randn(*s, dtype=torch.bfloat16) * 0.2)(P))
        Lambda_im = (lambda *s: torch.randn(*s, dtype=torch.bfloat16) * 0.8)(P) * 1.0
        B_re = (lambda *s: torch.randn(*s, dtype=torch.bfloat16) * 0.2)(P, D) * (D ** -0.5)
        B_im = (lambda *s: torch.randn(*s, dtype=torch.bfloat16) * 0.2)(P, D) * (D ** -0.5)
        C_re = (lambda *s: torch.randn(*s, dtype=torch.bfloat16) * 0.2)(D, P) * (P ** -0.5)
        C_im = (lambda *s: torch.randn(*s, dtype=torch.bfloat16) * 0.2)(D, P) * (P ** -0.5)
        log_dt = (lambda *s: torch.randn(*s, dtype=torch.bfloat16) * 0.2)(P) * 0.3 - 2.0
        Dskip = (lambda *s: torch.randn(*s, dtype=torch.bfloat16) * 0.05)(D)
        return {
            "x": _w((lambda *s: torch.randn(*s, dtype=torch.bfloat16) * 0.05)(B, S, D)),
            "Lambda_re": _w(Lambda_re),
            "Lambda_im": _w(Lambda_im),
            "B_re": _w(B_re),
            "B_im": _w(B_im),
            "C_re": _w(C_re),
            "C_im": _w(C_im),
            "log_dt": _w(log_dt),
            "Dskip": _w(Dskip),
        }
    _A_in = _A_setup()

    def _B_setup():
        B, S, D, F = 1, 2048, 512, 1792
        return {
            "x": _w((lambda *s: torch.randn(*s, dtype=torch.bfloat16) * 0.05)(B, S, D)),
            "w1": _w((lambda *s: torch.randn(*s, dtype=torch.bfloat16) * 0.4)(D, F) * (2.0 / D) ** 0.5),
            "w3": _w((lambda *s: torch.randn(*s, dtype=torch.bfloat16) * 0.4)(D, F) * (2.0 / D) ** 0.5),
            "w2": _w((lambda *s: torch.randn(*s, dtype=torch.bfloat16) * 0.4)(F, D) * (2.0 / F) ** 0.5),
        }
    _B_in = _B_setup()

    return {
        "A_x": _A_in["x"],
        "A_Lambda_re": _A_in["Lambda_re"],
        "A_Lambda_im": _A_in["Lambda_im"],
        "A_B_re": _A_in["B_re"],
        "A_B_im": _A_in["B_im"],
        "A_C_re": _A_in["C_re"],
        "A_C_im": _A_in["C_im"],
        "A_log_dt": _A_in["log_dt"],
        "A_Dskip": _A_in["Dskip"],
        "B_x": _B_in["x"],
        "B_w1": _B_in["w1"],
        "B_w3": _B_in["w3"],
        "B_w2": _B_in["w2"],
    }

def reference(A_x, A_Lambda_re, A_Lambda_im, A_B_re, A_B_im, A_C_re, A_C_im, A_log_dt, A_Dskip, B_x, B_w1, B_w3, B_w2):
    def _A_ref(x, Lambda_re, Lambda_im, B_re, B_im, C_re, C_im, log_dt, Dskip):
        Bsz, L, H = x.shape
        Lam = Lambda_re + 1j * Lambda_im
        Bmat = B_re + 1j * B_im
        Cmat = C_re + 1j * C_im
        dt = torch.exp(log_dt)
        Lam_bar = torch.exp(dt * Lam)
        B_bar = ((Lam_bar - 1.0) / Lam)[:, None] * Bmat
        Bu = torch.einsum("bld,pd->blp", x + 0j, B_bar)
        h = torch.zeros_like(Bu[:, 0, :])
        hs = []
        for t in range(L):
            h = Lam_bar[None, :] * h + Bu[:, t, :]
            hs.append(h)
        hseq = torch.stack(hs, 1)
        y = 2.0 * torch.einsum("blp,dp->bld", hseq, Cmat).real + x * Dskip[None, None, :]
        return y

    def _B_ref(x, w1, w3, w2):
        gate = torch.einsum("bsd,df->bsf", x, w1)
        up = torch.einsum("bsd,df->bsf", x, w3)
        h = _silu(gate) * up
        out = torch.einsum("bsf,fd->bsd", h, w2)
        return out

    x = A_x
    xn = _layernorm(x, axis=-1)
    return x + _A_ref(xn, A_Lambda_re, A_Lambda_im, A_B_re, A_B_im, A_C_re, A_C_im, A_log_dt, A_Dskip) + _B_ref(xn, B_w1, B_w3, B_w2)
```

```cpp
#include <hip/hip_runtime.h>
#include <hip/hip_bf16.h>
#include <stdint.h>

typedef unsigned short u16;
__device__ __forceinline__ float bf2f(u16 b){ return __builtin_bit_cast(float, (unsigned)b<<16); }
__device__ __forceinline__ u16 f2bf(float f){ __hip_bfloat16 h = __float2bfloat16(f); return __builtin_bit_cast(u16,h); }
__device__ __forceinline__ float rbf(float f){ return bf2f(f2bf(f)); }

constexpr int S=2048, D=512, P=512, F=1792;

__global__ void k_ln(const u16* x, u16* xn){
  int row=blockIdx.x; int t=threadIdx.x;
  float v[8]; float s=0;
  for(int i=0;i<8;i++){ v[i]=bf2f(x[row*D+t*8+i]); s+=v[i]; }
  for(int o=32;o;o>>=1) s+=__shfl_xor(s,o);
  float m=s/D; float q=0;
  for(int i=0;i<8;i++){ float d=v[i]-m; q+=d*d; }
  for(int o=32;o;o>>=1) q+=__shfl_xor(q,o);
  float mb=rbf(m), vb=rbf(q/D), sb=rbf(sqrtf(rbf(vb+1e-5f)));
  for(int i=0;i<8;i++) xn[row*D+t*8+i]=f2bf(rbf(v[i]-mb)/sb);
}
__global__ void k_u(const u16* xn,const u16* Bre,const u16* Bim,float* U){
  int t=blockIdx.x; int p=threadIdx.x;
  __shared__ float xs[D];
  xs[p]=bf2f(xn[t*D+p]); __syncthreads();
  float a=0,b=0;
  for(int d=0;d<D;d++){ a+=xs[d]*bf2f(Bre[p*D+d]); b+=xs[d]*bf2f(Bim[p*D+d]); }
  U[(t*P+p)*2]=a; U[(t*P+p)*2+1]=b;
}
__global__ void k_scan(const u16* lre,const u16* lim,const u16* ldt,float* U){
  int p=blockIdx.x*64+threadIdx.x;
  float lr=bf2f(lre[p]), li=bf2f(lim[p]); float dt=rbf(expf(bf2f(ldt[p])));
  float e=expf(dt*lr); float ar=e*cosf(dt*li), ai=e*sinf(dt*li);
  float nr=ar-1.f, ni=ai; float den=lr*lr+li*li;
  float cr=(nr*lr+ni*li)/den, ci=(ni*lr-nr*li)/den;
  float hr=0,hi=0;
  for(int t=0;t<S;t++){
    float ur=U[(t*P+p)*2], ui=U[(t*P+p)*2+1];
    float br=cr*ur-ci*ui, bi=cr*ui+ci*ur;
    float r=ar*hr-ai*hi+br, i=ar*hi+ai*hr+bi; hr=r;hi=i;
    U[(t*P+p)*2]=hr; U[(t*P+p)*2+1]=hi;
  }
}
__global__ void k_glu(const u16* xn,const u16* w1,const u16* w3,u16* H){
  int t=blockIdx.x; __shared__ float xs[D];
  for(int i=threadIdx.x;i<D;i+=256) xs[i]=bf2f(xn[t*D+i]); __syncthreads();
  for(int f=threadIdx.x;f<F;f+=256){
    float g=0,u=0;
    for(int d=0;d<D;d++){ g+=xs[d]*bf2f(w1[d*F+f]); u+=xs[d]*bf2f(w3[d*F+f]); }
    g=rbf(g);u=rbf(u);
    float s=rbf(g/(1.f+expf(-g)));
    H[t*F+f]=f2bf(s*u);
  }
}
__global__ void k_out(const u16* x,const u16* xn,const float* U,const u16* Cre,const u16* Cim,const u16* Dk,const u16* H,const u16* w2,float* out){
  int t=blockIdx.x; int d=threadIdx.x;
  __shared__ float hs[2*P]; __shared__ float gs[F];
  for(int i=d;i<2*P;i+=512) hs[i]=U[t*2*P+i];
  for(int i=d;i<F;i+=512) gs[i]=bf2f(H[t*F+i]);
  __syncthreads();
  float y=0; for(int p=0;p<P;p++) y+=hs[2*p]*bf2f(Cre[d*P+p])-hs[2*p+1]*bf2f(Cim[d*P+p]);
  float o=0; for(int f=0;f<F;f++) o+=gs[f]*bf2f(w2[f*D+d]);
  float sk=rbf(bf2f(xn[t*D+d])*bf2f(Dk[d]));
  out[t*D+d]=bf2f(x[t*D+d])+(2.f*y+sk)+rbf(o);
}
extern "C" void kernel_launch(void* const* d_in, const int* in_sizes, int n_in,
                              void* d_out, int out_size, void* d_ws, size_t ws_size,
                              hipStream_t stream) {
  const u16* x=(const u16*)d_in[0];
  char* ws=(char*)d_ws;
  u16* xn=(u16*)ws; float* U=(float*)(ws+(4<<20)); u16* H=(u16*)(ws+(16<<20));
  k_ln<<<S,64,0,stream>>>(x,xn);
  k_u<<<S,512,0,stream>>>(xn,(const u16*)d_in[3],(const u16*)d_in[4],U);
  k_scan<<<8,64,0,stream>>>((const u16*)d_in[1],(const u16*)d_in[2],(const u16*)d_in[7],U);
  k_glu<<<S,256,0,stream>>>(xn,(const u16*)d_in[10],(const u16*)d_in[11],H);
  k_out<<<S,512,0,stream>>>(x,xn,U,(const u16*)d_in[5],(const u16*)d_in[6],(const u16*)d_in[8],H,(const u16*)d_in[12],(float*)d_out);
}
```

```cpp
#include <hip/hip_runtime.h>
#include <stdint.h>

typedef unsigned short u16;
typedef unsigned int u32;
typedef unsigned long long u64;
typedef u32 v4u __attribute__((ext_vector_type(4)));
typedef u32 v2u __attribute__((ext_vector_type(2)));
typedef float f4 __attribute__((ext_vector_type(4)));
typedef short v4s __attribute__((ext_vector_type(4)));
typedef __bf16 v8bf __attribute__((ext_vector_type(8)));
typedef __bf16 v2bf __attribute__((ext_vector_type(2)));
#define LAS __attribute__((address_space(3)))
#define GAS __attribute__((address_space(1)))
#define DI __device__ __forceinline__
#define RLX_AGENT __ATOMIC_RELAXED, __HIP_MEMORY_SCOPE_AGENT

#define STAMPS 0

namespace {
constexpr int SM_OFF = 32768, G_OFF = 36864, G3D = 7184, SLOT = 51456, AUXO = 3 * SLOT;
constexpr int ST_MEAN = AUXO, ST_RS = AUXO + 1024, XCH = AUXO + 2048, CSP = AUXO + 2560, TRB = CSP;
constexpr int LDS_BYTES = 163840;
constexpr int WREG = 19200;
constexpr int WS_EP = 0, WS_FL = 4096, WS_AG = 65536, WS_HB = 1 << 20, HB_TILE = 88 * 1024;
constexpr int WS_BYTES = WS_HB + 128 * HB_TILE;
constexpr int WS_STAMP = 32 << 20;
constexpr int AUX_SC1 = 16;
constexpr u32 SPIN_CAP = 1u << 17;

DI float bf2f(u32 b) { return __builtin_bit_cast(float, b << 16); }
DI float u2f(u32 b) { return __builtin_bit_cast(float, b); }
DI u32 f2u(float f) { return __builtin_bit_cast(u32, f); }
DI float rbf(float f) { return (float)(__bf16)f; }
DI u32 pk2(float a, float b) { v2bf v = {(__bf16)a, (__bf16)b}; return __builtin_bit_cast(u32, v); }
DI f4 mfma(v4u a, v4u b, f4 c) {
  return __builtin_amdgcn_mfma_f32_16x16x32_bf16(__builtin_bit_cast(v8bf, a), __builtin_bit_cast(v8bf, b), c, 0, 0, 0);
}
DI float dot2(u32 a, u32 b, float c) {
  return __builtin_amdgcn_fdot2_f32_bf16(__builtin_bit_cast(v2bf, a), __builtin_bit_cast(v2bf, b), c, false);
}
DI v4u lds128(LAS char* L, u32 a) { return *(LAS v4u*)(L + a); }
DI v4u tr2(LAS char* L, u32 a, int d) {
  v2u lo = __builtin_bit_cast(v2u, __builtin_amdgcn_ds_read_tr16_b64_v4i16((LAS v4s*)(L + a)));
  v2u hi = __builtin_bit_cast(v2u, __builtin_amdgcn_ds_read_tr16_b64_v4i16((LAS v4s*)(L + a + d)));
  return v4u{lo.x, lo.y, hi.x, hi.y};
}
DI void glds(u32 voff, const void* sbase, u32 ldst) {
  u32 keep;
  asm volatile("s_mov_b32 %0, m0\n\ts_mov_b32 m0, %3\n\ts_nop 2\n\tglobal_load_lds_dwordx4 %1, %2\n\ts_mov_b32 m0, %0"
               : "=&s"(keep) : "v"(voff), "s"(sbase), "s"(ldst) : "memory");
}
DI void glds56(u32 voff, const void* sbase, u32 ldst) {
  u32 keep;
  asm volatile("s_mov_b32 %0, m0\n\ts_mov_b32 m0, %3\n\ts_mov_b32 exec_hi, 0xffffff\n\ts_nop 1\n\tglobal_load_lds_dwordx4 %1, %2\n\t"
               "s_mov_b32 exec_hi, -1\n\ts_mov_b32 m0, %0"
               : "=&s"(keep) : "v"(voff), "s"(sbase), "s"(ldst) : "memory");
}
#define WAITV(n) asm volatile("s_waitcnt vmcnt(" #n ")" ::: "memory")
#define WAITL0() do { asm volatile("" ::: "memory"); __builtin_amdgcn_s_waitcnt(0xC07F); asm volatile("" ::: "memory"); } while (0)
#define BAR() __builtin_amdgcn_s_barrier()
#define SCHED() __builtin_amdgcn_sched_barrier(0)

#define PIN4(a) asm volatile("" : "+v"((a)[0]), "+v"((a)[1]), "+v"((a)[2]), "+v"((a)[3]))

struct cf { float r, i; };
DI cf cmul(cf a, cf b) { return cf{a.r * b.r - a.i * b.i, a.r * b.i + a.i * b.r}; }
DI cf cfma(cf a, cf b, cf c) { return cf{c.r + a.r * b.r - a.i * b.i, c.i + a.r * b.i + a.i * b.r}; }
DI cf csel(bool p, cf a, cf b) { return cf{p ? a.r : b.r, p ? a.i : b.i}; }

struct Args {
  const u16 *x, *lre, *lim, *bre, *bim, *cre, *cim, *ldt, *dsk, *w1, *w3, *w2;
  float* out;
  unsigned char* ws;
};

#define STAMP(k) do { } while (0)
#define CSTAMP(k) do { } while (0)
#define ABL 0

DI float swiglu(float tg, float tu, float nrl, float ir2) {
  float e = __builtin_amdgcn_exp2f(tg * nrl);
  return tg * tu * __builtin_amdgcn_rcpf(__builtin_fmaf(e, ir2, ir2));
}

template <int H> struct Fr { v4u x[4]; v4u w[H == 0 ? 5 : 4]; };

template <int H> DI void ldfr(Fr<H>& F, LAS char* L, u32 xa, u32 sa, u32 ga, u32 gm, int ks) {
#pragma unroll
  for (int i = 0; i < 4; i++) F.x[i] = lds128(L, xa + i * 512);
  if (H == 0) {
    F.w[0] = lds128(L, sa);
    F.w[1] = lds128(L, sa + 2048);
    F.w[2] = tr2(L, ga + ks * 3584, 112);
    F.w[3] = tr2(L, ga + ks * 3584 + G3D, 112);
    F.w[4] = tr2(L, gm + ks * 3584, 112);
  } else {
    F.w[0] = tr2(L, ga + ks * 3584 + 32, 112);
    F.w[1] = tr2(L, ga + ks * 3584 + G3D + 32, 112);
    F.w[2] = tr2(L, ga + ks * 3584 + 64, 112);
    F.w[3] = tr2(L, ga + ks * 3584 + G3D + 64, 112);
  }
}

template <int H>
DI void kstep(const Fr<H>& F, f4 (&acc)[H == 0 ? 5 : 4][4], f4 (&cs)[H == 0 ? 5 : 4], float (&s1)[4], float (&s2)[4], bool docs) {
  constexpr int NT = H == 0 ? 5 : 4;
  const v4u ones = {0x3F803F80u, 0x3F803F80u, 0x3F803F80u, 0x3F803F80u};
#pragma unroll
  for (int t = 0; t < NT; t++)
#pragma unroll
    for (int i = 0; i < 4; i++) {
      if (H == 0 && t < 2) acc[t][i] = mfma(F.x[i], F.w[t], acc[t][i]);
      else acc[t][i] = mfma(F.w[t], F.x[i], acc[t][i]);
    }
  if (H == 1 && !(ABL & 8)) {
#pragma unroll
    for (int i = 0; i < 4; i++)
#pragma unroll
      for (int d = 0; d < 4; d++) {
        s1[i] = dot2(F.x[i][d], 0x3F803F80u, s1[i]);
        s2[i] = dot2(F.x[i][d], F.x[i][d], s2[i]);
      }
#pragma unroll
    for (int k = 0; k < 16; k++) {
      __builtin_amdgcn_sched_group_barrier(0x8, 1, 0);
      __builtin_amdgcn_sched_group_barrier(0x2, 2, 0);
    }
    PIN4(s1);
    PIN4(s2);
  }
  if (docs) {
#pragma unroll
    for (int t = 0; t < NT; t++) {
      if (H == 0 && t < 2) cs[t] = mfma(ones, F.w[t], cs[t]);
      else cs[t] = mfma(F.w[t], ones, cs[t]);
    }
  }
}

template <int H>
DI void phase1(const Args& p, LAS char* L, u32 lbase, __amdgpu_buffer_rsrc_t rs, int g, int j, int rot, int w, int lane, u32& tagLo, u32& tagHi) {
  constexpr int NT = H == 0 ? 5 : 4;
  const int tg = w & 3, c = lane & 15, q = lane >> 4, r4 = c >> 2, pp = lane & 3;
  v2u ep;
  u32 pl0 = 0, pl1 = 0, pl2 = 0;
  {
    const u32 zero = 0;
    asm volatile("global_load_dwordx2 %0, %1, %2 sc1" : "=v"(ep) : "v"(zero), "s"(p.ws + WS_EP) : "memory");
    if (H == 0) {
      const u32 o = (16 * j + c) * 2;
      asm volatile("global_load_ushort %0, %1, %2" : "=v"(pl0) : "v"(o), "s"(p.lre) : "memory");
      asm volatile("global_load_ushort %0, %1, %2" : "=v"(pl1) : "v"(o), "s"(p.lim) : "memory");
      asm volatile("global_load_ushort %0, %1, %2" : "=v"(pl2) : "v"(o), "s"(p.ldt) : "memory");
    }
  }
  u32 vx[4];
#pragma unroll
  for (int e = 0; e < 4; e++) {
    const int row = 32 * w + 8 * e + (lane >> 3);
    const int swz = 2 * ((row >> 4) & 3) + ((row >> 1) & 1);
    vx[e] = row * 1024 + (((lane & 7) ^ swz) << 4);
  }
  const int gl = lane < 56 ? lane : 0;
  const int gpos = gl / 7, gch = gl - gpos * 7;
  const u32 vg = (8 * w + 4 * (gpos & 1) + (gpos >> 1)) * 3584 + gch * 16;
  const int srow = 8 * (w & 1) + (lane >> 3);
  const u32 vs = srow * 1024 + (((lane & 7) ^ ((srow >> 1) & 7)) << 4);
  const char* xg = (const char*)p.x + (size_t)g * (256 * 1024);
  const char* w1g = (const char*)p.w1 + j * 112;
  const char* w3g = (const char*)p.w3 + j * 112;
  const char* bg = (const char*)((w & 2) ? p.bim : p.bre) + (size_t)j * (16 * 1024);
  auto issue = [&](int t, u32 slot) {
    const int kst = (rot + t) & 7;
    const u32 d = lbase + slot;
#pragma unroll
    for (int e = 0; e < 4; e++) if (!(ABL & 2) || t < 3) glds(vx[e], xg + kst * 128, d + (32 * w + 8 * e) * 128);
    if (!(ABL & 4) || t < 3) glds56(vg, w1g + kst * (64 * 3584), d + G_OFF + w * 896);
    if (!(ABL & 4) || t < 3) glds56(vg, w3g + kst * (64 * 3584), d + G_OFF + G3D + w * 896);
    if (H == 0) glds(vs, bg + kst * 128, d + SM_OFF + w * 1024);
  };
  issue(0, 0);
  issue(1, SLOT);
  issue(2, 2 * SLOT);
  const int swx = 2 * r4 + ((c >> 1) & 1);
  const u32 xa0 = (64 * tg + 16 * r4 + (c & 3)) * 128 + ((q ^ swx) << 4), xa1 = xa0 ^ 64;
  const u32 sa0 = SM_OFF + c * 128 + ((q ^ ((c >> 1) & 7)) << 4), sa1 = sa0 ^ 64;
  const u32 ga = G_OFF + q * 896 + r4 * 224 + 8 * pp;
  const u32 gm = ga + 96 + (pp >= 2 ? G3D - 16 : 0);
  if (H == 0) asm volatile("s_waitcnt vmcnt(21)" : "+v"(ep), "+v"(pl0), "+v"(pl1), "+v"(pl2)::"memory");
  else asm volatile("s_waitcnt vmcnt(18)" : "+v"(ep)::"memory");
  {
    const u64 tag = (((u64)ep.y << 32) | ep.x) + 1;
    tagLo = (u32)tag;
    tagHi = (u32)(tag >> 32);
  }
  cf a1{0, 0}, c2{0, 0}, a16{0, 0}, a32{0, 0}, a64{0, 0}, wq{0, 0}, wt{0, 0}, wg0{0, 0}, wg1{0, 0};
  if (H == 0) {
    const float lr = bf2f(pl0), li = bf2f(pl1), dt = rbf(expf(bf2f(pl2)));
    float sn, cn;
    sincosf(dt * li, &sn, &cn);
    const float e = expf(dt * lr);
    a1 = cf{e * cn, e * sn};
    const float nr = a1.r - 1.f, ni = a1.i, den = 2.f / (lr * lr + li * li);
    c2 = cf{(nr * lr + ni * li) * den, (ni * lr - nr * li) * den};
    cf t = cmul(a1, a1); t = cmul(t, t); t = cmul(t, t);
    a16 = cmul(t, t); a32 = cmul(a16, a16); a64 = cmul(a32, a32);
    const cf a128 = cmul(a64, a64), a256 = cmul(a128, a128), a512 = cmul(a256, a256), a1024 = cmul(a512, a512);
    const cf one{1.f, 0.f}, zero{0.f, 0.f};
    wq = cmul(csel(q & 1, a16, one), csel(q & 2, a32, one));
    wt = cmul(csel(tg & 1, a64, one), csel(tg & 2, a128, one));
    const int e0 = g - 1 - q, e1 = g - 5 - q;
    wg0 = cmul(cmul(csel(e0 & 1, a256, one), csel(e0 & 2, a512, one)), csel(e0 & 4, a1024, one));
    wg0 = csel(e0 >= 0, wg0, zero);
    wg1 = cmul(csel(e1 & 1, a256, one), csel(e1 & 2, a512, one));
    wg1 = csel(e1 >= 0, wg1, zero);
  }
  f4 acc[NT][4], cs[NT];
  float s1[4], s2[4];
#pragma unroll
  for (int t = 0; t < NT; t++) {
    cs[t] = f4{0, 0, 0, 0};
#pragma unroll
    for (int i = 0; i < 4; i++) acc[t][i] = f4{0, 0, 0, 0};
  }
#pragma unroll
  for (int i = 0; i < 4; i++) s1[i] = s2[i] = 0.f;
  STAMP(1);
  if (H == 0) WAITV(14); else WAITV(12);
  BAR();
  STAMP(2);
  CSTAMP(14);
  Fr<H> F0, F1;
  ldfr<H>(F0, L, xa0, sa0, ga, gm, 0);
  u32 cur = 0;
#pragma unroll 1
  for (int s = 0; s < 8; s++) {
    ldfr<H>(F1, L, cur + xa1, cur + sa1, cur + ga, cur + gm, 1);
    SCHED();
    kstep<H>(F0, acc, cs, s1, s2, ((2 * s) & 3) == tg);
    SCHED();
    u32 nxt = cur + SLOT;
    if (nxt == 3 * SLOT) nxt = 0;
    WAITL0();
    if (s < 6) { if (H == 0) WAITV(7); else WAITV(6); }
    else WAITV(0);
    BAR();
    if (s < 5 && !(ABL & 1)) issue(s + 3, cur);
    ldfr<H>(F0, L, nxt + xa0, nxt + sa0, nxt + ga, nxt + gm, 0);
    SCHED();
    kstep<H>(F1, acc, cs, s1, s2, ((2 * s + 1) & 3) == tg);
    SCHED();
    cur = nxt;
  }
  STAMP(3);
  CSTAMP(15);
  LAS float* Lf = (LAS float*)L;
  if (H == 1) {
    float m = 0.f, rsd = 0.f;
#pragma unroll
    for (int i = 0; i < 4; i++) {
      float a = s1[i], b = s2[i];
      a += __shfl_xor(a, 16); b += __shfl_xor(b, 16);
      a += __shfl_xor(a, 32); b += __shfl_xor(b, 32);
      if (q == i) { m = a; rsd = b; }
    }
    m *= (1.f / 512.f);
    const float var = fmaxf(rsd * (1.f / 512.f) - m * m, 0.f);
    const float sd = rbf(sqrtf(rbf(rbf(var) + 1e-5f)));
    const int tok = 64 * tg + 16 * r4 + 4 * q + (c & 3);
    Lf[ST_MEAN / 4 + tok] = rbf(m);
    Lf[ST_RS / 4 + tok] = 1.f / sd;
  }
  {
    LAS float* cp = Lf + CSP / 4 + tg * 144;
    if (H == 0) {
      if (q == 0) { cp[c] = cs[0][0]; cp[16 + c] = cs[1][0]; }
      if (c == 0) {
#pragma unroll
        for (int t = 2; t < 5; t++) *(LAS f4*)(cp + 16 * t + 4 * q) = cs[t];
      }
    } else if (c == 0) {
#pragma unroll
      for (int t = 0; t < 4; t++) *(LAS f4*)(cp + 80 + 16 * t + 4 * q) = cs[t];
    }
  }
  WAITL0();
  BAR();
#pragma unroll
  for (int t = 0; t < NT; t++) {
    if (H == 0 && t < 2) {
      const LAS float* cp = Lf + CSP / 4 + 16 * t + c;
      cs[t][0] = (cp[0] + cp[144]) + (cp[288] + cp[432]);
    } else {
      const LAS f4* cp = (const LAS f4*)(Lf + CSP / 4 + (H == 0 ? 0 : 80) + 16 * t + 4 * q);
      cs[t] = (cp[0] + cp[36]) + (cp[72] + cp[108]);
    }
  }
  float mcol[4], rcol[4];
#pragma unroll
  for (int i = 0; i < 4; i++) {
    const int tok = 64 * tg + 16 * r4 + 4 * i + (c & 3);
    mcol[i] = Lf[ST_MEAN / 4 + tok];
    rcol[i] = Lf[ST_RS / 4 + tok];
  }
  const int tile0 = (g * 4 + tg) * 4;
  const int oct0 = 44 * (j >> 2) + 16 + 7 * (j & 3);
  const u32 hb0 = WS_HB + tile0 * HB_TILE + c * 16;
  GAS u64* flg = (GAS u64*)(p.ws + WS_FL) + ((g * 4 + tg) * 32 + j) * 4;
  const u64 tag = ((u64)tagHi << 32) | tagLo;
  constexpr float NL2E = -1.4426950408889634f;
  if (H == 1) {
#pragma unroll
    for (int i = 0; i < 4; i++) {
      if (i == 2) { WAITL0(); BAR(); }
      const float nm = -mcol[i], nrl = rcol[i] * NL2E, ir2 = 1.f / (rcol[i] * rcol[i]);
      float v[8];
#pragma unroll
      for (int t = 0; t < 2; t++)
#pragma unroll
        for (int r = 0; r < 4; r++)
          v[4 * t + r] = swiglu(__builtin_fmaf(nm, cs[2 * t][r], acc[2 * t][i][r]), __builtin_fmaf(nm, cs[2 * t + 1][r], acc[2 * t + 1][i][r]), nrl, ir2);
      auto sx = __builtin_amdgcn_permlane16_swap(pk2(v[0], v[1]), pk2(v[4], v[5]), false, false);
      auto sy = __builtin_amdgcn_permlane16_swap(pk2(v[2], v[3]), pk2(v[6], v[7]), false, false);
      const v4u o = {sx[0], sy[0], sx[1], sy[1]};
      __builtin_amdgcn_raw_buffer_store_b128(o, rs, hb0 + i * HB_TILE + (oct0 + 2 + (q >> 1) + 2 * (q & 1)) * 256, 0, AUX_SC1);
    }
    WAITV(0);
    if (lane == 0) __hip_atomic_store(flg + 1, tag, RLX_AGENT);
  } else {
    cf u[16];
#pragma unroll
    for (int i = 0; i < 4; i++) {
      const f4 mr = *(const LAS f4*)(Lf + ST_MEAN / 4 + 64 * tg + 16 * q + 4 * i);
      const f4 rr = *(const LAS f4*)(Lf + ST_RS / 4 + 64 * tg + 16 * q + 4 * i);
#pragma unroll
      for (int r = 0; r < 4; r++) {
        u[4 * i + r].r = __builtin_fmaf(-mr[r], cs[0][0], acc[0][i][r]) * rr[r];
        u[4 * i + r].i = __builtin_fmaf(-mr[r], cs[1][0], acc[1][i][r]) * rr[r];
      }
    }
    cf e = u[0];
#pragma unroll
    for (int k = 1; k < 16; k++) e = cfma(a1, e, u[k]);
    {
      cf t{__shfl_up(e.r, 16), __shfl_up(e.i, 16)};
      if (q >= 1) e = cfma(a16, t, e);
      t = cf{__shfl_up(e.r, 32), __shfl_up(e.i, 32)};
      if (q >= 2) e = cfma(a32, t, e);
    }
    cf pq{__shfl_up(e.r, 16), __shfl_up(e.i, 16)};
    if (q == 0) pq = cf{0.f, 0.f};
    if (q == 3) { Lf[XCH / 4 + (tg * 16 + c) * 2] = e.r; Lf[XCH / 4 + (tg * 16 + c) * 2 + 1] = e.i; }
    WAITL0();
    BAR();
    cf lw{0.f, 0.f}, eb{0.f, 0.f};
#pragma unroll
    for (int t = 0; t < 4; t++) {
      if (t == tg) lw = eb;
      const cf wv{Lf[XCH / 4 + (t * 16 + c) * 2], Lf[XCH / 4 + (t * 16 + c) * 2 + 1]};
      eb = cfma(a64, eb, wv);
    }
    if (tg == 0 && q == 0) {
      const v4u o = {f2u(eb.r), tagLo, f2u(eb.i), tagHi};
      __builtin_amdgcn_raw_buffer_store_b128(o, rs, WS_AG + (g * 512 + 16 * j + c) * 16, 0, AUX_SC1);
    }
    STAMP(4);
    u32 zx[2], zy[2];
#pragma unroll
    for (int ip = 0; ip < 2; ip++) {
      u32 px[2], py[2];
      f4 tm[2];
#pragma unroll
      for (int ii = 0; ii < 2; ii++) {
        const int i = 2 * ip + ii;
        const float nm = -mcol[i], nrl = rcol[i] * NL2E, ir2 = 1.f / (rcol[i] * rcol[i]);
        float v[4];
#pragma unroll
        for (int r = 0; r < 4; r++) {
          v[r] = swiglu(__builtin_fmaf(nm, cs[2][r], acc[2][i][r]), __builtin_fmaf(nm, cs[3][r], acc[3][i][r]), nrl, ir2);
          tm[ii][r] = __builtin_fmaf(nm, cs[4][r], acc[4][i][r]);
        }
        px[ii] = pk2(v[0], v[1]);
        py[ii] = pk2(v[2], v[3]);
      }
      auto sx = __builtin_amdgcn_permlane16_swap(px[0], px[1], false, false);
      auto sy = __builtin_amdgcn_permlane16_swap(py[0], py[1], false, false);
      const v4u o = {sx[0], sy[0], sx[1], sy[1]};
      __builtin_amdgcn_raw_buffer_store_b128(o, rs, hb0 + (2 * ip + (q & 1)) * HB_TILE + (oct0 + (q >> 1)) * 256, 0, AUX_SC1);
      float rlo = rcol[2 * ip];
      asm volatile("" : "+v"(rlo));
      const float rsel = q < 2 ? rlo : rcol[2 * ip + 1];
      const float nrl = rsel * NL2E, ir2 = 1.f / (rsel * rsel);
      float v[4];
#pragma unroll
      for (int r = 0; r < 4; r++) {
        auto s = __builtin_amdgcn_permlane32_swap(f2u(tm[0][r]), f2u(tm[1][r]), false, false);
        v[r] = swiglu(u2f(s[0]), u2f(s[1]), nrl, ir2);
      }
      zx[ip] = pk2(v[0], v[1]);
      zy[ip] = pk2(v[2], v[3]);
    }
    {
      auto sx = __builtin_amdgcn_permlane16_swap(zx[0], zx[1], false, false);
      auto sy = __builtin_amdgcn_permlane16_swap(zy[0], zy[1], false, false);
      const v4u o = {sx[0], sy[0], sx[1], sy[1]};
      __builtin_amdgcn_raw_buffer_store_b128(o, rs, hb0 + (2 * (q & 1) + (q >> 1)) * HB_TILE + (oct0 + 6) * 256, 0, AUX_SC1);
    }
#pragma unroll
    for (int k = 0; k < 16; k++) u[k] = cmul(c2, u[k]);
    WAITV(0);
    if (lane == 0) __hip_atomic_store(flg + 0, tag, RLX_AGENT);
    STAMP(5);
    cf hin{0.f, 0.f};
    if (g > 0) {
      const u32 ao = WS_AG + (q * 512 + 16 * j + c) * 16;
      v4u g0, g1;
      for (u32 spins = 0;; spins++) {
        g0 = __builtin_amdgcn_raw_buffer_load_b128(rs, ao, 0, AUX_SC1);
        g1 = __builtin_amdgcn_raw_buffer_load_b128(rs, ao + 4 * 512 * 16, 0, AUX_SC1);
        const bool ok = (q >= g || (g0.y == tagLo && g0.w == tagHi)) && (q + 4 >= g || (g1.y == tagLo && g1.w == tagHi));
        if (__all(ok) || spins > SPIN_CAP) break;
        __builtin_amdgcn_s_sleep(1);
      }
      const cf z{0.f, 0.f};
      const cf e0 = csel(q < g, cf{u2f(g0.x), u2f(g0.z)}, z);
      const cf e1 = csel(q + 4 < g, cf{u2f(g1.x), u2f(g1.z)}, z);
      hin = cfma(wg0, e0, cmul(wg1, e1));
      hin.r += __shfl_xor(hin.r, 16); hin.i += __shfl_xor(hin.i, 16);
      hin.r += __shfl_xor(hin.r, 32); hin.i += __shfl_xor(hin.i, 32);
    }
    STAMP(6);
    cf h = cmul(c2, cfma(wq, cfma(wt, hin, lw), pq));
    const u32 tb = TRB + tg * 1280;
    const u32 twr = tb + c * 40 + q * 8;
    const u32 trd = tb + (q >> 1) * 640 + (8 * (q & 1) + r4) * 40 + pp * 8;
#pragma unroll
    for (int i = 0; i < 4; i++) {
      cf hv[4];
#pragma unroll
      for (int r = 0; r < 4; r++) { h = cfma(a1, h, u[4 * i + r]); hv[r] = h; }
      *(LAS v2u*)(L + twr) = v2u{pk2(hv[0].r, hv[1].r), pk2(hv[2].r, hv[3].r)};
      *(LAS v2u*)(L + twr + 640) = v2u{pk2(-hv[0].i, -hv[1].i), pk2(-hv[2].i, -hv[3].i)};
      WAITL0();
      const v4u o = tr2(L, trd, 160);
      __builtin_amdgcn_raw_buffer_store_b128(o, rs, WS_HB + (tile0 + i) * HB_TILE + (11 * (j >> 2) + (j & 3)) * 1024 + lane * 16, 0, AUX_SC1);
      WAITL0();
    }
    WAITV(0);
    if (lane == 0) __hip_atomic_store(flg + 2, tag, RLX_AGENT);
  }
  STAMP(7);
}

DI bool poll_flags(const Args& p, int g, int u, int w, int lane, u32 tagLo, u32 tagHi, int mask) {
  const GAS u64* f = (const GAS u64*)(p.ws + WS_FL) + ((g * 4 + u) * 32 + 4 * w) * 4 + (lane & 15);
  const u64 tag = ((u64)tagHi << 32) | tagLo;
  const bool need = (mask >> (lane & 3)) & 1;
  for (u32 spins = 0;; spins++) {
    const u64 v = __hip_atomic_load(f, RLX_AGENT);
    if (__all(!need || v == tag)) return true;
    if (spins > SPIN_CAP) return false;
    __builtin_amdgcn_s_sleep(1);
  }
}

DI void phase2(const Args& p, LAS char* L, u32 lbase, __amdgpu_buffer_rsrc_t rs, int g, int j, int w, int lane, u32 tagLo, u32 tagHi) {
  const int u = j >> 3, n = j & 7;
  const int c = lane & 15, q = lane >> 4, r4 = c >> 2, pp = lane & 3;
  const u32 reg = w * WREG;
  const int lr = lane >> 3, ls = lane & 7;
  const u32 vwe = lr * 1024 + ((ls ^ (2 * ((lr >> 1) & 1))) << 4), vwo = vwe ^ 64;
  const u32 vce = lr * 1024 + ((ls ^ (lr >> 1)) << 4), vco = vce ^ 64;
  const char* w2g = (const char*)p.w2 + (size_t)w * (224 * 1024) + n * 128;
  const char* crg = (const char*)p.cre + (size_t)n * (64 * 1024) + w * 128;
  const char* cig = (const char*)p.cim + (size_t)n * (64 * 1024) + w * 128;
#pragma unroll
  for (int o = 0; o < 16; o++) glds((o & 1) ? vwo : vwe, w2g + o * 8192, lbase + reg + o * 1024);
  u32 ta[4];
#pragma unroll
  for (int nn = 0; nn < 4; nn++)
    ta[nn] = reg + q * 1024 + r4 * 128 + ((((2 * nn) ^ (2 * (((r4 >> 1) & 1) + 2 * (q & 1)))) + (pp >> 1)) << 4) + (pp & 1) * 8;
  v4u wf[7][4];
  WAITV(0);
#pragma unroll
  for (int s = 0; s < 4; s++)
#pragma unroll
    for (int nn = 0; nn < 4; nn++) wf[s][nn] = tr2(L, ta[nn] + s * 4096, 512);
  WAITL0();
  SCHED();
#pragma unroll
  for (int o = 0; o < 12; o++) glds((o & 1) ? vwo : vwe, w2g + (16 + o) * 8192, lbase + reg + o * 1024);
  WAITV(0);
#pragma unroll
  for (int s = 0; s < 3; s++)
#pragma unroll
    for (int nn = 0; nn < 4; nn++) wf[4 + s][nn] = tr2(L, ta[nn] + s * 4096, 512);
  WAITL0();
  SCHED();
#pragma unroll
  for (int e = 0; e < 8; e++) {
    glds((e & 1) ? vco : vce, crg + e * 8192, lbase + reg + e * 1024);
    glds((e & 1) ? vco : vce, cig + e * 8192, lbase + reg + 8192 + e * 1024);
  }
  STAMP(8);
  poll_flags(p, g, u, w, lane, tagLo, tagHi, 3);
  __builtin_amdgcn_fence(__ATOMIC_ACQUIRE, "wavefront");
  STAMP(9);
  const u32 ha = WS_HB + (g * 4 + u) * 4 * HB_TILE + 11 * w * 1024 + lane * 16;
  f4 acc[4][4];
#pragma unroll
  for (int i = 0; i < 4; i++)
#pragma unroll
    for (int nn = 0; nn < 4; nn++) acc[i][nn] = f4{0, 0, 0, 0};
  constexpr int WN = 3;
  v4u hf[WN][4];
#pragma unroll
  for (int s = 0; s < WN; s++) {
#pragma unroll
    for (int i = 0; i < 4; i++) hf[s][i] = __builtin_amdgcn_raw_buffer_load_b128(rs, ha + i * HB_TILE + (4 + s) * 1024, 0, AUX_SC1);
    SCHED();
  }
#pragma unroll
  for (int s = 0; s < 7; s++) {
#pragma unroll
    for (int i = 0; i < 4; i++) {
#pragma unroll
      for (int nn = 0; nn < 4; nn++) acc[i][nn] = mfma(wf[s][nn], hf[s % WN][i], acc[i][nn]);
      PIN4(acc[i]);
      if (s + WN < 7) hf[s % WN][i] = __builtin_amdgcn_raw_buffer_load_b128(rs, ha + i * HB_TILE + (4 + s + WN) * 1024, 0, AUX_SC1);
      SCHED();
    }
  }
  STAMP(10);
  poll_flags(p, g, u, w, lane, tagLo, tagHi, 4);
  __builtin_amdgcn_fence(__ATOMIC_ACQUIRE, "wavefront");
  STAMP(11);
  v4u hs[4][4];
#pragma unroll
  for (int s = 0; s < 4; s++) {
#pragma unroll
    for (int i = 0; i < 4; i++) hs[s][i] = __builtin_amdgcn_raw_buffer_load_b128(rs, ha + i * HB_TILE + s * 1024, 0, AUX_SC1);
    SCHED();
  }
  const int ti = w >> 1;
  int tok[2];
  v2u xv[2];
#pragma unroll
  for (int e = 0; e < 2; e++) {
    const int cc = 8 * (w & 1) + 4 * e + q;
    tok[e] = 64 * u + 16 * (cc >> 2) + 4 * ti + (cc & 3);
    xv[e] = *(const GAS v2u*)(p.x + (size_t)(256 * g + tok[e]) * 512 + 64 * n + 4 * c);
  }
  const v2u dk = *(const GAS v2u*)(p.dsk + 64 * n + 4 * c);
  SCHED();
#pragma unroll
  for (int s = 0; s < 4; s++) {
    const u32 ca = reg + (q >> 1) * 8192 + c * 128 + (((2 * s + (q & 1)) ^ ((c >> 1) & 7)) << 4);
#pragma unroll
    for (int nn = 0; nn < 4; nn++) wf[s][nn] = lds128(L, ca + nn * 2048);
  }
#pragma unroll
  for (int s = 0; s < 4; s++)
#pragma unroll
    for (int i = 0; i < 4; i++)
#pragma unroll
      for (int nn = 0; nn < 4; nn++) acc[i][nn] = mfma(wf[s][nn], hs[s][i], acc[i][nn]);
  STAMP(12);
#pragma unroll
  for (int i = 0; i < 4; i++)
#pragma unroll
    for (int nn = 0; nn < 4; nn++) *(LAS f4*)(L + reg + (4 * i + nn) * 1024 + q * 256 + ((c ^ (4 * nn + q)) << 4)) = acc[i][nn];
  WAITL0();
  BAR();
  const LAS float* Lf = (const LAS float*)L;
#pragma unroll
  for (int e = 0; e < 2; e++) {
    const int cc = 8 * (w & 1) + 4 * e + q;
    const u32 ra = (4 * ti + r4) * 1024 + pp * 256 + ((cc ^ c) << 4);
    f4 s = *(const LAS f4*)(L + ra);
#pragma unroll
    for (int v = 1; v < 8; v++) s += *(const LAS f4*)(L + v * WREG + ra);
    const float m = Lf[ST_MEAN / 4 + tok[e]], r = Lf[ST_RS / 4 + tok[e]];
    const float xs[4] = {bf2f(xv[e].x & 0xFFFFu), bf2f(xv[e].x >> 16), bf2f(xv[e].y & 0xFFFFu), bf2f(xv[e].y >> 16)};
    const float ds[4] = {bf2f(dk.x & 0xFFFFu), bf2f(dk.x >> 16), bf2f(dk.y & 0xFFFFu), bf2f(dk.y >> 16)};
#pragma unroll
    for (int k = 0; k < 4; k++) s[k] += xs[k] + rbf(rbf(rbf(xs[k] - m) * r) * ds[k]);
    *(GAS f4*)(p.out + (size_t)(256 * g + tok[e]) * 512 + 64 * n + 4 * c) = s;
  }
  if (g == 7 && j == 0 && threadIdx.x == 0) __hip_atomic_store((GAS u64*)(p.ws + WS_EP), ((u64)tagHi << 32) | tagLo, RLX_AGENT);
  STAMP(13);
}
}

__global__ __launch_bounds__(512, 2) void s5_glu_fused(Args p) {
  __shared__ __attribute__((aligned(256))) char smem[LDS_BYTES];
  LAS char* L = (LAS char*)smem;
  const u32 lbase = (u32)(size_t)L;
  const int lane = threadIdx.x & 63, w = __builtin_amdgcn_readfirstlane(threadIdx.x >> 6);
  const int b = blockIdx.x, xcd = b & 7, idx = b >> 3;
  const int g = (xcd & 1) * 4 + (idx & 3), j = (xcd >> 1) * 8 + (idx >> 2);
  const int rot = ((idx >> 2) + 2 * (idx & 3)) & 7;
  const __amdgpu_buffer_rsrc_t rs = __builtin_amdgcn_make_buffer_rsrc((void*)p.ws, 0, WS_BYTES, 0x00020000);
  STAMP(0);
  u32 tagLo, tagHi;
  if (w < 4) phase1<0>(p, L, lbase, rs, g, j, rot, w, lane, tagLo, tagHi);
  else phase1<1>(p, L, lbase, rs, g, j, rot, w, lane, tagLo, tagHi);
  phase2(p, L, lbase, rs, g, j, w, lane, tagLo, tagHi);
}

extern "C" void kernel_launch(void* const* d_in, const int* in_sizes, int n_in, void* d_out, int out_size, void* d_ws, size_t ws_size,
                              hipStream_t stream) {
  Args p;
  p.x = (const u16*)d_in[0];
  p.lre = (const u16*)d_in[1];
  p.lim = (const u16*)d_in[2];
  p.bre = (const u16*)d_in[3];
  p.bim = (const u16*)d_in[4];
  p.cre = (const u16*)d_in[5];
  p.cim = (const u16*)d_in[6];
  p.ldt = (const u16*)d_in[7];
  p.dsk = (const u16*)d_in[8];
  p.w1 = (const u16*)d_in[10];
  p.w3 = (const u16*)d_in[11];
  p.w2 = (const u16*)d_in[12];
  p.out = (float*)d_out;
  p.ws = (unsigned char*)d_ws;
  s5_glu_fused<<<256, 512, 0, stream>>>(p);
}
```

```cpp
#include <hip/hip_runtime.h>
#include <stdint.h>

typedef unsigned short u16;
typedef unsigned int u32;
typedef unsigned long long u64;
typedef u32 v4u __attribute__((ext_vector_type(4)));
typedef u32 v2u __attribute__((ext_vector_type(2)));
typedef float f4 __attribute__((ext_vector_type(4)));
typedef float f2 __attribute__((ext_vector_type(2)));
typedef short v4s __attribute__((ext_vector_type(4)));
typedef __bf16 v8bf __attribute__((ext_vector_type(8)));
typedef __bf16 v2bf __attribute__((ext_vector_type(2)));
typedef __amdgpu_buffer_rsrc_t srd_t;
#define LAS __attribute__((address_space(3)))
#define GAS __attribute__((address_space(1)))
#define DI __device__ __forceinline__
#define RLX_AGENT __ATOMIC_RELAXED, __HIP_MEMORY_SCOPE_AGENT

#define STAMPS 0
#define WARM 1
#define BSPLIT 1
#define ROT 2

namespace {
constexpr int SM_OFF = 32768, G_OFF = 36864, G3D = 7184, SLOT = 51456, AUXO = 3 * SLOT;
constexpr int GR = AUXO, CSP = AUXO + 4096, TRB = CSP, XCH = 49152, DUMMY = AUXO + 9216;
constexpr int LDS_BYTES = 163840;
constexpr int PW = 6144;
constexpr int unitb(int v) { return ((v / 3 + 2) % 3) * SLOT + (v % 3) * 2048; }
constexpr int WS_EP = 0, WS_FL = 4096, WS_AG = 65536, WS_SG = 131072, WS_HB = 1 << 20, HB_TILE = 88 * 1024;
constexpr int WS_BYTES = WS_HB + 128 * HB_TILE;
constexpr int WS_STAMP = 32 << 20, NSTAMP = 14;
constexpr int AUX_SC1 = 16;
constexpr u32 SPIN_CAP = 1u << 17;
constexpr float NL2E = -1.4426950408889634f;

DI float bf2f(u32 b) { return __builtin_bit_cast(float, b << 16); }
DI float bfhi(u32 b) { return __builtin_bit_cast(float, b & 0xFFFF0000u); }
DI float u2f(u32 b) { return __builtin_bit_cast(float, b); }
DI u32 f2u(float f) { return __builtin_bit_cast(u32, f); }
DI float rbf(float f) { return (float)(__bf16)f; }
DI u32 pk2(f2 a) { return __builtin_bit_cast(u32, __builtin_convertvector(a, v2bf)); }
DI float rcp(float x) { return __builtin_amdgcn_rcpf(x); }
DI float ffma(float a, float b, float c) { return __builtin_fmaf(a, b, c); }
DI f2 fma2(f2 a, f2 b, f2 c) { return __builtin_elementwise_fma(a, b, c); }
DI f2 sp(float a) { return f2{a, a}; }
DI f2 half2(f4 v, int h) { return h ? v.zw : v.xy; }
DI f4 mfma(v4u a, v4u b, f4 c) {
  return __builtin_amdgcn_mfma_f32_16x16x32_bf16(__builtin_bit_cast(v8bf, a), __builtin_bit_cast(v8bf, b), c, 0, 0, 0);
}
DI v4u lds128(LAS char* L, u32 a) { return *(LAS v4u*)(L + a); }
DI f4 ldsf4(LAS char* L, u32 a) { return *(LAS f4*)(L + a); }
DI float ldsf(LAS char* L, u32 a) { return *(LAS float*)(L + a); }
DI v4u tr2(LAS char* L, u32 a, int d) {
  v2u lo = __builtin_bit_cast(v2u, __builtin_amdgcn_ds_read_tr16_b64_v4i16((LAS v4s*)(L + a)));
  v2u hi = __builtin_bit_cast(v2u, __builtin_amdgcn_ds_read_tr16_b64_v4i16((LAS v4s*)(L + a + d)));
  return v4u{lo.x, lo.y, hi.x, hi.y};
}
DI srd_t mksrd(const void* p, int bytes) { return __builtin_amdgcn_make_buffer_rsrc((void*)p, 0, bytes, 0x00020000); }
DI u32 rfl(u32 x) { return __builtin_amdgcn_readfirstlane(x); }
#pragma clang diagnostic ignored "-Winline-asm"
template <int OFF> DI void glds(u32 voff, srd_t srd, u32 soff, u32 la, u32 lb) {
  asm volatile("s_add_u32 m0, %3, %4\n\ts_nop 0\n\tbuffer_load_dwordx4 %0, %1, %2 offen offset:%5 lds" ::"v"(voff), "s"(srd), "s"(soff), "s"(la), "s"(lb),
               "n"(OFF)
               : "memory", "m0", "scc");
}
DI void glds56(u32 voff, srd_t srd, u32 soff, u32 la, u32 lb) {
  asm volatile("s_add_u32 m0, %3, %4\n\ts_mov_b32 exec_hi, 0xffffff\n\tbuffer_load_dwordx4 %0, %1, %2 offen lds\n\ts_mov_b32 exec_hi, -1" ::"v"(voff),
               "s"(srd), "s"(soff), "s"(la), "s"(lb)
               : "memory", "m0", "scc");
}
template <int LB> DI void glds2(u32 ve, u32 vo, srd_t srd, u32 soff, u32 la) {
  asm volatile("s_add_u32 m0, %4, %5\n\ts_nop 0\n\tbuffer_load_dwordx4 %0, %2, %3 offen lds\n\tbuffer_load_dwordx4 %1, %2, %3 offen offset:1024 lds" ::"v"(ve),
               "v"(vo), "s"(srd), "s"(soff), "s"(rfl(la)), "n"(LB)
               : "memory", "m0", "scc");
}
#define WAITV(n) asm volatile("s_waitcnt vmcnt(" #n ")" ::: "memory")
#define WAITL0() do { asm volatile("" ::: "memory"); __builtin_amdgcn_s_waitcnt(0xC07F); asm volatile("" ::: "memory"); } while (0)
#define BAR() __builtin_amdgcn_s_barrier()
#define SCHED() __builtin_amdgcn_sched_barrier(0)
#define SGB(m, n) __builtin_amdgcn_sched_group_barrier(m, n, 0)
#define PIN4(a) asm volatile("" : "+v"((a)[0]), "+v"((a)[1]), "+v"((a)[2]), "+v"((a)[3]))

DI bool allok(bool c) { return __builtin_amdgcn_ballot_w64(!c) == 0; }
struct cf { float r, i; };
DI cf cmul(cf a, cf b) { return cf{ffma(a.r, b.r, -a.i * b.i), ffma(a.r, b.i, a.i * b.r)}; }
DI cf cfma(cf a, cf b, cf c) { return cf{ffma(a.r, b.r, ffma(-a.i, b.i, c.r)), ffma(a.r, b.i, ffma(a.i, b.r, c.i))}; }
DI cf csq(cf a) { return cf{ffma(a.r, a.r, -a.i * a.i), 2.f * a.r * a.i}; }
DI cf csel(bool p, cf a, cf b) { return cf{p ? a.r : b.r, p ? a.i : b.i}; }
struct cf2 { f2 r, i; };
DI cf2 cfma2(cf a, cf2 b, cf2 c) { return cf2{fma2(sp(a.r), b.r, fma2(sp(-a.i), b.i, c.r)), fma2(sp(a.r), b.i, fma2(sp(a.i), b.r, c.i))}; }
DI cf2 cmul2(cf a, cf2 b) { return cf2{fma2(sp(a.r), b.r, sp(-a.i) * b.i), fma2(sp(a.r), b.i, sp(a.i) * b.r)}; }
template <int CTRL> DI float dpp(float x) { return u2f(__builtin_amdgcn_mov_dpp(f2u(x), CTRL, 0xf, 0xf, true)); }
DI float sum16(float v) {
  v += dpp<0xB1>(v); v += dpp<0x4E>(v); v += dpp<0x141>(v); v += dpp<0x140>(v);
  return v;
}
DI float sumx16(float v) { auto r = __builtin_amdgcn_permlane16_swap(f2u(v), f2u(v), false, false); return u2f(r[0]) + u2f(r[1]); }
DI float sumx32(float v) { auto r = __builtin_amdgcn_permlane32_swap(f2u(v), f2u(v), false, false); return u2f(r[0]) + u2f(r[1]); }
DI void bcast4(float v, float (&o)[4]) {
  auto a = __builtin_amdgcn_permlane16_swap(f2u(v), f2u(v), false, false);
  auto e = __builtin_amdgcn_permlane32_swap(a[0], a[0], false, false);
  auto d = __builtin_amdgcn_permlane32_swap(a[1], a[1], false, false);
  o[0] = u2f(e[0]); o[1] = u2f(d[0]); o[2] = u2f(e[1]); o[3] = u2f(d[1]);
}
DI v4u swap16x2(u32 ax, u32 ay, u32 bx, u32 by) {
  auto sx = __builtin_amdgcn_permlane16_swap(ax, bx, false, false);
  auto sy = __builtin_amdgcn_permlane16_swap(ay, by, false, false);
  return v4u{sx[0], sy[0], sx[1], sy[1]};
}

struct Args {
  const u16 *x, *lre, *lim, *bre, *bim, *cre, *cim, *ldt, *dsk, *w1, *w3, *w2;
  float* out;
  unsigned char* ws;
};

#define STAMP(k) do { } while (0)
#define CSTAMP(k) do { } while (0)
#define DSTAMP(k) do { } while (0)

DI u32 swiglu2(f2 tg, f2 tu, float nrl, float ir2) {
  const f2 a = tg * sp(nrl);
  const f2 e = {__builtin_amdgcn_exp2f(a.x), __builtin_amdgcn_exp2f(a.y)};
  const f2 d = fma2(e, sp(ir2), sp(ir2));
  const f2 r = {rcp(d.x), rcp(d.y)};
  return pk2(tg * tu * r);
}

template <int H> struct Fr { v4u x[4]; v4u w[H == 0 ? 5 : 4]; };
struct Ra { u32 xa, sa, ga, gm; };

template <int H> DI void ldpiece(Fr<H>& F, LAS char* L, const Ra& a, int k) {
  if (k == 0) { F.x[0] = lds128(L, a.xa); F.x[1] = lds128(L, a.xa + 256); F.x[2] = lds128(L, a.xa + 512); }
  if (H == 0) {
    if (k == 1) { F.x[3] = lds128(L, a.xa + 768); F.w[0] = lds128(L, a.sa); F.w[1] = lds128(L, a.sa + 2048); }
    if (k == 2) { F.w[2] = tr2(L, a.ga, 112); F.w[3] = tr2(L, a.ga + G3D, 112); }
    if (k == 3) F.w[4] = tr2(L, a.gm, 112);
  } else {
    if (k == 0) F.x[3] = lds128(L, a.xa + 768);
    if (k == 1) { F.w[0] = tr2(L, a.ga + 32, 112); F.w[1] = tr2(L, a.ga + G3D + 32, 112); }
    if (k == 2) { F.w[2] = tr2(L, a.ga + 64, 112); F.w[3] = tr2(L, a.ga + G3D + 64, 112); }
  }
}
template <int H> DI void mmtile(const Fr<H>& F, f4 (&acc)[4], int t) {
#pragma unroll
  for (int i = 0; i < 4; i++) {
    if (H == 0 && t < 2) acc[i] = mfma(F.x[i], F.w[t], acc[i]);
    else acc[i] = mfma(F.w[t], F.x[i], acc[i]);
  }
}
template <int H> DI void colsum(const Fr<H>& F, f4 (&cs)[H == 0 ? 5 : 4], v4u ones) {
#pragma unroll
  for (int t = 0; t < (H == 0 ? 5 : 4); t++) {
    if (H == 0 && t < 2) cs[t] = mfma(ones, F.w[t], cs[t]);
    else cs[t] = mfma(F.w[t], ones, cs[t]);
  }
}
template <int H, class I>
DI void half(const Fr<H>& Fc, Fr<H>& Fn, LAS char* L, const Ra& a, f4 (&acc)[H == 0 ? 5 : 4][4], f4 (&cs)[H == 0 ? 5 : 4], v4u ones, bool docs, I iss) {
  constexpr int NT = H == 0 ? 5 : 4;
#pragma unroll
  for (int t = 0; t < NT; t++) {
    ldpiece<H>(Fn, L, a, t);
    mmtile<H>(Fc, acc[t], t);
#pragma unroll
    for (int k = 0; k < 4; k++) { SGB(0x8, 1); SGB(0x100, 1); }
    PIN4(acc[t]);
    SCHED();
    iss(t);
    if (t == NT - 2 && docs) colsum<H>(Fc, cs, ones);
    SCHED();
  }
}

DI int rot7(int s, int r) { const int k = s + r; return k >= 7 ? k - 7 : k; }
DI u32 p2_pair(int r, int spn) { return (2 * rot7(spn >> 1, r) + (spn & 1)) * 16384; }
constexpr int cunit(int m, int nn) { return m == 0 ? (nn == 0 ? 5 : nn - 1) : (nn < 2 ? 3 + nn : 4 + nn); }

struct Sa {
  cf2 u[8];
  cf a1, a8, a16, a64, pq, lw, e0;
  float lamr, lami;
};

template <int H>
DI void phase1(const Args& p, LAS char* L, u32 lbase, srd_t rs, srd_t sx, srd_t s1, srd_t s3, srd_t sw2, int g, int j, int rot, int r7, int w, int lane,
               u32& tagLo, u32& tagHi, const u32 (&vo)[5], Sa& sa, u32& stampv) {
  constexpr int NT = H == 0 ? 5 : 4;
  const int tg = w & 3, c = lane & 15, q = lane >> 4, r4 = c >> 2, pp = lane & 3;
  const u32 ld0 = lbase + w * 4096, ld4 = lbase + G_OFF + w * 896, ld5 = ld4 + G3D, lds_s = lbase + SM_OFF + w * 1024;
  const int srow = 8 * (w & 1) + (lane >> 3);
  const u32 vs = srow * 1024 + (((lane & 7) ^ ((srow >> 1) & 7)) << 4);
  const srd_t sb = mksrd((const char*)((w & 2) ? p.bim : p.bre) + (size_t)j * (16 * 1024), 16 * 1024);
  v2u ep;
  u32 pl0 = 0, pl1 = 0, pl2 = 0;
  v4u xr0 = {0, 0, 0, 0}, xr1 = {0, 0, 0, 0};
  {
    const u32 zero = 0;
    if (H == 0) glds<0>(vs, sb, rot * 128, lds_s, 0);
    asm volatile("global_load_dwordx2 %0, %1, %2 sc1" : "=v"(ep) : "v"(zero), "s"(p.ws + WS_EP) : "memory");
    if (H == 0) {
      const u32 o = (16 * j + c) * 2;
      asm volatile("global_load_ushort %0, %1, %2" : "=v"(pl0) : "v"(o), "s"(p.lre) : "memory");
      asm volatile("global_load_ushort %0, %1, %2" : "=v"(pl1) : "v"(o), "s"(p.lim) : "memory");
      asm volatile("global_load_ushort %0, %1, %2" : "=v"(pl2) : "v"(o), "s"(p.ldt) : "memory");
    } else {
      const u32 o = (8 * j + 2 * tg + (lane >> 5)) * 1024 + (lane & 31) * 16;
      asm volatile("buffer_load_dwordx4 %0, %1, %2, 0 offen" : "=v"(xr0) : "v"(o), "s"(sx) : "memory");
      asm volatile("buffer_load_dwordx4 %0, %1, %2, 0 offen offset:512" : "=v"(xr1) : "v"(o), "s"(sx) : "memory");
    }
  }
  auto op = [&](int i, u32 kk, u32 slot) {
    if (i == 0) glds<0>(vo[0], sx, kk * 128, ld0, slot);
    else if (i == 1) glds<1024>(vo[1], sx, kk * 128, ld0, slot);
    else if (i == 2) glds<2048>(vo[2], sx, kk * 128, ld0, slot);
    else if (i == 3) glds<3072>(vo[3], sx, kk * 128, ld0, slot);
    else if (i == 4) glds56(vo[4], s1, kk * (64 * 3584), ld4, slot);
    else if (i == 5) glds56(vo[4], s3, kk * (64 * 3584), ld5, slot);
    else if (H == 0) glds<0>(vs, sb, kk * 128, lds_s, slot);
  };
#pragma unroll
  for (int i = 0; i < 7; i++) op(i, (rot + 1) & 7, SLOT);
  const u32 xa0 = (64 * tg + 16 * r4 + 8 * (c & 1) + ((c >> 1) & 1)) * 128 + ((q ^ (2 * r4 + (c & 1))) << 4);
  const u32 sa0 = SM_OFF + c * 128 + ((q ^ ((c >> 1) & 7)) << 4);
  const u32 ga0 = G_OFF + q * 896 + r4 * 224 + 8 * pp;
  const u32 gm0 = ga0 + 96 + (pp >= 2 ? G3D - 16 : 0);
  auto ra = [&](u32 slot, int ks) { return Ra{slot + (xa0 ^ (ks * 64)), slot + (sa0 ^ (ks * 64)), slot + ga0 + ks * 3584, slot + gm0 + ks * 3584}; };
  f4 acc[NT][4], cs[NT];
#pragma unroll
  for (int t = 0; t < NT; t++) {
    cs[t] = f4{0, 0, 0, 0};
#pragma unroll
    for (int i = 0; i < 4; i++) acc[t][i] = f4{0, 0, 0, 0};
  }
  v4u ones = {0x3F803F80u, 0x3F803F80u, 0x3F803F80u, 0x3F803F80u};
  asm volatile("" : "+v"(ones));
  const int own = (tg + 2 * H) & 3;
  STAMP(1);
  if (H == 0) asm volatile("s_waitcnt vmcnt(7)" : "+v"(ep), "+v"(pl0), "+v"(pl1), "+v"(pl2)::"memory");
  else asm volatile("s_waitcnt vmcnt(6)" : "+v"(ep), "+v"(xr0), "+v"(xr1)::"memory");
  BAR();
  STAMP(2);
  CSTAMP(14);
  Fr<H> F0, F1;
  {
    const Ra a = ra(0, 0);
#pragma unroll
    for (int t = 0; t < NT; t++) ldpiece<H>(F0, L, a, t);
    SCHED();
  }
  {
    const u64 tag = (((u64)ep.y << 32) | ep.x) + 1;
    tagLo = (u32)tag;
    tagHi = (u32)(tag >> 32);
  }
  half<H>(F0, F1, L, ra(0, 1), acc, cs, ones, own == 0, [&](int t) {
    if (t == 0) op(6, (rot + 2) & 7, 2 * SLOT);
    if (t < 3) { op(2 * t, (rot + 2) & 7, 2 * SLOT); op(2 * t + 1, (rot + 2) & 7, 2 * SLOT); }
  });
  if (H == 0) {
    sa.lamr = bf2f(pl0); sa.lami = bf2f(pl1);
    const float dt = rbf(expf(bf2f(pl2)));
    float sn, cn;
    sincosf(dt * sa.lami, &sn, &cn);
    const float e = expf(dt * sa.lamr);
    sa.a1 = cf{e * cn, e * sn};
    sa.a8 = csq(csq(csq(sa.a1)));
    sa.a16 = csq(sa.a8);
    sa.a64 = csq(csq(sa.a16));
    asm volatile("" : "+v"(sa.a1.r), "+v"(sa.a1.i), "+v"(sa.a8.r), "+v"(sa.a8.i), "+v"(sa.a16.r), "+v"(sa.a16.i), "+v"(sa.a64.r), "+v"(sa.a64.i));
  } else {
    f2 v[8];
#pragma unroll
    for (int k = 0; k < 4; k++) { v[k] = f2{bf2f(xr0[k]), bfhi(xr0[k])}; v[4 + k] = f2{bf2f(xr1[k]), bfhi(xr1[k])}; }
    const f2 s = ((v[0] + v[1]) + (v[2] + v[3])) + ((v[4] + v[5]) + (v[6] + v[7]));
    const float m = sumx16(sum16(s.x + s.y)) * (1.f / 512.f);
    f2 s2 = {0.f, 0.f};
#pragma unroll
    for (int k = 0; k < 8; k++) { const f2 d = v[k] - sp(m); s2 = fma2(d, d, s2); }
    const float var = sumx16(sum16(s2.x + s2.y)) * (1.f / 512.f);
    const float sd = rbf(sqrtf(rbf(rbf(var) + 1e-5f)));
    const v4u o = {f2u(rbf(m)), tagLo, f2u(1.f / sd), tagHi};
    __builtin_amdgcn_raw_buffer_store_b128(o, rs, (lane & 31) ? 0x7FFFFFF0 : WS_SG + (g * 256 + 8 * j + 2 * tg + (lane >> 5)) * 16, 0, AUX_SC1);
    if (tg < 3) {
      const int b = g * 32 + j;
      const char* src = tg == 0 ? (const char*)p.w2 + b * 7168 : (const char*)(tg == 1 ? p.cre : p.cim) + b * 2048;
      const u32 off = (tg == 0 ? (lane < 56 ? lane : 0) : (lane & 15)) * 128;
      asm volatile("s_mov_b32 m0, %2\n\ts_nop 0\n\tglobal_load_lds_dword %0, %1" ::"v"(off), "s"(src), "s"(lbase + DUMMY) : "memory", "m0");
    }
  }
  u32 cur = 0, nxt = SLOT;
  SCHED();
#pragma unroll 1
  for (int s = 0; s < 5; s++) {
    const Ra an0 = ra(nxt, 0);
    const u32 kk = (rot + s + 3) & 7;
    WAITL0();
    DSTAMP(16 + 3 * s);
    WAITV(6);
    DSTAMP(17 + 3 * s);
    BAR();
    DSTAMP(18 + 3 * s);
    SCHED();
    half<H>(F1, F0, L, an0, acc, cs, ones, ((2 * s + 1) & 3) == own, [&](int t) {
      if (H == 0) {
        if (t == 0) op(6, kk, cur);
        else if (t < 4) op(t - 1, kk, cur);
      } else if (t < 3) op(t, kk, cur);
    });
    half<H>(F0, F1, L, ra(nxt, 1), acc, cs, ones, ((2 * s + 2) & 3) == own, [&](int t) {
      if (t < 3) op(3 + t, kk, cur);
    });
    cur = nxt;
    nxt = nxt == 2 * SLOT ? 0 : nxt + SLOT;
  }
  const u32 vwe = (lane >> 3) * 1024 + (((lane & 7) ^ (2 * ((lane >> 4) & 1))) << 4), vwo = (vwe ^ 64) + 8192 - 1024;
  const u32 lw2 = lbase + w * PW;
#pragma unroll 1
  for (int s = 5; s < 7; s++) {
    const Ra an0 = ra(nxt, 0);
    const u32 o0 = p2_pair(r7, 3 * s - 15), o1 = p2_pair(r7, 3 * s - 14), o2 = p2_pair(r7, 3 * s - 13), lc = lw2 + cur;
    WAITL0();
    DSTAMP(16 + 3 * s);
    WAITV(6);
    DSTAMP(17 + 3 * s);
    BAR();
    DSTAMP(18 + 3 * s);
    SCHED();
    half<H>(F1, F0, L, an0, acc, cs, ones, ((2 * s + 1) & 3) == own, [&](int t) {
      if (H == 1 && t == 0) {
        if (s == 5) {
          asm volatile("s_mov_b32 m0, %3\n\ts_nop 0\n\tbuffer_load_dwordx4 %0, %1, %2 offen sc1 lds" ::"v"(lane * 16), "s"(rs),
                       "s"(WS_SG + (g * 256 + 64 * tg) * 16), "s"(lbase + GR + tg * 1024)
                       : "memory", "m0");
        } else {
          v4u gr = lds128(L, GR + tg * 1024 + lane * 16);
#pragma unroll 1
          for (u32 spins = 0; !allok(gr.y == tagLo && gr.w == tagHi) && spins < SPIN_CAP; spins++)
            asm volatile("s_sleep 1\n\tbuffer_load_dwordx4 %0, %1, %2, %3 offen sc1\n\ts_waitcnt vmcnt(0)"
                         : "=&v"(gr)
                         : "v"(lane * 16), "s"(rs), "s"(WS_SG + (g * 256 + 64 * tg) * 16)
                         : "memory");
          const u32 pos = GR + tg * 1024 + ((lane & 48) + 4 * ((lane >> 1) & 3) + 2 * (lane & 1) + ((lane >> 3) & 1)) * 4;
          *(LAS u32*)(L + pos) = gr.x;
          *(LAS u32*)(L + pos + 256) = gr.z;
        }
      }
      if (t == 1) glds2<0>(vwe, vwo, sw2, o0, lc);
    });
    half<H>(F0, F1, L, ra(nxt, 1), acc, cs, ones, ((2 * s + 2) & 3) == own, [&](int t) {
      if (t == 0) glds2<2048>(vwe, vwo, sw2, o1, lc);
      if (t == 2) glds2<4096>(vwe, vwo, sw2, o2, lc);
    });
    cur = nxt;
    nxt = nxt == 2 * SLOT ? 0 : nxt + SLOT;
  }
  if (own == 3) colsum<H>(F1, cs, ones);
  {
    const u32 cp = CSP + tg * 576;
    if (H == 0) {
      if (q == 0) { *(LAS float*)(L + cp + c * 4) = cs[0][0]; *(LAS float*)(L + cp + 64 + c * 4) = cs[1][0]; }
      if (c == 0) {
#pragma unroll
        for (int t = 2; t < 5; t++) *(LAS f4*)(L + cp + 64 * t + 16 * q) = cs[t];
      }
    } else if (c == 0) {
#pragma unroll
      for (int t = 0; t < 4; t++) *(LAS f4*)(L + cp + 320 + 64 * t + 16 * q) = cs[t];
    }
  }
  WAITL0();
  CSTAMP(37);
  BAR();
  CSTAMP(39);
  float mcol[4], rcol[4];
  f4 mrow[4], rrow[4], cp4[NT][4];
  {
    const u32 csa = CSP + (H == 0 ? 0 : 320) + 16 * q, cs1 = CSP + c * 4, gra = GR + tg * 1024 + (16 * r4 + (c & 3)) * 4, grr = GR + tg * 1024 + 64 * q;
#pragma unroll
    for (int t = 0; t < NT; t++) {
#pragma unroll
      for (int k = 0; k < 4; k++) {
        if (H == 0 && t < 2) cp4[t][k][0] = ldsf(L, cs1 + 64 * t + 576 * k);
        else cp4[t][k] = ldsf4(L, csa + 64 * t + 576 * k);
      }
      if (t < 4) {
        mcol[t] = ldsf(L, gra + 16 * t);
        rcol[t] = ldsf(L, gra + 16 * t + 256);
        if (H == 0) { mrow[t] = ldsf4(L, grr + 16 * t); rrow[t] = ldsf4(L, grr + 16 * t + 256); }
      }
      mmtile<H>(F1, acc[t], t);
#pragma unroll
      for (int k = 0; k < 4; k++) { SGB(0x8, 1); SGB(0x100, 2); }
      PIN4(acc[t]);
      SCHED();
      if (t == 0) glds2<SLOT>(vwe, vwo, sw2, p2_pair(r7, 6), lw2);
      if (t == 1) glds2<SLOT + 2048>(vwe, vwo, sw2, p2_pair(r7, 7), lw2);
      if (t == 2) glds2<SLOT + 4096>(vwe, vwo, sw2, p2_pair(r7, 8), lw2);
      SCHED();
    }
  }
  STAMP(3);
  CSTAMP(15);
#pragma unroll
  for (int t = 0; t < NT; t++) {
    if (H == 0 && t < 2) cs[t][0] = (cp4[t][0][0] + cp4[t][1][0]) + (cp4[t][2][0] + cp4[t][3][0]);
    else cs[t] = (cp4[t][0] + cp4[t][1]) + (cp4[t][2] + cp4[t][3]);
  }
  const int oct0 = 44 * (j >> 2) + 16 + 7 * (j & 3);
  const u32 hbs = WS_HB + (g * 4 + tg) * 4 * HB_TILE + oct0 * 256;
  if (H == 1) {
    const u32 hv = c * 16 + (2 + (q >> 1) + 2 * (q & 1)) * 256;
#pragma unroll
    for (int i = 0; i < 4; i++) {
      if (i == BSPLIT) { SCHED(); WAITL0(); BAR(); SCHED(); }
      const f2 nm = sp(-mcol[i]);
      const float nrl = rcol[i] * NL2E, ir2 = rcp(rcol[i] * rcol[i]);
      u32 v[4];
#pragma unroll
      for (int k = 0; k < 4; k++) {
        const int t = 2 * (k >> 1), h = k & 1;
        v[k] = swiglu2(fma2(nm, half2(cs[t], h), half2(acc[t][i], h)), fma2(nm, half2(cs[t + 1], h), half2(acc[t + 1][i], h)), nrl, ir2);
      }
      __builtin_amdgcn_raw_buffer_store_b128(swap16x2(v[0], v[1], v[2], v[3]), rs, hv + hbs + i * HB_TILE, 0, AUX_SC1);
    }
  } else {
#pragma unroll
    for (int k = 0; k < 8; k++) {
      const f2 m2 = half2(mrow[k >> 1], k & 1), r2 = half2(rrow[k >> 1], k & 1);
      sa.u[k].r = fma2(-m2, sp(cs[0][0]), half2(acc[0][k >> 1], k & 1)) * r2;
      sa.u[k].i = fma2(-m2, sp(cs[1][0]), half2(acc[1][k >> 1], k & 1)) * r2;
    }
    cf2 e2 = sa.u[0];
#pragma unroll
    for (int k = 1; k < 8; k++) e2 = cfma2(sa.a1, e2, sa.u[k]);
    {
      sa.e0 = cf{e2.r.x, e2.i.x};
      const cf e = cfma(sa.a8, sa.e0, cf{e2.r.y, e2.i.y});
      float er[4], ei[4];
      bcast4(e.r, er);
      bcast4(e.i, ei);
      const cf i0{er[0], ei[0]};
      const cf i1 = cfma(sa.a16, i0, cf{er[1], ei[1]});
      const cf i2 = cfma(sa.a16, i1, cf{er[2], ei[2]});
      const cf i3 = cfma(sa.a16, i2, cf{er[3], ei[3]});
      *(LAS v2u*)(L + XCH + (tg * 16 + c) * 8) = v2u{f2u(i3.r), f2u(i3.i)};
      sa.pq = csel(q & 2, csel(q & 1, i2, i1), csel(q & 1, i0, cf{0.f, 0.f}));
    }
    SCHED();
    WAITL0();
    BAR();
    cf eb{0.f, 0.f};
    sa.lw = eb;
#pragma unroll
    for (int t = 0; t < 4; t++) {
      if (t == tg) sa.lw = eb;
      const v2u wv = *(LAS v2u*)(L + XCH + (t * 16 + c) * 8);
      eb = cfma(sa.a64, eb, cf{u2f(wv.x), u2f(wv.y)});
    }
    if (tg == 0) {
      const v4u o = {f2u(eb.r), tagLo, f2u(eb.i), tagHi};
      __builtin_amdgcn_raw_buffer_store_b128(o, rs, q ? 0x7FFFFFF0 : WS_AG + (g * 512 + 16 * j + c) * 16, 0, AUX_SC1);
    }
    SCHED();
    STAMP(8);
    u32 zx[2], zy[2];
#pragma unroll
    for (int ip = 0; ip < 2; ip++) {
      u32 px[2], py[2];
      f4 tm[2];
#pragma unroll
      for (int ii = 0; ii < 2; ii++) {
        const int i = 2 * ip + ii;
        const f2 nm = sp(-mcol[i]);
        const float nrl = rcol[i] * NL2E, ir2 = rcp(rcol[i] * rcol[i]);
        px[ii] = swiglu2(fma2(nm, cs[2].xy, acc[2][i].xy), fma2(nm, cs[3].xy, acc[3][i].xy), nrl, ir2);
        py[ii] = swiglu2(fma2(nm, cs[2].zw, acc[2][i].zw), fma2(nm, cs[3].zw, acc[3][i].zw), nrl, ir2);
        tm[ii].xy = fma2(nm, cs[4].xy, acc[4][i].xy);
        tm[ii].zw = fma2(nm, cs[4].zw, acc[4][i].zw);
      }
      __builtin_amdgcn_raw_buffer_store_b128(swap16x2(px[0], py[0], px[1], py[1]), rs, c * 16 + (q & 1) * HB_TILE + (q >> 1) * 256 + hbs + 2 * ip * HB_TILE, 0,
                                             AUX_SC1);
      float rlo = rcol[2 * ip];
      asm volatile("" : "+v"(rlo));
      const float rsel = q < 2 ? rlo : rcol[2 * ip + 1];
      const float nrl = rsel * NL2E, ir2 = rcp(rsel * rsel);
      f4 gm, um;
#pragma unroll
      for (int r = 0; r < 4; r++) {
        auto s = __builtin_amdgcn_permlane32_swap(f2u(tm[0][r]), f2u(tm[1][r]), false, false);
        gm[r] = u2f(s[0]);
        um[r] = u2f(s[1]);
      }
      zx[ip] = swiglu2(gm.xy, um.xy, nrl, ir2);
      zy[ip] = swiglu2(gm.zw, um.zw, nrl, ir2);
    }
    __builtin_amdgcn_raw_buffer_store_b128(swap16x2(zx[0], zy[0], zx[1], zy[1]), rs, c * 16 + (2 * (q & 1) + (q >> 1)) * HB_TILE + hbs + 6 * 256, 0, AUX_SC1);
  }
  STAMP(4);
}

DI void poll_flags(const GAS u64* f, int lane, u64 tag, int mask, u64 v) {
  const bool need = (mask >> (lane & 3)) & 1;
#pragma unroll 1
  for (u32 spins = 0; !allok(!need || v == tag) && spins < SPIN_CAP; spins++) {
    __builtin_amdgcn_s_sleep(1);
    v = __hip_atomic_load(f, RLX_AGENT);
  }
  __builtin_amdgcn_fence(__ATOMIC_ACQUIRE, "wavefront");
}
}

__global__ __launch_bounds__(512, 2) void s5_glu_fused(Args p) {
  __shared__ __attribute__((aligned(256))) char smem[LDS_BYTES];
  LAS char* L = (LAS char*)smem;
  const u32 lbase = (u32)(size_t)L;
  int lane = threadIdx.x & 63;
  const int w = __builtin_amdgcn_readfirstlane(threadIdx.x >> 6);
  const int b = blockIdx.x, xcd = b & 7, idx = b >> 3;
  const int g = (xcd & 1) * 4 + (idx & 3), j = (xcd >> 1) * 8 + (idx >> 2);
  const int rot = ROT == 0 ? ((idx >> 2) + 2 * (idx & 3)) & 7 : ROT == 1 ? 0 : ROT == 2 ? 4 * (((idx >> 2) + idx) & 1) : 2 * (((idx >> 2) + idx) & 3);
  u32 stampv = 0;
  STAMP(0);
  const srd_t sx = mksrd((const char*)p.x + (size_t)g * (256 * 1024), 256 * 1024);
  u32 vo[5];
#pragma unroll
  for (int e = 0; e < 4; e++) vo[e] = (32 * w + 8 * e + (lane >> 3)) * 1024 + (((lane & 7) ^ ((4 * w + e) & 7)) << 4) - e * 1024;
  glds<0>(vo[0], sx, rot * 128, lbase + w * 4096, 0);
  glds<1024>(vo[1], sx, rot * 128, lbase + w * 4096, 0);
  glds<2048>(vo[2], sx, rot * 128, lbase + w * 4096, 0);
  glds<3072>(vo[3], sx, rot * 128, lbase + w * 4096, 0);
  const srd_t s1 = mksrd((const char*)p.w1 + j * 112, 512 * 3584 - j * 112), s3 = mksrd((const char*)p.w3 + j * 112, 512 * 3584 - j * 112);
  {
    const int gpos = (lane * 37) >> 8, gch = lane - gpos * 7;
    vo[4] = (8 * w + 4 * (gpos & 1) + (gpos >> 1)) * 3584 + gch * 16;
    glds56(vo[4], s1, rot * (64 * 3584), lbase + G_OFF + w * 896, 0);
    glds56(vo[4], s3, rot * (64 * 3584), lbase + G_OFF + w * 896, G3D);
  }
  SCHED();
  const int u = j >> 3, n = j & 7, xn = g & 3, r7 = rot7(n == 7 ? 0 : n, 2 * xn);
  const srd_t rs = mksrd(p.ws, WS_BYTES);
  const srd_t sw2 = mksrd((const char*)p.w2 + (size_t)w * (224 * 1024) + n * 128, 224 * 1024 - n * 128);
  u32 tagLo, tagHi;
  Sa sa;
  if (w < 4) phase1<0>(p, L, lbase, rs, sx, s1, s3, sw2, g, j, rot, r7, w, lane, tagLo, tagHi, vo, sa, stampv);
  else phase1<1>(p, L, lbase, rs, sx, s1, s3, sw2, g, j, rot, r7, w, lane, tagLo, tagHi, vo, sa, stampv);

  asm volatile("" : "+v"(lane));
  const int tg = w & 3, c = lane & 15, q = lane >> 4, r4 = c >> 2, pp = lane & 3;
  const u64 tag = ((u64)tagHi << 32) | tagLo;
  GAS u64* flg = (GAS u64*)(p.ws + WS_FL) + ((g * 4 + tg) * 32 + j) * 4;
  const u32 wo = w * PW, lw2 = lbase + wo;
  const u32 vwe = (lane >> 3) * 1024 + (((lane & 7) ^ (2 * (q & 1))) << 4), vwo = (vwe ^ 64) + 8192 - 1024;
  const u32 vce = (lane >> 3) * 1024 + (((lane & 7) ^ q) << 4), vco = (vce ^ 64) + 8192 - 1024;
  const srd_t scr = mksrd((const char*)p.cre + (size_t)n * (64 * 1024) + w * 128, 64 * 1024 - w * 128);
  const srd_t sci = mksrd((const char*)p.cim + (size_t)n * (64 * 1024) + w * 128, 64 * 1024 - w * 128);
  u32 tl[4];
#pragma unroll
  for (int nn = 0; nn < 4; nn++)
    tl[nn] = wo + (q & 1) * 1024 + r4 * 128 + ((((2 * (nn ^ xn)) ^ (2 * (((r4 >> 1) & 1) + 2 * (q & 1)))) + (pp >> 1)) << 4) + (pp & 1) * 8;
  v4u wf[7][4];
  auto rdw = [&](int s, int ua, int ub) {
    const u32 o = (q >> 1) ? unitb(ub) : unitb(ua);
#pragma unroll
    for (int nn = 0; nn < 4; nn++) wf[s][nn] = tr2(L, o + tl[nn], 512);
  };
  WAITV(9);
  rdw(0, 0, 1); rdw(1, 2, 3); rdw(2, 4, 5);
  WAITL0();
  glds2<unitb(0)>(vwe, vwo, sw2, p2_pair(r7, 9), lw2);
  glds2<unitb(1)>(vwe, vwo, sw2, p2_pair(r7, 10), lw2);
  glds2<unitb(2)>(vwe, vwo, sw2, p2_pair(r7, 11), lw2);
  glds2<unitb(3)>(vwe, vwo, sw2, p2_pair(r7, 12), lw2);
  glds2<unitb(4)>(vwe, vwo, sw2, p2_pair(r7, 13), lw2);
  glds2<unitb(cunit(0, 0))>(vce, vco, scr, xn * 16384, lw2);
  cf wt, wg0, wg1, wt8;
  cf2 h0;
  if (w < 4) {
    const float nr = sa.a1.r - 1.f, ni = sa.a1.i, den = 2.f / (sa.lamr * sa.lamr + sa.lami * sa.lami);
    const cf c2{(nr * sa.lamr + ni * sa.lami) * den, (ni * sa.lamr - nr * sa.lami) * den};
    const cf a32 = csq(sa.a16), a128 = csq(sa.a64), a256 = csq(a128), a512 = csq(a256), a1024 = csq(a512);
    const cf one{1.f, 0.f}, zero{0.f, 0.f};
    const cf cw = cmul(c2, cmul(csel(q & 1, sa.a16, one), csel(q & 2, a32, one)));
    wt = cmul(cw, cmul(csel(tg & 1, sa.a64, one), csel(tg & 2, a128, one)));
    wt8 = cmul(wt, sa.a8);
    const int e0 = g - 1 - q, e1 = g - 5 - q;
    wg1 = cmul(csel(e0 & 1, a256, one), csel(e0 & 2, a512, one));
    wg0 = csel(e0 >= 0, cmul(wg1, csel(e0 & 4, a1024, one)), zero);
    wg1 = csel(e1 >= 0, wg1, zero);
    const cf k0 = cfma(cw, sa.lw, cmul(c2, sa.pq));
    const cf k1 = cfma(sa.a8, k0, cmul(c2, sa.e0));
    h0 = cf2{f2{k0.r, k1.r}, f2{k0.i, k1.i}};
#pragma unroll
    for (int k = 0; k < 8; k++) {
      sa.u[k] = cmul2(c2, sa.u[k]);
      asm volatile("" : "+v"(sa.u[k].r), "+v"(sa.u[k].i));
    }
    asm volatile("" : "+v"(wt.r), "+v"(wt.i), "+v"(wt8.r), "+v"(wt8.i), "+v"(wg0.r), "+v"(wg0.i), "+v"(wg1.r), "+v"(wg1.i), "+v"(h0.r), "+v"(h0.i));
  }
  WAITV(12);
  if (lane == 0) __hip_atomic_store(flg + (w >> 2), tag, RLX_AGENT);
  STAMP(5);
  if (w < 4) {
    cf2 h = h0;
    if (g > 0) {
      const u32 ao = WS_AG + (q * 512 + 16 * j + c) * 16;
      v4u g0, g1;
#pragma unroll 1
      for (u32 spins = 0;; spins++) {
        g0 = __builtin_amdgcn_raw_buffer_load_b128(rs, ao, 0, AUX_SC1);
        g1 = __builtin_amdgcn_raw_buffer_load_b128(rs, ao + 4 * 512 * 16, 0, AUX_SC1);
        const bool ok = (q >= g || (g0.y == tagLo && g0.w == tagHi)) && (q + 4 >= g || (g1.y == tagLo && g1.w == tagHi));
        if (allok(ok) || spins > SPIN_CAP) break;
        __builtin_amdgcn_s_sleep(1);
      }
      const cf zz{0.f, 0.f};
      cf hin = cfma(wg0, csel(q < g, cf{u2f(g0.x), u2f(g0.z)}, zz), cmul(wg1, csel(q + 4 < g, cf{u2f(g1.x), u2f(g1.z)}, zz)));
      hin.r = sumx32(sumx16(hin.r));
      hin.i = sumx32(sumx16(hin.i));
      h = cfma2(hin, cf2{f2{wt.r, wt8.r}, f2{wt.i, wt8.i}}, h0);
    }
    STAMP(6);
    const u32 tb = TRB + tg * 1280;
    const u32 twr = tb + c * 40 + q * 8;
    const u32 trd = tb + (q >> 1) * 640 + (8 * (q & 1) + r4) * 40 + pp * 8;
#pragma unroll
    for (int i = 0; i < 4; i++) {
      const cf2 ha = cfma2(sa.a1, h, sa.u[2 * i]);
      h = cfma2(sa.a1, ha, sa.u[2 * i + 1]);
      *(LAS v2u*)(L + twr) = v2u{pk2(ha.r), pk2(h.r)};
      *(LAS v2u*)(L + twr + 640) = v2u{pk2(-ha.i), pk2(-h.i)};
      asm volatile("" ::: "memory");
      const v4u o = tr2(L, trd, 160);
      asm volatile("" ::: "memory");
      __builtin_amdgcn_raw_buffer_store_b128(o, rs, lane * 16 + WS_HB + ((g * 4 + tg) * 4 + i) * HB_TILE + (11 * (j >> 2) + (j & 3)) * 1024, 0, AUX_SC1);
    }
    WAITV(4);
  } else {
    WAITV(1);
    STAMP(6);
  }
  rdw(3, 6, 7); rdw(4, 8, 0); rdw(5, 1, 2); rdw(6, 3, 4);
  WAITL0();
  glds2<unitb(cunit(0, 1))>(vce, vco, scr, (1 ^ xn) * 16384, lw2);
  glds2<unitb(cunit(0, 2))>(vce, vco, scr, (2 ^ xn) * 16384, lw2);
  glds2<unitb(cunit(0, 3))>(vce, vco, scr, (3 ^ xn) * 16384, lw2);
  glds2<unitb(cunit(1, 0))>(vce, vco, sci, (0 ^ xn) * 16384, lw2);
  glds2<unitb(cunit(1, 1))>(vce, vco, sci, (1 ^ xn) * 16384, lw2);
  glds2<unitb(cunit(1, 2))>(vce, vco, sci, (2 ^ xn) * 16384, lw2);
  glds2<unitb(cunit(1, 3))>(vce, vco, sci, (3 ^ xn) * 16384, lw2);
  if (w < 4) {
    WAITV(14);
    if (lane == 0) __hip_atomic_store(flg + 2, tag, RLX_AGENT);
  }
  STAMP(7);

  const GAS u64* fl = (const GAS u64*)(p.ws + WS_FL) + ((g * 4 + u) * 32 + 4 * w) * 4 + (lane & 15);
  poll_flags(fl, lane, tag, 3, __hip_atomic_load(fl, RLX_AGENT));
  STAMP(9);
  const u32 hs = WS_HB + (g * 4 + u) * 4 * HB_TILE + 11 * w * 1024;
  f4 acc[4][4];
#pragma unroll
  for (int i = 0; i < 4; i++)
#pragma unroll
    for (int nn = 0; nn < 4; nn++) acc[i][nn] = f4{0, 0, 0, 0};
  v4u hf[11][4];
  auto ldh = [&](int s) {
    const int ks = s < 7 ? 4 + rot7(s, r7) : s - 7;
#pragma unroll
    for (int i = 0; i < 4; i++) hf[s][i] = __builtin_amdgcn_raw_buffer_load_b128(rs, lane * 16 + i * HB_TILE, hs + ks * 1024, AUX_SC1);
    SCHED();
  };
  auto step = [&](int s, const v4u (&wv)[4]) {
#pragma unroll
    for (int i = 0; i < 4; i++) {
#pragma unroll
      for (int nn = 0; nn < 4; nn++) acc[i][nn] = mfma(wv[nn], hf[s][i], acc[i][nn]);
      PIN4(acc[i]);
    }
    SCHED();
  };
  ldh(0); ldh(1); ldh(2);
  step(0, wf[0]);
  ldh(3); ldh(4);
  step(1, wf[1]);
  const u64 fv = __hip_atomic_load(fl, RLX_AGENT);
  ldh(5); ldh(6);
  step(2, wf[2]);
  step(3, wf[3]);
  step(4, wf[4]);
  STAMP(10);
  poll_flags(fl, lane, tag, 4, fv);
  STAMP(11);
  ldh(7); ldh(8); ldh(9); ldh(10);
  const int ti = w >> 1;
  const int dcol = 64 * n + 16 * (r4 ^ xn) + 4 * pp;
  int tok[2];
  v2u xv[2];
#pragma unroll
  for (int e = 0; e < 2; e++) {
    const int cc = 8 * (w & 1) + 4 * e + q;
    tok[e] = 16 * (cc >> 2) + 8 * (cc & 1) + 2 * ti + ((cc >> 1) & 1);
    xv[e] = *(const GAS v2u*)(p.x + (size_t)(256 * g + 64 * u + tok[e]) * 512 + dcol);
  }
  const v2u dk = *(const GAS v2u*)(p.dsk + dcol);
  u32 ub[4];
#pragma unroll
  for (int nn = 0; nn < 4; nn++) ub[nn] = wo + c * 128 + ((q >> 1) ? unitb(cunit(1, nn)) : unitb(cunit(0, nn)));
  auto ldc = [&](int s) {
    const u32 ca = ((2 * s + (q & 1)) ^ ((c >> 1) & 7)) << 4;
#pragma unroll
    for (int nn = 0; nn < 4; nn++) wf[s][nn] = lds128(L, ub[nn] + ca);
    SCHED();
  };
  ldc(0);
  step(5, wf[5]);
  ldc(1); ldc(2);
  step(6, wf[6]);
  ldc(3);
  step(7, wf[0]); step(8, wf[1]); step(9, wf[2]); step(10, wf[3]);
  STAMP(12);
#pragma unroll
  for (int i = 0; i < 4; i++)
#pragma unroll
    for (int nn = 0; nn < 4; nn++)
      *(LAS f4*)(L + wo + unitb(2 * i + (nn >> 1)) + (nn & 1) * 1024 + q * 256 + ((c ^ (4 * nn + q)) << 4)) = acc[i][nn];
  WAITL0();
  BAR();
  const int v0 = 2 * ti, v1 = v0 + 1;
  const u32 u0 = ((((v0 * 11) >> 5) + 2) % 3) * SLOT + (v0 % 3) * 2048, u1 = ((((v1 * 11) >> 5) + 2) % 3) * SLOT + (v1 % 3) * 2048;
#pragma unroll
  for (int e = 0; e < 2; e++) {
    const int cc = 8 * (w & 1) + 4 * e + q;
    const u32 ra = ((r4 >> 1) ? u1 : u0) + (r4 & 1) * 1024 + pp * 256 + ((cc ^ c) << 4);
    f4 s = ldsf4(L, ra);
#pragma unroll
    for (int v = 1; v < 8; v++) s += ldsf4(L, v * PW + ra);
    const u32 sp_ = GR + u * 1024 + (16 * (cc >> 2) + 4 * ti + (cc & 3)) * 4;
    const float m = ldsf(L, sp_), r = ldsf(L, sp_ + 256);
    const float xs[4] = {bf2f(xv[e].x), bfhi(xv[e].x), bf2f(xv[e].y), bfhi(xv[e].y)};
    const float ds[4] = {bf2f(dk.x), bfhi(dk.x), bf2f(dk.y), bfhi(dk.y)};
#pragma unroll
    for (int k = 0; k < 4; k++) s[k] += xs[k] + rbf(rbf(rbf(xs[k] - m) * r) * ds[k]);
    *(GAS f4*)(p.out + (size_t)(256 * g + 64 * u + tok[e]) * 512 + dcol) = s;
  }
  if (g == 7 && j == 0 && threadIdx.x == 0) __hip_atomic_store((GAS u64*)(p.ws + WS_EP), tag, RLX_AGENT);
  STAMP(13);
}

extern "C" void kernel_launch(void* const* d_in, const int* in_sizes, int n_in, void* d_out, int out_size, void* d_ws, size_t ws_size,
                              hipStream_t stream) {
  Args p;
  p.x = (const u16*)d_in[0];
  p.lre = (const u16*)d_in[1];
  p.lim = (const u16*)d_in[2];
  p.bre = (const u16*)d_in[3];
  p.bim = (const u16*)d_in[4];
  p.cre = (const u16*)d_in[5];
  p.cim = (const u16*)d_in[6];
  p.ldt = (const u16*)d_in[7];
  p.dsk = (const u16*)d_in[8];
  p.w1 = (const u16*)d_in[10];
  p.w3 = (const u16*)d_in[11];
  p.w2 = (const u16*)d_in[12];
  p.out = (float*)d_out;
  p.ws = (unsigned char*)d_ws;
  s5_glu_fused<<<256, 512, 0, stream>>>(p);
}
```

```cpp
#include <hip/hip_runtime.h>
#include <stdint.h>

typedef unsigned short u16;
typedef unsigned int u32;
typedef unsigned long long u64;
typedef u32 v4u __attribute__((ext_vector_type(4)));
typedef u32 v2u __attribute__((ext_vector_type(2)));
typedef float f4 __attribute__((ext_vector_type(4)));
typedef float f2 __attribute__((ext_vector_type(2)));
typedef short v4s __attribute__((ext_vector_type(4)));
typedef __bf16 v8bf __attribute__((ext_vector_type(8)));
typedef __bf16 v2bf __attribute__((ext_vector_type(2)));
typedef __amdgpu_buffer_rsrc_t srd_t;
#define LAS __attribute__((address_space(3)))
#define GAS __attribute__((address_space(1)))
#define DI __device__ __forceinline__
#define RLX_AGENT __ATOMIC_RELAXED, __HIP_MEMORY_SCOPE_AGENT

#define STAMPS 0
#define WARM 1
#define BSPLIT 1
#define ROT 1

namespace {
constexpr int SM_OFF = 32768, G_OFF = 36864, G3D = 7184, SLOT = 51456, AUXO = 3 * SLOT;
constexpr int GR = AUXO, CSP = AUXO + 4096, TRB = CSP, XCH = 49152, DUMMY = AUXO + 9216;
constexpr int LDS_BYTES = 163840;
constexpr int PW = 6144;
constexpr int unitb(int v) { return ((v / 3 + 2) % 3) * SLOT + (v % 3) * 2048; }
constexpr int WS_EP = 0, WS_FL = 4096, WS_AG = 65536, WS_SG = 131072, WS_HB = 1 << 20, HB_TILE = 88 * 1024;
constexpr int WS_BYTES = WS_HB + 128 * HB_TILE;
constexpr int WS_STAMP = 32 << 20, NSTAMP = 14;
constexpr int AUX_SC1 = 16;
constexpr u32 SPIN_CAP = 1u << 17;
constexpr float NL2E = -1.4426950408889634f;

DI float bf2f(u32 b) { return __builtin_bit_cast(float, b << 16); }
DI float bfhi(u32 b) { return __builtin_bit_cast(float, b & 0xFFFF0000u); }
DI float u2f(u32 b) { return __builtin_bit_cast(float, b); }
DI u32 f2u(float f) { return __builtin_bit_cast(u32, f); }
DI float rbf(float f) { return (float)(__bf16)f; }
DI u32 pk2(f2 a) { return __builtin_bit_cast(u32, __builtin_convertvector(a, v2bf)); }
DI float rcp(float x) { return __builtin_amdgcn_rcpf(x); }
DI float ffma(float a, float b, float c) { return __builtin_fmaf(a, b, c); }
DI f2 fma2(f2 a, f2 b, f2 c) { return __builtin_elementwise_fma(a, b, c); }
DI f2 sp(float a) { return f2{a, a}; }
DI f2 half2(f4 v, int h) { return h ? v.zw : v.xy; }
DI f4 mfma(v4u a, v4u b, f4 c) {
  return __builtin_amdgcn_mfma_f32_16x16x32_bf16(__builtin_bit_cast(v8bf, a), __builtin_bit_cast(v8bf, b), c, 0, 0, 0);
}
DI v4u lds128(LAS char* L, u32 a) { return *(LAS v4u*)(L + a); }
DI f4 ldsf4(LAS char* L, u32 a) { return *(LAS f4*)(L + a); }
DI float ldsf(LAS char* L, u32 a) { return *(LAS float*)(L + a); }
DI v4u tr2(LAS char* L, u32 a, int d) {
  v2u lo = __builtin_bit_cast(v2u, __builtin_amdgcn_ds_read_tr16_b64_v4i16((LAS v4s*)(L + a)));
  v2u hi = __builtin_bit_cast(v2u, __builtin_amdgcn_ds_read_tr16_b64_v4i16((LAS v4s*)(L + a + d)));
  return v4u{lo.x, lo.y, hi.x, hi.y};
}
DI srd_t mksrd(const void* p, int bytes) { return __builtin_amdgcn_make_buffer_rsrc((void*)p, 0, bytes, 0x00020000); }
DI u32 rfl(u32 x) { return __builtin_amdgcn_readfirstlane(x); }
#pragma clang diagnostic ignored "-Winline-asm"
template <int OFF> DI void glds(u32 voff, srd_t srd, u32 soff, u32 la, u32 lb) {
  asm volatile("s_add_u32 m0, %3, %4\n\ts_nop 0\n\tbuffer_load_dwordx4 %0, %1, %2 offen offset:%5 lds" ::"v"(voff), "s"(srd), "s"(soff), "s"(la), "s"(lb),
               "n"(OFF)
               : "memory", "m0", "scc");
}
DI void glds56(u32 voff, srd_t srd, u32 soff, u32 la, u32 lb) {
  asm volatile("s_add_u32 m0, %3, %4\n\ts_mov_b32 exec_hi, 0xffffff\n\tbuffer_load_dwordx4 %0, %1, %2 offen lds\n\ts_mov_b32 exec_hi, -1" ::"v"(voff),
               "s"(srd), "s"(soff), "s"(la), "s"(lb)
               : "memory", "m0", "scc");
}
template <int LB> DI void glds2(u32 ve, u32 vo, srd_t srd, u32 soff, u32 la) {
  asm volatile("s_add_u32 m0, %4, %5\n\ts_nop 0\n\tbuffer_load_dwordx4 %0, %2, %3 offen lds\n\tbuffer_load_dwordx4 %1, %2, %3 offen offset:1024 lds" ::"v"(ve),
               "v"(vo), "s"(srd), "s"(soff), "s"(rfl(la)), "n"(LB)
               : "memory", "m0", "scc");
}
#define WAITV(n) asm volatile("s_waitcnt vmcnt(" #n ")" ::: "memory")
#define WAITL0() do { asm volatile("" ::: "memory"); __builtin_amdgcn_s_waitcnt(0xC07F); asm volatile("" ::: "memory"); } while (0)
#define BAR() __builtin_amdgcn_s_barrier()
#define SCHED() __builtin_amdgcn_sched_barrier(0)
#define SGB(m, n) __builtin_amdgcn_sched_group_barrier(m, n, 0)
#define PIN4(a) asm volatile("" : "+v"((a)[0]), "+v"((a)[1]), "+v"((a)[2]), "+v"((a)[3]))

DI bool allok(bool c) { return __builtin_amdgcn_ballot_w64(!c) == 0; }
struct cf { float r, i; };
DI cf cmul(cf a, cf b) { return cf{ffma(a.r, b.r, -a.i * b.i), ffma(a.r, b.i, a.i * b.r)}; }
DI cf cfma(cf a, cf b, cf c) { return cf{ffma(a.r, b.r, ffma(-a.i, b.i, c.r)), ffma(a.r, b.i, ffma(a.i, b.r, c.i))}; }
DI cf csq(cf a) { return cf{ffma(a.r, a.r, -a.i * a.i), 2.f * a.r * a.i}; }
DI cf csel(bool p, cf a, cf b) { return cf{p ? a.r : b.r, p ? a.i : b.i}; }
struct cf2 { f2 r, i; };
DI cf2 cfma2(cf a, cf2 b, cf2 c) { return cf2{fma2(sp(a.r), b.r, fma2(sp(-a.i), b.i, c.r)), fma2(sp(a.r), b.i, fma2(sp(a.i), b.r, c.i))}; }
DI cf2 cmul2(cf a, cf2 b) { return cf2{fma2(sp(a.r), b.r, sp(-a.i) * b.i), fma2(sp(a.r), b.i, sp(a.i) * b.r)}; }
template <int CTRL> DI float dpp(float x) { return u2f(__builtin_amdgcn_mov_dpp(f2u(x), CTRL, 0xf, 0xf, true)); }
DI float sum16(float v) {
  v += dpp<0xB1>(v); v += dpp<0x4E>(v); v += dpp<0x141>(v); v += dpp<0x140>(v);
  return v;
}
DI float sumx16(float v) { auto r = __builtin_amdgcn_permlane16_swap(f2u(v), f2u(v), false, false); return u2f(r[0]) + u2f(r[1]); }
DI float sumx32(float v) { auto r = __builtin_amdgcn_permlane32_swap(f2u(v), f2u(v), false, false); return u2f(r[0]) + u2f(r[1]); }
DI void bcast4(float v, float (&o)[4]) {
  auto a = __builtin_amdgcn_permlane16_swap(f2u(v), f2u(v), false, false);
  auto e = __builtin_amdgcn_permlane32_swap(a[0], a[0], false, false);
  auto d = __builtin_amdgcn_permlane32_swap(a[1], a[1], false, false);
  o[0] = u2f(e[0]); o[1] = u2f(d[0]); o[2] = u2f(e[1]); o[3] = u2f(d[1]);
}
DI v4u swap16x2(u32 ax, u32 ay, u32 bx, u32 by) {
  auto sx = __builtin_amdgcn_permlane16_swap(ax, bx, false, false);
  auto sy = __builtin_amdgcn_permlane16_swap(ay, by, false, false);
  return v4u{sx[0], sy[0], sx[1], sy[1]};
}

struct Args {
  const u16 *x, *lre, *lim, *bre, *bim, *cre, *cim, *ldt, *dsk, *w1, *w3, *w2;
  float* out;
  unsigned char* ws;
};

#define STAMP(k) do { } while (0)
#define CSTAMP(k) do { } while (0)
#define DSTAMP(k) do { } while (0)

DI u32 swiglu2(f2 tg, f2 tu, float nrl, float ir2) {
  const f2 a = tg * sp(nrl);
  const f2 e = {__builtin_amdgcn_exp2f(a.x), __builtin_amdgcn_exp2f(a.y)};
  const f2 d = fma2(e, sp(ir2), sp(ir2));
  const f2 r = {rcp(d.x), rcp(d.y)};
  return pk2(tg * tu * r);
}

template <int H> struct Fr { v4u x[4]; v4u w[H == 0 ? 5 : 4]; };
struct Ra { u32 xa, sa, ga, gm; };

template <int H> DI void ldpiece(Fr<H>& F, LAS char* L, const Ra& a, int k) {
  if (k == 0) { F.x[0] = lds128(L, a.xa); F.x[1] = lds128(L, a.xa + 256); F.x[2] = lds128(L, a.xa + 512); }
  if (H == 0) {
    if (k == 1) { F.x[3] = lds128(L, a.xa + 768); F.w[0] = lds128(L, a.sa); F.w[1] = lds128(L, a.sa + 2048); }
    if (k == 2) { F.w[2] = tr2(L, a.ga, 112); F.w[3] = tr2(L, a.ga + G3D, 112); }
    if (k == 3) F.w[4] = tr2(L, a.gm, 112);
  } else {
    if (k == 0) F.x[3] = lds128(L, a.xa + 768);
    if (k == 1) { F.w[0] = tr2(L, a.ga + 32, 112); F.w[1] = tr2(L, a.ga + G3D + 32, 112); }
    if (k == 2) { F.w[2] = tr2(L, a.ga + 64, 112); F.w[3] = tr2(L, a.ga + G3D + 64, 112); }
  }
}
template <int H> DI void mmtile(const Fr<H>& F, f4 (&acc)[4], int t) {
#pragma unroll
  for (int i = 0; i < 4; i++) {
    if (H == 0 && t < 2) acc[i] = mfma(F.x[i], F.w[t], acc[i]);
    else acc[i] = mfma(F.w[t], F.x[i], acc[i]);
  }
}
template <int H> DI void colsum(const Fr<H>& F, f4 (&cs)[H == 0 ? 5 : 4], v4u ones) {
#pragma unroll
  for (int t = 0; t < (H == 0 ? 5 : 4); t++) {
    if (H == 0 && t < 2) cs[t] = mfma(ones, F.w[t], cs[t]);
    else cs[t] = mfma(F.w[t], ones, cs[t]);
  }
}
template <int H, class I>
DI void half(const Fr<H>& Fc, Fr<H>& Fn, LAS char* L, const Ra& a, f4 (&acc)[H == 0 ? 5 : 4][4], f4 (&cs)[H == 0 ? 5 : 4], v4u ones, bool docs, I iss) {
  constexpr int NT = H == 0 ? 5 : 4;
#pragma unroll
  for (int t = 0; t < NT; t++) {
    ldpiece<H>(Fn, L, a, t);
    mmtile<H>(Fc, acc[t], t);
#pragma unroll
    for (int k = 0; k < 4; k++) { SGB(0x8, 1); SGB(0x100, 1); }
    PIN4(acc[t]);
    SCHED();
    iss(t);
    if (t == NT - 2 && docs) colsum<H>(Fc, cs, ones);
    SCHED();
  }
}

DI int rot7(int s, int r) { const int k = s + r; return k >= 7 ? k - 7 : k; }
DI u32 p2_pair(int r, int spn) { return (2 * rot7(spn >> 1, r) + (spn & 1)) * 16384; }
constexpr int cunit(int m, int nn) { return m == 0 ? (nn == 0 ? 5 : nn - 1) : (nn < 2 ? 3 + nn : 4 + nn); }

struct Sa {
  cf2 u[8];
  cf a1, a8, a16, a64, pq, lw, e0;
  float lamr, lami;
};

template <int H>
DI void phase1(const Args& p, LAS char* L, u32 lbase, srd_t rs, srd_t sx, srd_t s1, srd_t s3, srd_t sw2, int g, int j, int rot, int r7, int w, int lane,
               u32& tagLo, u32& tagHi, const u32 (&vo)[5], Sa& sa, u32& stampv) {
  constexpr int NT = H == 0 ? 5 : 4;
  const int tg = w & 3, c = lane & 15, q = lane >> 4, r4 = c >> 2, pp = lane & 3;
  const u32 ld0 = lbase + w * 4096, ld4 = lbase + G_OFF + w * 896, ld5 = ld4 + G3D, lds_s = lbase + SM_OFF + w * 1024;
  const int srow = 8 * (w & 1) + (lane >> 3);
  const u32 vs = srow * 1024 + (((lane & 7) ^ ((srow >> 1) & 7)) << 4);
  const srd_t sb = mksrd((const char*)((w & 2) ? p.bim : p.bre) + (size_t)j * (16 * 1024), 16 * 1024);
  v2u ep;
  u32 pl0 = 0, pl1 = 0, pl2 = 0;
  v4u xr0 = {0, 0, 0, 0}, xr1 = {0, 0, 0, 0};
  {
    const u32 zero = 0;
    if (H == 0) glds<0>(vs, sb, rot * 128, lds_s, 0);
    asm volatile("global_load_dwordx2 %0, %1, %2 sc1" : "=v"(ep) : "v"(zero), "s"(p.ws + WS_EP) : "memory");
    if (H == 0) {
      const u32 o = (16 * j + c) * 2;
      asm volatile("global_load_ushort %0, %1, %2" : "=v"(pl0) : "v"(o), "s"(p.lre) : "memory");
      asm volatile("global_load_ushort %0, %1, %2" : "=v"(pl1) : "v"(o), "s"(p.lim) : "memory");
      asm volatile("global_load_ushort %0, %1, %2" : "=v"(pl2) : "v"(o), "s"(p.ldt) : "memory");
    } else {
      const u32 o = (8 * j + 2 * tg + (lane >> 5)) * 1024 + (lane & 31) * 16;
      asm volatile("buffer_load_dwordx4 %0, %1, %2, 0 offen" : "=v"(xr0) : "v"(o), "s"(sx) : "memory");
      asm volatile("buffer_load_dwordx4 %0, %1, %2, 0 offen offset:512" : "=v"(xr1) : "v"(o), "s"(sx) : "memory");
    }
  }
  auto op = [&](int i, u32 kk, u32 slot) {
    if (i == 0) glds<0>(vo[0], sx, kk * 128, ld0, slot);
    else if (i == 1) glds<1024>(vo[1], sx, kk * 128, ld0, slot);
    else if (i == 2) glds<2048>(vo[2], sx, kk * 128, ld0, slot);
    else if (i == 3) glds<3072>(vo[3], sx, kk * 128, ld0, slot);
    else if (i == 4) glds56(vo[4], s1, kk * (64 * 3584), ld4, slot);
    else if (i == 5) glds56(vo[4], s3, kk * (64 * 3584), ld5, slot);
    else if (H == 0) glds<0>(vs, sb, kk * 128, lds_s, slot);
  };
#pragma unroll
  for (int i = 0; i < 7; i++) op(i, (rot + 1) & 7, SLOT);
  const u32 xa0 = (64 * tg + 16 * r4 + 8 * (c & 1) + ((c >> 1) & 1)) * 128 + ((q ^ (2 * r4 + (c & 1))) << 4);
  const u32 sa0 = SM_OFF + c * 128 + ((q ^ ((c >> 1) & 7)) << 4);
  const u32 ga0 = G_OFF + q * 896 + r4 * 224 + 8 * pp;
  const u32 gm0 = ga0 + 96 + (pp >= 2 ? G3D - 16 : 0);
  auto ra = [&](u32 slot, int ks) { return Ra{slot + (xa0 ^ (ks * 64)), slot + (sa0 ^ (ks * 64)), slot + ga0 + ks * 3584, slot + gm0 + ks * 3584}; };
  f4 acc[NT][4], cs[NT];
#pragma unroll
  for (int t = 0; t < NT; t++) {
    cs[t] = f4{0, 0, 0, 0};
#pragma unroll
    for (int i = 0; i < 4; i++) acc[t][i] = f4{0, 0, 0, 0};
  }
  v4u ones = {0x3F803F80u, 0x3F803F80u, 0x3F803F80u, 0x3F803F80u};
  asm volatile("" : "+v"(ones));
  const int own = (tg + 2 * H) & 3;
  STAMP(1);
  if (H == 0) asm volatile("s_waitcnt vmcnt(7)" : "+v"(ep), "+v"(pl0), "+v"(pl1), "+v"(pl2)::"memory");
  else asm volatile("s_waitcnt vmcnt(6)" : "+v"(ep), "+v"(xr0), "+v"(xr1)::"memory");
  BAR();
  STAMP(2);
  CSTAMP(14);
  Fr<H> F0, F1;
  {
    const Ra a = ra(0, 0);
#pragma unroll
    for (int t = 0; t < NT; t++) ldpiece<H>(F0, L, a, t);
    SCHED();
  }
  {
    const u64 tag = (((u64)ep.y << 32) | ep.x) + 1;
    tagLo = (u32)tag;
    tagHi = (u32)(tag >> 32);
  }
  half<H>(F0, F1, L, ra(0, 1), acc, cs, ones, own == 0, [&](int t) {
    if (t == 0) op(6, (rot + 2) & 7, 2 * SLOT);
    if (t < 3) { op(2 * t, (rot + 2) & 7, 2 * SLOT); op(2 * t + 1, (rot + 2) & 7, 2 * SLOT); }
  });
  if (H == 0) {
    sa.lamr = bf2f(pl0); sa.lami = bf2f(pl1);
    const float dt = rbf(expf(bf2f(pl2)));
    float sn, cn;
    sincosf(dt * sa.lami, &sn, &cn);
    const float e = expf(dt * sa.lamr);
    sa.a1 = cf{e * cn, e * sn};
    sa.a8 = csq(csq(csq(sa.a1)));
    sa.a16 = csq(sa.a8);
    sa.a64 = csq(csq(sa.a16));
    asm volatile("" : "+v"(sa.a1.r), "+v"(sa.a1.i), "+v"(sa.a8.r), "+v"(sa.a8.i), "+v"(sa.a16.r), "+v"(sa.a16.i), "+v"(sa.a64.r), "+v"(sa.a64.i));
  } else {
    f2 v[8];
#pragma unroll
    for (int k = 0; k < 4; k++) { v[k] = f2{bf2f(xr0[k]), bfhi(xr0[k])}; v[4 + k] = f2{bf2f(xr1[k]), bfhi(xr1[k])}; }
    const f2 s = ((v[0] + v[1]) + (v[2] + v[3])) + ((v[4] + v[5]) + (v[6] + v[7]));
    const float m = sumx16(sum16(s.x + s.y)) * (1.f / 512.f);
    f2 s2 = {0.f, 0.f};
#pragma unroll
    for (int k = 0; k < 8; k++) { const f2 d = v[k] - sp(m); s2 = fma2(d, d, s2); }
    const float var = sumx16(sum16(s2.x + s2.y)) * (1.f / 512.f);
    const float sd = rbf(sqrtf(rbf(rbf(var) + 1e-5f)));
    const v4u o = {f2u(rbf(m)), tagLo, f2u(1.f / sd), tagHi};
    __builtin_amdgcn_raw_buffer_store_b128(o, rs, (lane & 31) ? 0x7FFFFFF0 : WS_SG + (g * 256 + 8 * j + 2 * tg + (lane >> 5)) * 16, 0, AUX_SC1);
    if (tg < 3) {
      const int b = g * 32 + j;
      const char* src = tg == 0 ? (const char*)p.w2 + b * 7168 : (const char*)(tg == 1 ? p.cre : p.cim) + b * 2048;
      const u32 off = (tg == 0 ? (lane < 56 ? lane : 0) : (lane & 15)) * 128;
      asm volatile("s_mov_b32 m0, %2\n\ts_nop 0\n\tglobal_load_lds_dword %0, %1" ::"v"(off), "s"(src), "s"(lbase + DUMMY) : "memory", "m0");
    }
  }
  u32 cur = 0, nxt = SLOT;
  SCHED();
#pragma unroll 1
  for (int s = 0; s < 5; s++) {
    const Ra an0 = ra(nxt, 0);
    const u32 kk = (rot + s + 3) & 7;
    WAITL0();
    DSTAMP(16 + 3 * s);
    WAITV(6);
    DSTAMP(17 + 3 * s);
    BAR();
    DSTAMP(18 + 3 * s);
    SCHED();
    half<H>(F1, F0, L, an0, acc, cs, ones, ((2 * s + 1) & 3) == own, [&](int t) {
      if (H == 0) {
        if (t == 0) op(6, kk, cur);
        else if (t < 4) op(t - 1, kk, cur);
      } else if (t < 3) op(t, kk, cur);
    });
    half<H>(F0, F1, L, ra(nxt, 1), acc, cs, ones, ((2 * s + 2) & 3) == own, [&](int t) {
      if (t < 3) op(3 + t, kk, cur);
    });
    cur = nxt;
    nxt = nxt == 2 * SLOT ? 0 : nxt + SLOT;
  }
  const u32 vwe = (lane >> 3) * 1024 + (((lane & 7) ^ (2 * ((lane >> 4) & 1))) << 4), vwo = (vwe ^ 64) + 8192 - 1024;
  const u32 lw2 = lbase + w * PW;
#pragma unroll 1
  for (int s = 5; s < 7; s++) {
    const Ra an0 = ra(nxt, 0);
    const u32 o0 = p2_pair(r7, 3 * s - 15), o1 = p2_pair(r7, 3 * s - 14), o2 = p2_pair(r7, 3 * s - 13), lc = lw2 + cur;
    WAITL0();
    DSTAMP(16 + 3 * s);
    WAITV(6);
    DSTAMP(17 + 3 * s);
    BAR();
    DSTAMP(18 + 3 * s);
    SCHED();
    half<H>(F1, F0, L, an0, acc, cs, ones, ((2 * s + 1) & 3) == own, [&](int t) {
      if (H == 1 && t == 0) {
        if (s == 5) {
          asm volatile("s_mov_b32 m0, %3\n\ts_nop 0\n\tbuffer_load_dwordx4 %0, %1, %2 offen sc1 lds" ::"v"(lane * 16), "s"(rs),
                       "s"(WS_SG + (g * 256 + 64 * tg) * 16), "s"(lbase + GR + tg * 1024)
                       : "memory", "m0");
        } else {
          v4u gr = lds128(L, GR + tg * 1024 + lane * 16);
#pragma unroll 1
          for (u32 spins = 0; !allok(gr.y == tagLo && gr.w == tagHi) && spins < SPIN_CAP; spins++)
            asm volatile("s_sleep 1\n\tbuffer_load_dwordx4 %0, %1, %2, %3 offen sc1\n\ts_waitcnt vmcnt(0)"
                         : "=&v"(gr)
                         : "v"(lane * 16), "s"(rs), "s"(WS_SG + (g * 256 + 64 * tg) * 16)
                         : "memory");
          const u32 pos = GR + tg * 1024 + ((lane & 48) + 4 * ((lane >> 1) & 3) + 2 * (lane & 1) + ((lane >> 3) & 1)) * 4;
          *(LAS u32*)(L + pos) = gr.x;
          *(LAS u32*)(L + pos + 256) = gr.z;
        }
      }
      if (t == 1) glds2<0>(vwe, vwo, sw2, o0, lc);
    });
    half<H>(F0, F1, L, ra(nxt, 1), acc, cs, ones, ((2 * s + 2) & 3) == own, [&](int t) {
      if (t == 0) glds2<2048>(vwe, vwo, sw2, o1, lc);
      if (t == 2) glds2<4096>(vwe, vwo, sw2, o2, lc);
    });
    cur = nxt;
    nxt = nxt == 2 * SLOT ? 0 : nxt + SLOT;
  }
  if (own == 3) colsum<H>(F1, cs, ones);
  {
    const u32 cp = CSP + tg * 576;
    if (H == 0) {
      if (q == 0) { *(LAS float*)(L + cp + c * 4) = cs[0][0]; *(LAS float*)(L + cp + 64 + c * 4) = cs[1][0]; }
      if (c == 0) {
#pragma unroll
        for (int t = 2; t < 5; t++) *(LAS f4*)(L + cp + 64 * t + 16 * q) = cs[t];
      }
    } else if (c == 0) {
#pragma unroll
      for (int t = 0; t < 4; t++) *(LAS f4*)(L + cp + 320 + 64 * t + 16 * q) = cs[t];
    }
  }
  WAITL0();
  CSTAMP(37);
  BAR();
  CSTAMP(39);
  float mcol[4], rcol[4];
  f4 mrow[4], rrow[4], cp4[NT][4];
  {
    const u32 csa = CSP + (H == 0 ? 0 : 320) + 16 * q, cs1 = CSP + c * 4, gra = GR + tg * 1024 + (16 * r4 + (c & 3)) * 4, grr = GR + tg * 1024 + 64 * q;
#pragma unroll
    for (int t = 0; t < NT; t++) {
#pragma unroll
      for (int k = 0; k < 4; k++) {
        if (H == 0 && t < 2) cp4[t][k][0] = ldsf(L, cs1 + 64 * t + 576 * k);
        else cp4[t][k] = ldsf4(L, csa + 64 * t + 576 * k);
      }
      if (t < 4) {
        mcol[t] = ldsf(L, gra + 16 * t);
        rcol[t] = ldsf(L, gra + 16 * t + 256);
        if (H == 0) { mrow[t] = ldsf4(L, grr + 16 * t); rrow[t] = ldsf4(L, grr + 16 * t + 256); }
      }
      mmtile<H>(F1, acc[t], t);
#pragma unroll
      for (int k = 0; k < 4; k++) { SGB(0x8, 1); SGB(0x100, 2); }
      PIN4(acc[t]);
      SCHED();
      if (t == 0) glds2<SLOT>(vwe, vwo, sw2, p2_pair(r7, 6), lw2);
      if (t == 1) glds2<SLOT + 2048>(vwe, vwo, sw2, p2_pair(r7, 7), lw2);
      if (t == 2) glds2<SLOT + 4096>(vwe, vwo, sw2, p2_pair(r7, 8), lw2);
      SCHED();
    }
  }
  STAMP(3);
  CSTAMP(15);
#pragma unroll
  for (int t = 0; t < NT; t++) {
    if (H == 0 && t < 2) cs[t][0] = (cp4[t][0][0] + cp4[t][1][0]) + (cp4[t][2][0] + cp4[t][3][0]);
    else cs[t] = (cp4[t][0] + cp4[t][1]) + (cp4[t][2] + cp4[t][3]);
  }
  const int oct0 = 44 * (j >> 2) + 16 + 7 * (j & 3);
  const u32 hbs = WS_HB + (g * 4 + tg) * 4 * HB_TILE + oct0 * 256;
  if (H == 1) {
    const u32 hv = c * 16 + (2 + (q >> 1) + 2 * (q & 1)) * 256;
#pragma unroll
    for (int i = 0; i < 4; i++) {
      if (i == BSPLIT) { SCHED(); WAITL0(); BAR(); SCHED(); }
      const f2 nm = sp(-mcol[i]);
      const float nrl = rcol[i] * NL2E, ir2 = rcp(rcol[i] * rcol[i]);
      u32 v[4];
#pragma unroll
      for (int k = 0; k < 4; k++) {
        const int t = 2 * (k >> 1), h = k & 1;
        v[k] = swiglu2(fma2(nm, half2(cs[t], h), half2(acc[t][i], h)), fma2(nm, half2(cs[t + 1], h), half2(acc[t + 1][i], h)), nrl, ir2);
      }
      __builtin_amdgcn_raw_buffer_store_b128(swap16x2(v[0], v[1], v[2], v[3]), rs, hv + hbs + i * HB_TILE, 0, AUX_SC1);
    }
  } else {
#pragma unroll
    for (int k = 0; k < 8; k++) {
      const f2 m2 = half2(mrow[k >> 1], k & 1), r2 = half2(rrow[k >> 1], k & 1);
      sa.u[k].r = fma2(-m2, sp(cs[0][0]), half2(acc[0][k >> 1], k & 1)) * r2;
      sa.u[k].i = fma2(-m2, sp(cs[1][0]), half2(acc[1][k >> 1], k & 1)) * r2;
    }
    cf2 e2 = sa.u[0];
#pragma unroll
    for (int k = 1; k < 8; k++) e2 = cfma2(sa.a1, e2, sa.u[k]);
    {
      sa.e0 = cf{e2.r.x, e2.i.x};
      const cf e = cfma(sa.a8, sa.e0, cf{e2.r.y, e2.i.y});
      float er[4], ei[4];
      bcast4(e.r, er);
      bcast4(e.i, ei);
      const cf i0{er[0], ei[0]};
      const cf i1 = cfma(sa.a16, i0, cf{er[1], ei[1]});
      const cf i2 = cfma(sa.a16, i1, cf{er[2], ei[2]});
      const cf i3 = cfma(sa.a16, i2, cf{er[3], ei[3]});
      *(LAS v2u*)(L + XCH + (tg * 16 + c) * 8) = v2u{f2u(i3.r), f2u(i3.i)};
      sa.pq = csel(q & 2, csel(q & 1, i2, i1), csel(q & 1, i0, cf{0.f, 0.f}));
    }
    SCHED();
    WAITL0();
    BAR();
    cf eb{0.f, 0.f};
    sa.lw = eb;
#pragma unroll
    for (int t = 0; t < 4; t++) {
      if (t == tg) sa.lw = eb;
      const v2u wv = *(LAS v2u*)(L + XCH + (t * 16 + c) * 8);
      eb = cfma(sa.a64, eb, cf{u2f(wv.x), u2f(wv.y)});
    }
    if (tg == 0) {
      const v4u o = {f2u(eb.r), tagLo, f2u(eb.i), tagHi};
      __builtin_amdgcn_raw_buffer_store_b128(o, rs, q ? 0x7FFFFFF0 : WS_AG + (g * 512 + 16 * j + c) * 16, 0, AUX_SC1);
    }
    SCHED();
    STAMP(8);
    u32 zx[2], zy[2];
#pragma unroll
    for (int ip = 0; ip < 2; ip++) {
      u32 px[2], py[2];
      f4 tm[2];
#pragma unroll
      for (int ii = 0; ii < 2; ii++) {
        const int i = 2 * ip + ii;
        const f2 nm = sp(-mcol[i]);
        const float nrl = rcol[i] * NL2E, ir2 = rcp(rcol[i] * rcol[i]);
        px[ii] = swiglu2(fma2(nm, cs[2].xy, acc[2][i].xy), fma2(nm, cs[3].xy, acc[3][i].xy), nrl, ir2);
        py[ii] = swiglu2(fma2(nm, cs[2].zw, acc[2][i].zw), fma2(nm, cs[3].zw, acc[3][i].zw), nrl, ir2);
        tm[ii].xy = fma2(nm, cs[4].xy, acc[4][i].xy);
        tm[ii].zw = fma2(nm, cs[4].zw, acc[4][i].zw);
      }
      __builtin_amdgcn_raw_buffer_store_b128(swap16x2(px[0], py[0], px[1], py[1]), rs, c * 16 + (q & 1) * HB_TILE + (q >> 1) * 256 + hbs + 2 * ip * HB_TILE, 0,
                                             AUX_SC1);
      float rlo = rcol[2 * ip];
      asm volatile("" : "+v"(rlo));
      const float rsel = q < 2 ? rlo : rcol[2 * ip + 1];
      const float nrl = rsel * NL2E, ir2 = rcp(rsel * rsel);
      f4 gm, um;
#pragma unroll
      for (int r = 0; r < 4; r++) {
        auto s = __builtin_amdgcn_permlane32_swap(f2u(tm[0][r]), f2u(tm[1][r]), false, false);
        gm[r] = u2f(s[0]);
        um[r] = u2f(s[1]);
      }
      zx[ip] = swiglu2(gm.xy, um.xy, nrl, ir2);
      zy[ip] = swiglu2(gm.zw, um.zw, nrl, ir2);
    }
    __builtin_amdgcn_raw_buffer_store_b128(swap16x2(zx[0], zy[0], zx[1], zy[1]), rs, c * 16 + (2 * (q & 1) + (q >> 1)) * HB_TILE + hbs + 6 * 256, 0, AUX_SC1);
  }
  STAMP(4);
}

DI void poll_flags(const GAS u64* f, int lane, u64 tag, int mask, u64 v) {
  const bool need = (mask >> (lane & 3)) & 1;
#pragma unroll 1
  for (u32 spins = 0; !allok(!need || v == tag) && spins < SPIN_CAP; spins++) {
    __builtin_amdgcn_s_sleep(1);
    v = __hip_atomic_load(f, RLX_AGENT);
  }
  __builtin_amdgcn_fence(__ATOMIC_ACQUIRE, "wavefront");
}
}

__global__ __launch_bounds__(512, 2) void s5_glu_fused(Args p) {
  __shared__ __attribute__((aligned(256))) char smem[LDS_BYTES];
  LAS char* L = (LAS char*)smem;
  const u32 lbase = (u32)(size_t)L;
  int lane = threadIdx.x & 63;
  const int w = __builtin_amdgcn_readfirstlane(threadIdx.x >> 6);
  const int b = blockIdx.x, xcd = b & 7, idx = b >> 3;
  const int g = (xcd & 1) * 4 + (idx & 3), j = (xcd >> 1) * 8 + (idx >> 2);
  const int rot = ROT == 0 ? ((idx >> 2) + 2 * (idx & 3)) & 7 : ROT == 1 ? 0 : ROT == 2 ? 4 * (((idx >> 2) + idx) & 1) : 2 * (((idx >> 2) + idx) & 3);
  u32 stampv = 0;
  u64 tstart; asm volatile("s_memrealtime %0\n\ts_waitcnt lgkmcnt(0)" : "=s"(tstart)::"memory");
  const srd_t sx = mksrd((const char*)p.x + (size_t)g * (256 * 1024), 256 * 1024);
  u32 vo[5];
#pragma unroll
  for (int e = 0; e < 4; e++) vo[e] = (32 * w + 8 * e + (lane >> 3)) * 1024 + (((lane & 7) ^ ((4 * w + e) & 7)) << 4) - e * 1024;
  glds<0>(vo[0], sx, rot * 128, lbase + w * 4096, 0);
  glds<1024>(vo[1], sx, rot * 128, lbase + w * 4096, 0);
  glds<2048>(vo[2], sx, rot * 128, lbase + w * 4096, 0);
  glds<3072>(vo[3], sx, rot * 128, lbase + w * 4096, 0);
  const srd_t s1 = mksrd((const char*)p.w1 + j * 112, 512 * 3584 - j * 112), s3 = mksrd((const char*)p.w3 + j * 112, 512 * 3584 - j * 112);
  {
    const int gpos = (lane * 37) >> 8, gch = lane - gpos * 7;
    vo[4] = (8 * w + 4 * (gpos & 1) + (gpos >> 1)) * 3584 + gch * 16;
    glds56(vo[4], s1, rot * (64 * 3584), lbase + G_OFF + w * 896, 0);
    glds56(vo[4], s3, rot * (64 * 3584), lbase + G_OFF + w * 896, G3D);
  }
  SCHED();
  const int u = j >> 3, n = j & 7, xn = g & 3, r7 = rot7(n == 7 ? 0 : n, 2 * xn);
  const srd_t rs = mksrd(p.ws, WS_BYTES);
  const srd_t sw2 = mksrd((const char*)p.w2 + (size_t)w * (224 * 1024) + n * 128, 224 * 1024 - n * 128);
  u32 tagLo, tagHi;
  Sa sa;
  if (w < 4) phase1<0>(p, L, lbase, rs, sx, s1, s3, sw2, g, j, rot, r7, w, lane, tagLo, tagHi, vo, sa, stampv);
  else phase1<1>(p, L, lbase, rs, sx, s1, s3, sw2, g, j, rot, r7, w, lane, tagLo, tagHi, vo, sa, stampv);

  asm volatile("" : "+v"(lane));
  const int tg = w & 3, c = lane & 15, q = lane >> 4, r4 = c >> 2, pp = lane & 3;
  const u64 tag = ((u64)tagHi << 32) | tagLo;
  GAS u64* flg = (GAS u64*)(p.ws + WS_FL) + ((g * 4 + tg) * 32 + j) * 4;
  const u32 wo = w * PW, lw2 = lbase + wo;
  const u32 vwe = (lane >> 3) * 1024 + (((lane & 7) ^ (2 * (q & 1))) << 4), vwo = (vwe ^ 64) + 8192 - 1024;
  const u32 vce = (lane >> 3) * 1024 + (((lane & 7) ^ q) << 4), vco = (vce ^ 64) + 8192 - 1024;
  const srd_t scr = mksrd((const char*)p.cre + (size_t)n * (64 * 1024) + w * 128, 64 * 1024 - w * 128);
  const srd_t sci = mksrd((const char*)p.cim + (size_t)n * (64 * 1024) + w * 128, 64 * 1024 - w * 128);
  u32 tl[4];
#pragma unroll
  for (int nn = 0; nn < 4; nn++)
    tl[nn] = wo + (q & 1) * 1024 + r4 * 128 + ((((2 * (nn ^ xn)) ^ (2 * (((r4 >> 1) & 1) + 2 * (q & 1)))) + (pp >> 1)) << 4) + (pp & 1) * 8;
  v4u wf[7][4];
  auto rdw = [&](int s, int ua, int ub) {
    const u32 o = (q >> 1) ? unitb(ub) : unitb(ua);
#pragma unroll
    for (int nn = 0; nn < 4; nn++) wf[s][nn] = tr2(L, o + tl[nn], 512);
  };
  WAITV(9);
  rdw(0, 0, 1); rdw(1, 2, 3); rdw(2, 4, 5);
  WAITL0();
  glds2<unitb(0)>(vwe, vwo, sw2, p2_pair(r7, 9), lw2);
  glds2<unitb(1)>(vwe, vwo, sw2, p2_pair(r7, 10), lw2);
  glds2<unitb(2)>(vwe, vwo, sw2, p2_pair(r7, 11), lw2);
  glds2<unitb(3)>(vwe, vwo, sw2, p2_pair(r7, 12), lw2);
  glds2<unitb(4)>(vwe, vwo, sw2, p2_pair(r7, 13), lw2);
  glds2<unitb(cunit(0, 0))>(vce, vco, scr, xn * 16384, lw2);
  cf wt, wg0, wg1, wt8;
  cf2 h0;
  if (w < 4) {
    const float nr = sa.a1.r - 1.f, ni = sa.a1.i, den = 2.f / (sa.lamr * sa.lamr + sa.lami * sa.lami);
    const cf c2{(nr * sa.lamr + ni * sa.lami) * den, (ni * sa.lamr - nr * sa.lami) * den};
    const cf a32 = csq(sa.a16), a128 = csq(sa.a64), a256 = csq(a128), a512 = csq(a256), a1024 = csq(a512);
    const cf one{1.f, 0.f}, zero{0.f, 0.f};
    const cf cw = cmul(c2, cmul(csel(q & 1, sa.a16, one), csel(q & 2, a32, one)));
    wt = cmul(cw, cmul(csel(tg & 1, sa.a64, one), csel(tg & 2, a128, one)));
    wt8 = cmul(wt, sa.a8);
    const int e0 = g - 1 - q, e1 = g - 5 - q;
    wg1 = cmul(csel(e0 & 1, a256, one), csel(e0 & 2, a512, one));
    wg0 = csel(e0 >= 0, cmul(wg1, csel(e0 & 4, a1024, one)), zero);
    wg1 = csel(e1 >= 0, wg1, zero);
    const cf k0 = cfma(cw, sa.lw, cmul(c2, sa.pq));
    const cf k1 = cfma(sa.a8, k0, cmul(c2, sa.e0));
    h0 = cf2{f2{k0.r, k1.r}, f2{k0.i, k1.i}};
#pragma unroll
    for (int k = 0; k < 8; k++) {
      sa.u[k] = cmul2(c2, sa.u[k]);
      asm volatile("" : "+v"(sa.u[k].r), "+v"(sa.u[k].i));
    }
    asm volatile("" : "+v"(wt.r), "+v"(wt.i), "+v"(wt8.r), "+v"(wt8.i), "+v"(wg0.r), "+v"(wg0.i), "+v"(wg1.r), "+v"(wg1.i), "+v"(h0.r), "+v"(h0.i));
  }
  WAITV(12);
  if (lane == 0) __hip_atomic_store(flg + (w >> 2), tag, RLX_AGENT);
  STAMP(5);
  if (w < 4) {
    cf2 h = h0;
    if (g > 0) {
      const u32 ao = WS_AG + (q * 512 + 16 * j + c) * 16;
      v4u g0, g1;
#pragma unroll 1
      for (u32 spins = 0;; spins++) {
        g0 = __builtin_amdgcn_raw_buffer_load_b128(rs, ao, 0, AUX_SC1);
        g1 = __builtin_amdgcn_raw_buffer_load_b128(rs, ao + 4 * 512 * 16, 0, AUX_SC1);
        const bool ok = (q >= g || (g0.y == tagLo && g0.w == tagHi)) && (q + 4 >= g || (g1.y == tagLo && g1.w == tagHi));
        if (allok(ok) || spins > SPIN_CAP) break;
        __builtin_amdgcn_s_sleep(1);
      }
      const cf zz{0.f, 0.f};
      cf hin = cfma(wg0, csel(q < g, cf{u2f(g0.x), u2f(g0.z)}, zz), cmul(wg1, csel(q + 4 < g, cf{u2f(g1.x), u2f(g1.z)}, zz)));
      hin.r = sumx32(sumx16(hin.r));
      hin.i = sumx32(sumx16(hin.i));
      h = cfma2(hin, cf2{f2{wt.r, wt8.r}, f2{wt.i, wt8.i}}, h0);
    }
    STAMP(6);
    const u32 tb = TRB + tg * 1280;
    const u32 twr = tb + c * 40 + q * 8;
    const u32 trd = tb + (q >> 1) * 640 + (8 * (q & 1) + r4) * 40 + pp * 8;
#pragma unroll
    for (int i = 0; i < 4; i++) {
      const cf2 ha = cfma2(sa.a1, h, sa.u[2 * i]);
      h = cfma2(sa.a1, ha, sa.u[2 * i + 1]);
      *(LAS v2u*)(L + twr) = v2u{pk2(ha.r), pk2(h.r)};
      *(LAS v2u*)(L + twr + 640) = v2u{pk2(-ha.i), pk2(-h.i)};
      asm volatile("" ::: "memory");
      const v4u o = tr2(L, trd, 160);
      asm volatile("" ::: "memory");
      __builtin_amdgcn_raw_buffer_store_b128(o, rs, lane * 16 + WS_HB + ((g * 4 + tg) * 4 + i) * HB_TILE + (11 * (j >> 2) + (j & 3)) * 1024, 0, AUX_SC1);
    }
    WAITV(4);
  } else {
    WAITV(1);
    STAMP(6);
  }
  rdw(3, 6, 7); rdw(4, 8, 0); rdw(5, 1, 2); rdw(6, 3, 4);
  WAITL0();
  glds2<unitb(cunit(0, 1))>(vce, vco, scr, (1 ^ xn) * 16384, lw2);
  glds2<unitb(cunit(0, 2))>(vce, vco, scr, (2 ^ xn) * 16384, lw2);
  glds2<unitb(cunit(0, 3))>(vce, vco, scr, (3 ^ xn) * 16384, lw2);
  glds2<unitb(cunit(1, 0))>(vce, vco, sci, (0 ^ xn) * 16384, lw2);
  glds2<unitb(cunit(1, 1))>(vce, vco, sci, (1 ^ xn) * 16384, lw2);
  glds2<unitb(cunit(1, 2))>(vce, vco, sci, (2 ^ xn) * 16384, lw2);
  glds2<unitb(cunit(1, 3))>(vce, vco, sci, (3 ^ xn) * 16384, lw2);
  if (w < 4) {
    WAITV(14);
    if (lane == 0) __hip_atomic_store(flg + 2, tag, RLX_AGENT);
  }
  STAMP(7);

  const GAS u64* fl = (const GAS u64*)(p.ws + WS_FL) + ((g * 4 + u) * 32 + 4 * w) * 4 + (lane & 15);
  poll_flags(fl, lane, tag, 3, __hip_atomic_load(fl, RLX_AGENT));
  STAMP(9);
  const u32 hs = WS_HB + (g * 4 + u) * 4 * HB_TILE + 11 * w * 1024;
  f4 acc[4][4];
#pragma unroll
  for (int i = 0; i < 4; i++)
#pragma unroll
    for (int nn = 0; nn < 4; nn++) acc[i][nn] = f4{0, 0, 0, 0};
  v4u hf[11][4];
  auto ldh = [&](int s) {
    const int ks = s < 7 ? 4 + rot7(s, r7) : s - 7;
#pragma unroll
    for (int i = 0; i < 4; i++) hf[s][i] = __builtin_amdgcn_raw_buffer_load_b128(rs, lane * 16 + i * HB_TILE, hs + ks * 1024, AUX_SC1);
    SCHED();
  };
  auto step = [&](int s, const v4u (&wv)[4]) {
#pragma unroll
    for (int i = 0; i < 4; i++) {
#pragma unroll
      for (int nn = 0; nn < 4; nn++) acc[i][nn] = mfma(wv[nn], hf[s][i], acc[i][nn]);
      PIN4(acc[i]);
    }
    SCHED();
  };
  ldh(0); ldh(1); ldh(2);
  step(0, wf[0]);
  ldh(3); ldh(4);
  step(1, wf[1]);
  const u64 fv = __hip_atomic_load(fl, RLX_AGENT);
  ldh(5); ldh(6);
  step(2, wf[2]);
  step(3, wf[3]);
  step(4, wf[4]);
  STAMP(10);
  poll_flags(fl, lane, tag, 4, fv);
  STAMP(11);
  ldh(7); ldh(8); ldh(9); ldh(10);
  const int ti = w >> 1;
  const int dcol = 64 * n + 16 * (r4 ^ xn) + 4 * pp;
  int tok[2];
  v2u xv[2];
#pragma unroll
  for (int e = 0; e < 2; e++) {
    const int cc = 8 * (w & 1) + 4 * e + q;
    tok[e] = 16 * (cc >> 2) + 8 * (cc & 1) + 2 * ti + ((cc >> 1) & 1);
    xv[e] = *(const GAS v2u*)(p.x + (size_t)(256 * g + 64 * u + tok[e]) * 512 + dcol);
  }
  const v2u dk = *(const GAS v2u*)(p.dsk + dcol);
  u32 ub[4];
#pragma unroll
  for (int nn = 0; nn < 4; nn++) ub[nn] = wo + c * 128 + ((q >> 1) ? unitb(cunit(1, nn)) : unitb(cunit(0, nn)));
  auto ldc = [&](int s) {
    const u32 ca = ((2 * s + (q & 1)) ^ ((c >> 1) & 7)) << 4;
#pragma unroll
    for (int nn = 0; nn < 4; nn++) wf[s][nn] = lds128(L, ub[nn] + ca);
    SCHED();
  };
  ldc(0);
  step(5, wf[5]);
  ldc(1); ldc(2);
  step(6, wf[6]);
  ldc(3);
  step(7, wf[0]); step(8, wf[1]); step(9, wf[2]); step(10, wf[3]);
  STAMP(12);
#pragma unroll
  for (int i = 0; i < 4; i++)
#pragma unroll
    for (int nn = 0; nn < 4; nn++)
      *(LAS f4*)(L + wo + unitb(2 * i + (nn >> 1)) + (nn & 1) * 1024 + q * 256 + ((c ^ (4 * nn + q)) << 4)) = acc[i][nn];
  WAITL0();
  BAR();
  const int v0 = 2 * ti, v1 = v0 + 1;
  const u32 u0 = ((((v0 * 11) >> 5) + 2) % 3) * SLOT + (v0 % 3) * 2048, u1 = ((((v1 * 11) >> 5) + 2) % 3) * SLOT + (v1 % 3) * 2048;
#pragma unroll
  for (int e = 0; e < 2; e++) {
    const int cc = 8 * (w & 1) + 4 * e + q;
    const u32 ra = ((r4 >> 1) ? u1 : u0) + (r4 & 1) * 1024 + pp * 256 + ((cc ^ c) << 4);
    f4 s = ldsf4(L, ra);
#pragma unroll
    for (int v = 1; v < 8; v++) s += ldsf4(L, v * PW + ra);
    const u32 sp_ = GR + u * 1024 + (16 * (cc >> 2) + 4 * ti + (cc & 3)) * 4;
    const float m = ldsf(L, sp_), r = ldsf(L, sp_ + 256);
    const float xs[4] = {bf2f(xv[e].x), bfhi(xv[e].x), bf2f(xv[e].y), bfhi(xv[e].y)};
    const float ds[4] = {bf2f(dk.x), bfhi(dk.x), bf2f(dk.y), bfhi(dk.y)};
#pragma unroll
    for (int k = 0; k < 4; k++) s[k] += xs[k] + rbf(rbf(rbf(xs[k] - m) * r) * ds[k]);
    __builtin_amdgcn_raw_buffer_store_b128(__builtin_bit_cast(v4u, s), mksrd(p.out, 4 << 20), ((256 * g + 64 * u + tok[e]) * 512 + dcol) * 4, 0, AUX_SC1);
  }
  if (g == 7 && j == 0 && threadIdx.x == 0) __hip_atomic_store((GAS u64*)(p.ws + WS_EP), tag, RLX_AGENT);
  if (true) while (__builtin_amdgcn_s_memrealtime() < tstart + 4000) __builtin_amdgcn_s_sleep(1);
}

extern "C" void kernel_launch(void* const* d_in, const int* in_sizes, int n_in, void* d_out, int out_size, void* d_ws, size_t ws_size,
                              hipStream_t stream) {
  Args p;
  p.x = (const u16*)d_in[0];
  p.lre = (const u16*)d_in[1];
  p.lim = (const u16*)d_in[2];
  p.bre = (const u16*)d_in[3];
  p.bim = (const u16*)d_in[4];
  p.cre = (const u16*)d_in[5];
  p.cim = (const u16*)d_in[6];
  p.ldt = (const u16*)d_in[7];
  p.dsk = (const u16*)d_in[8];
  p.w1 = (const u16*)d_in[10];
  p.w3 = (const u16*)d_in[11];
  p.w2 = (const u16*)d_in[12];
  p.out = (float*)d_out;
  p.ws = (unsigned char*)d_ws;
  s5_glu_fused<<<256, 512, 0, stream>>>(p);
}
```

```cpp
#include <hip/hip_runtime.h>
#include <stdint.h>

typedef unsigned short u16;
typedef unsigned int u32;
typedef unsigned long long u64;
typedef u32 v4u __attribute__((ext_vector_type(4)));
typedef u32 v2u __attribute__((ext_vector_type(2)));
typedef float f4 __attribute__((ext_vector_type(4)));
typedef float f2 __attribute__((ext_vector_type(2)));
typedef short v4s __attribute__((ext_vector_type(4)));
typedef __bf16 v8bf __attribute__((ext_vector_type(8)));
typedef __bf16 v2bf __attribute__((ext_vector_type(2)));
typedef __amdgpu_buffer_rsrc_t srd_t;
#define LAS __attribute__((address_space(3)))
#define GAS __attribute__((address_space(1)))
#define DI __device__ __forceinline__
#define RLX_AGENT __ATOMIC_RELAXED, __HIP_MEMORY_SCOPE_AGENT

#define STAMPS 0
#define WARM 1
#define HAUX 16
#define R7M 0
#define DST 0
#define PSLEEP "4"
#define NSH 0
#define BSPLIT 1
#define ROT 1

namespace {
constexpr int SM_OFF = 32768, G_OFF = 36864, G3D = 7184, SLOT = 51456, AUXO = 3 * SLOT;
constexpr int GR = AUXO, CSP = AUXO + 4096, TRB = CSP, XCH = 49152, DUMMY = AUXO + 9216;
constexpr int LDS_BYTES = 163840;
constexpr int PW = 6144;
constexpr int unitb(int v) { return ((v / 3 + 2) % 3) * SLOT + (v % 3) * 2048; }
#define SH_FL 0
#define SH_AG 0
#define SH_HB 0
constexpr int WS_EP = 0, WS_FL = 4096 + SH_FL, WS_AG = 65536 + SH_AG, WS_SG = 196608, WS_HB = (1 << 20) + SH_HB, HB_TILE = 88 * 1024;
constexpr int WS_BYTES = WS_HB + 128 * HB_TILE;
constexpr int WS_STAMP = 32 << 20, NSTAMP = 14;
constexpr int AUX_SC1 = 16;
constexpr u32 SPIN_CAP = 1u << 17;
constexpr float NL2E = -1.4426950408889634f;

DI float bf2f(u32 b) { return __builtin_bit_cast(float, b << 16); }
DI float bfhi(u32 b) { return __builtin_bit_cast(float, b & 0xFFFF0000u); }
DI float u2f(u32 b) { return __builtin_bit_cast(float, b); }
DI u32 f2u(float f) { return __builtin_bit_cast(u32, f); }
DI float rbf(float f) { return (float)(__bf16)f; }
DI u32 pk2(f2 a) { return __builtin_bit_cast(u32, __builtin_convertvector(a, v2bf)); }
DI float rcp(float x) { return __builtin_amdgcn_rcpf(x); }
DI float ffma(float a, float b, float c) { return __builtin_fmaf(a, b, c); }
DI f2 fma2(f2 a, f2 b, f2 c) { return __builtin_elementwise_fma(a, b, c); }
DI f2 sp(float a) { return f2{a, a}; }
DI f2 half2(f4 v, int h) { return h ? v.zw : v.xy; }
DI f4 mfma(v4u a, v4u b, f4 c) {
  return __builtin_amdgcn_mfma_f32_16x16x32_bf16(__builtin_bit_cast(v8bf, a), __builtin_bit_cast(v8bf, b), c, 0, 0, 0);
}
DI v4u lds128(LAS char* L, u32 a) { return *(LAS v4u*)(L + a); }
DI f4 ldsf4(LAS char* L, u32 a) { return *(LAS f4*)(L + a); }
DI float ldsf(LAS char* L, u32 a) { return *(LAS float*)(L + a); }
DI v4u tr2(LAS char* L, u32 a, int d) {
  v2u lo = __builtin_bit_cast(v2u, __builtin_amdgcn_ds_read_tr16_b64_v4i16((LAS v4s*)(L + a)));
  v2u hi = __builtin_bit_cast(v2u, __builtin_amdgcn_ds_read_tr16_b64_v4i16((LAS v4s*)(L + a + d)));
  return v4u{lo.x, lo.y, hi.x, hi.y};
}
DI srd_t mksrd(const void* p, int bytes) { return __builtin_amdgcn_make_buffer_rsrc((void*)p, 0, bytes, 0x00020000); }
DI u32 rfl(u32 x) { return __builtin_amdgcn_readfirstlane(x); }
#pragma clang diagnostic ignored "-Winline-asm"
template <int OFF> DI void glds(u32 voff, srd_t srd, u32 soff, u32 la, u32 lb) {
  asm volatile("s_add_u32 m0, %3, %4\n\ts_nop 0\n\tbuffer_load_dwordx4 %0, %1, %2 offen offset:%5 lds" ::"v"(voff), "s"(srd), "s"(soff), "s"(la), "s"(lb),
               "n"(OFF)
               : "memory", "m0", "scc");
}
DI void glds56(u32 voff, srd_t srd, u32 soff, u32 la, u32 lb) {
  asm volatile("s_add_u32 m0, %3, %4\n\ts_mov_b32 exec_hi, 0xffffff\n\tbuffer_load_dwordx4 %0, %1, %2 offen lds\n\ts_mov_b32 exec_hi, -1" ::"v"(voff),
               "s"(srd), "s"(soff), "s"(la), "s"(lb)
               : "memory", "m0", "scc");
}
template <int LB> DI void glds2(u32 ve, u32 vo, srd_t srd, u32 soff, u32 la) {
  asm volatile("s_add_u32 m0, %4, %5\n\ts_nop 0\n\tbuffer_load_dwordx4 %0, %2, %3 offen lds\n\tbuffer_load_dwordx4 %1, %2, %3 offen offset:1024 lds" ::"v"(ve),
               "v"(vo), "s"(srd), "s"(soff), "s"(rfl(la)), "n"(LB)
               : "memory", "m0", "scc");
}
#define WAITV(n) asm volatile("s_waitcnt vmcnt(" #n ")" ::: "memory")
#define WAITL0() do { asm volatile("" ::: "memory"); __builtin_amdgcn_s_waitcnt(0xC07F); asm volatile("" ::: "memory"); } while (0)
#define BAR() __builtin_amdgcn_s_barrier()
#define SCHED() __builtin_amdgcn_sched_barrier(0)
#define SGB(m, n) __builtin_amdgcn_sched_group_barrier(m, n, 0)
#define PIN4(a) asm volatile("" : "+v"((a)[0]), "+v"((a)[1]), "+v"((a)[2]), "+v"((a)[3]))

template <int V> struct IC { static constexpr int value = V; };
template <int B, int E, class F> DI void static_for(F f) {
  if constexpr (B < E) {
    f(IC<B>{});
    static_for<B + 1, E>(f);
  }
}
DI bool allok(bool c) { return __builtin_amdgcn_ballot_w64(!c) == 0; }
struct cf { float r, i; };
DI float afma(float a, float b, float c) { float r; asm("v_fma_f32 %0, %1, %2, %3" : "=v"(r) : "v"(a), "v"(b), "v"(c)); return r; }
DI float anfma(float a, float b, float c) { float r; asm("v_fma_f32 %0, -%1, %2, %3" : "=v"(r) : "v"(a), "v"(b), "v"(c)); return r; }
DI cf cmul(cf a, cf b) { return cf{anfma(a.i, b.i, a.r * b.r), ffma(a.r, b.i, a.i * b.r)}; }
DI cf cfma(cf a, cf b, cf c) { return cf{afma(a.r, b.r, anfma(a.i, b.i, c.r)), ffma(a.r, b.i, ffma(a.i, b.r, c.i))}; }
DI cf csq(cf a) { return cf{anfma(a.i, a.i, a.r * a.r), 2.f * a.r * a.i}; }
DI cf csel(bool p, cf a, cf b) { return cf{p ? a.r : b.r, p ? a.i : b.i}; }
struct cf2 { f2 r, i; };
DI cf2 cfma2(cf a, cf2 b, cf2 c) { return cf2{fma2(sp(a.r), b.r, fma2(sp(-a.i), b.i, c.r)), fma2(sp(a.r), b.i, fma2(sp(a.i), b.r, c.i))}; }
DI cf2 cmul2(cf a, cf2 b) { return cf2{fma2(sp(a.r), b.r, sp(-a.i) * b.i), fma2(sp(a.r), b.i, sp(a.i) * b.r)}; }
template <int CTRL> DI float dpp(float x) { return u2f(__builtin_amdgcn_mov_dpp(f2u(x), CTRL, 0xf, 0xf, true)); }
DI float sum16(float v) {
  v += dpp<0xB1>(v); v += dpp<0x4E>(v); v += dpp<0x141>(v); v += dpp<0x140>(v);
  return v;
}
DI void sw16(u32& a, u32& b) { auto r = __builtin_amdgcn_permlane16_swap(a, b, false, false); a = r[0]; b = r[1]; }
DI void sw32(u32& a, u32& b) { auto r = __builtin_amdgcn_permlane32_swap(a, b, false, false); a = r[0]; b = r[1]; }
#define SWPAD2(a, b) asm volatile("s_nop 1" : "+v"(a), "+v"(b))
#define SWPAD4(a, b, c, d) asm volatile("s_nop 1" : "+v"(a), "+v"(b), "+v"(c), "+v"(d))
DI float sumx16(float v) { u32 a = f2u(v), b = a; sw16(a, b); SWPAD2(a, b); return u2f(a) + u2f(b); }
DI float sumx32(float v) { u32 a = f2u(v), b = a; sw32(a, b); SWPAD2(a, b); return u2f(a) + u2f(b); }
DI void bcast4(float v, float (&o)[4]) {
  u32 a = f2u(v), b = a;
  sw16(a, b);
  SWPAD2(a, b);
  u32 a2 = a, b2 = b;
  sw32(a, a2);
  sw32(b, b2);
  SWPAD4(a, b, a2, b2);
  o[0] = u2f(a); o[1] = u2f(b); o[2] = u2f(a2); o[3] = u2f(b2);
}
DI v4u swap16x2(u32 ax, u32 ay, u32 bx, u32 by) {
  sw16(ax, bx);
  sw16(ay, by);
  v4u o = {ax, ay, bx, by};
  asm volatile("s_nop 1" : "+v"(o));
  return o;
}

struct Args {
  const u16 *x, *lre, *lim, *bre, *bim, *cre, *cim, *ldt, *dsk, *w1, *w3, *w2;
  float* out;
  unsigned char* ws;
};

#define STAMP(k) do { } while (0)
#define CSTAMP(k) do { } while (0)
#define DSTAMP(k) do { } while (0)

DI u32 swiglu2(f2 tg, f2 tu, float nrl, float ir2) {
  const f2 a = tg * sp(nrl);
  const f2 e = {__builtin_amdgcn_exp2f(a.x), __builtin_amdgcn_exp2f(a.y)};
  const f2 d = fma2(e, sp(ir2), sp(ir2));
  const f2 r = {rcp(d.x), rcp(d.y)};
  return pk2(tg * tu * r);
}

template <int H> struct Fr { v4u x[4]; v4u w[H == 0 ? 5 : 4]; };
struct Ra { u32 xa, sa, ga, gm; };

template <int H> DI void ldpiece(Fr<H>& F, LAS char* L, const Ra& a, int k) {
  if (k == 0) { F.x[0] = lds128(L, a.xa); F.x[1] = lds128(L, a.xa + 256); F.x[2] = lds128(L, a.xa + 512); }
  if (H == 0) {
    if (k == 1) { F.x[3] = lds128(L, a.xa + 768); F.w[0] = lds128(L, a.sa); F.w[1] = lds128(L, a.sa + 2048); }
    if (k == 2) { F.w[2] = tr2(L, a.ga, 112); F.w[3] = tr2(L, a.ga + G3D, 112); }
    if (k == 3) F.w[4] = tr2(L, a.gm, 112);
  } else {
    if (k == 0) F.x[3] = lds128(L, a.xa + 768);
    if (k == 1) { F.w[0] = tr2(L, a.ga + 32, 112); F.w[1] = tr2(L, a.ga + G3D + 32, 112); }
    if (k == 2) { F.w[2] = tr2(L, a.ga + 64, 112); F.w[3] = tr2(L, a.ga + G3D + 64, 112); }
  }
}
template <int H> DI void mmtile(const Fr<H>& F, f4 (&acc)[4], int t) {
#pragma unroll
  for (int i = 0; i < 4; i++) {
    if (H == 0 && t < 2) acc[i] = mfma(F.x[i], F.w[t], acc[i]);
    else acc[i] = mfma(F.w[t], F.x[i], acc[i]);
  }
}
template <int H> DI void colsum(const Fr<H>& F, f4 (&cs)[H == 0 ? 5 : 4], v4u ones) {
#pragma unroll
  for (int t = 0; t < (H == 0 ? 5 : 4); t++) {
    if (H == 0 && t < 2) cs[t] = mfma(ones, F.w[t], cs[t]);
    else cs[t] = mfma(F.w[t], ones, cs[t]);
  }
}
template <int H, class I>
DI void half(const Fr<H>& Fc, Fr<H>& Fn, LAS char* L, const Ra& a, f4 (&acc)[H == 0 ? 5 : 4][4], f4 (&cs)[H == 0 ? 5 : 4], v4u ones, bool docs, I iss) {
  constexpr int NT = H == 0 ? 5 : 4;
#pragma unroll
  for (int t = 0; t < NT; t++) {
    ldpiece<H>(Fn, L, a, t);
    mmtile<H>(Fc, acc[t], t);
#pragma unroll
    for (int k = 0; k < 4; k++) { SGB(0x8, 1); SGB(0x100, 1); }
    PIN4(acc[t]);
    SCHED();
    iss(t);
    if (t == NT - 2 && docs) colsum<H>(Fc, cs, ones);
    SCHED();
  }
}

DI int rot7(int s, int r) { const int k = s + r; return k >= 7 ? k - 7 : k; }
DI u32 p2_pair(int r, int spn) { return (2 * rot7(spn >> 1, r) + (spn & 1)) * 16384; }
constexpr int cunit(int m, int nn) { return m == 0 ? (nn == 0 ? 5 : nn == 3 ? 0 : 5 + nn) : 1 + nn; }

struct Sa {
  cf2 u[8];
  cf a1, a8, a16, a64, pq, lw, e0, c2, cw, wt, wg0, wg1;
};
struct Pw {
  u32 vwe, vwo, vce, vco, tl[4];
};
DI Pw pw_setup(int lane, int w, int xn) {
  Pw z;
  const int q = lane >> 4, r4 = (lane & 15) >> 2, pp = lane & 3;
  z.vwe = (lane >> 3) * 1024 + (((lane & 7) ^ (2 * (q & 1))) << 4);
  z.vwo = (z.vwe ^ 64) + 8192 - 1024;
  z.vce = (lane >> 3) * 1024 + (((lane & 7) ^ q) << 4);
  z.vco = (z.vce ^ 64) + 8192 - 1024;
#pragma unroll
  for (int nn = 0; nn < 4; nn++)
    z.tl[nn] = w * PW + (q & 1) * 1024 + r4 * 128 + ((((2 * (nn ^ xn)) ^ (2 * (((r4 >> 1) & 1) + 2 * (q & 1)))) + (pp >> 1)) << 4) + (pp & 1) * 8;
  return z;
}
DI void rdw(v4u (&wf)[4], LAS char* L, const Pw& z, int q, int ua, int ub) {
  const u32 o = (q >> 1) ? unitb(ub) : unitb(ua);
#pragma unroll
  for (int nn = 0; nn < 4; nn++) wf[nn] = tr2(L, o + z.tl[nn], 512);
}

#define HW 8
constexpr int HI0 = HW, HCHK = 28 - HW, HFL = HCHK - HW - 1;
constexpr int hiss(int m) { return m + 1 + HW < 44 ? m + 1 + HW : 44; }
constexpr int ncopy(int m) { return 0; }
constexpr int nother(int m) { return ncopy(m) + (m == HFL ? 1 : 0); }
constexpr int issued_upto(int m) {
  int n = hiss(m);
  for (int k = 0; k <= m; k++) n += nother(k);
  return n;
}
constexpr int pos_h(int m) {
  int n = m;
  for (int k = 0; k < 44; k++) if (hiss(k) <= m) n += nother(k);
  return n;
}
static_assert(hiss(HCHK - 1) == 28, "schedule");

DI void flag_read(v2u& v, srd_t rs, u32 l16, u32 so) {
  asm volatile("buffer_load_dwordx2 %0, %1, %2, %3 offen sc1" : "=&v"(v) : "v"((l16 & 0xF0) >> 1), "s"(rs), "s"(so) : "memory");
}
DI void poll_flags(srd_t rs, u32 l16, u32 so, u32 tagLo, u32 tagHi, int mask, v2u v) {
  const bool need = (mask >> ((l16 >> 4) & 3)) & 1;
#pragma unroll 1
  for (u32 spins = 0; !allok(!need || (v.x == tagLo && v.y == tagHi)) && spins < SPIN_CAP; spins++)
    asm volatile("s_sleep " PSLEEP "\n\tbuffer_load_dwordx2 %0, %1, %2, %3 offen sc1\n\ts_waitcnt vmcnt(0)"
                 : "=&v"(v)
                 : "v"((l16 & 0xF0) >> 1), "s"(rs), "s"(so)
                 : "memory");
  asm volatile("" ::: "memory");
}

DI void stage_offsets(int lane, int w, u32 (&vo)[5], u32& vs) {
#pragma unroll
  for (int e = 0; e < 4; e++) vo[e] = (32 * w + 8 * e + (lane >> 3)) * 1024 + (((lane & 7) ^ ((4 * w + e) & 7)) << 4) - e * 1024;
  const int gpos = (lane * 37) >> 8, gch = lane - gpos * 7;
  vo[4] = (8 * w + 4 * (gpos & 1) + (gpos >> 1)) * 3584 + gch * 16;
  vs = (8 * (w & 1) + (lane >> 3)) * 1024 + (((lane & 7) ^ ((4 * (w & 1) + (lane >> 4)) & 7)) << 4);
}

template <int H>
DI void phase1(const Args& p, LAS char* L, u32 lbase, srd_t rs, srd_t sx, srd_t s1, srd_t s3, srd_t sw2, srd_t scr, srd_t sb, int g, int j, int rot, int r7, int w,
               int lane, u32 tagLo, u32 tagHi, u32 pl0, u32 pl1, cf a1, u32 stampv) {
  constexpr int NT = H == 0 ? 5 : 4;
  u32 vo[5], vs;
  stage_offsets(lane, w, vo, vs);
  Sa sa;
  v4u wf[7][4];
  const int tg = w & 3, c = lane & 15, q = lane >> 4, r4 = c >> 2, pp = lane & 3;
  const u32 ld0 = lbase + w * 4096, ld4 = lbase + G_OFF + w * 896, ld5 = ld4 + G3D, lds_s = lbase + SM_OFF + w * 1024;
  auto op = [&](int i, u32 kk, u32 slot) {
    if (i == 0) glds<0>(vo[0], sx, kk * 128, ld0, slot);
    else if (i == 1) glds<1024>(vo[1], sx, kk * 128, ld0, slot);
    else if (i == 2) glds<2048>(vo[2], sx, kk * 128, ld0, slot);
    else if (i == 3) glds<3072>(vo[3], sx, kk * 128, ld0, slot);
    else if (i == 4) glds56(vo[4], s1, kk * (64 * 3584), ld4, slot);
    else if (i == 5) glds56(vo[4], s3, kk * (64 * 3584), ld5, slot);
    else if (H == 0) glds<0>(vs, sb, kk * 128, lds_s, slot);
  };
  if (H == 0) {
    sa.a1 = a1;
    sa.a8 = csq(csq(csq(sa.a1)));
    sa.a16 = csq(sa.a8);
    sa.a64 = csq(csq(sa.a16));
    asm volatile("" : "+v"(sa.a1.r), "+v"(sa.a1.i), "+v"(sa.a8.r), "+v"(sa.a8.i), "+v"(sa.a16.r), "+v"(sa.a16.i), "+v"(sa.a64.r), "+v"(sa.a64.i));
  }
  SCHED();
  op(6, (rot + 1) & 7, SLOT);
#pragma unroll
  for (int i = 0; i < 6; i++) op(i, (rot + 1) & 7, SLOT);
  const u32 xa0 = (64 * tg + 16 * r4 + 8 * (c & 1) + ((c >> 1) & 1)) * 128 + ((q ^ (2 * r4 + (c & 1))) << 4);
  const u32 sa0 = SM_OFF + c * 128 + ((q ^ ((c >> 1) & 7)) << 4);
  const u32 ga0 = G_OFF + q * 896 + r4 * 224 + 8 * pp;
  const u32 gm0 = ga0 + 96 + (pp >= 2 ? G3D - 16 : 0);
  auto ra = [&](u32 slot, int ks) { return Ra{slot + (xa0 ^ (ks * 64)), slot + (sa0 ^ (ks * 64)), slot + ga0 + ks * 3584, slot + gm0 + ks * 3584}; };
  f4 acc[NT][4], cs[NT];
#pragma unroll
  for (int t = 0; t < NT; t++) {
    cs[t] = f4{0, 0, 0, 0};
#pragma unroll
    for (int i = 0; i < 4; i++) acc[t][i] = f4{0, 0, 0, 0};
  }
  v4u ones = {0x3F803F80u, 0x3F803F80u, 0x3F803F80u, 0x3F803F80u};
  asm volatile("" : "+v"(ones));
  const int own = (tg + 2 * H) & 3;
  WAITV(7);
  BAR();
  STAMP(2);
  CSTAMP(14);
  Fr<H> F0, F1;
  {
    const Ra a = ra(0, 0);
#pragma unroll
    for (int t = 0; t < NT; t++) ldpiece<H>(F0, L, a, t);
    SCHED();
  }
  half<H>(F0, F1, L, ra(0, 1), acc, cs, ones, own == 0, [&](int t) {
    if (t == 0) op(6, (rot + 2) & 7, 2 * SLOT);
    if (t < 3) { op(2 * t, (rot + 2) & 7, 2 * SLOT); op(2 * t + 1, (rot + 2) & 7, 2 * SLOT); }
    if (H == 0) {
      if (t == 1) {
        const float lamr = bf2f(pl0), lami = bf2f(pl1);
        const float nr = sa.a1.r - 1.f, ni = sa.a1.i, den = 2.f / (lamr * lamr + lami * lami);
        sa.c2 = cf{(nr * lamr + ni * lami) * den, (ni * lamr - nr * lami) * den};
        asm volatile("" : "+v"(sa.c2.r), "+v"(sa.c2.i));
      }
    }
  });
  u32 cur = 0, nxt = SLOT;
  SCHED();
#pragma unroll 1
  for (int s = 0; s < 5; s++) {
    const Ra an0 = ra(nxt, 0);
    const u32 kk = (rot + s + 3) & 7;
    WAITL0();
    DSTAMP(16 + 3 * s);
    WAITV(6);
    DSTAMP(17 + 3 * s);
    BAR();
    DSTAMP(18 + 3 * s);
    SCHED();
    half<H>(F1, F0, L, an0, acc, cs, ones, ((2 * s + 1) & 3) == own, [&](int t) {
      if (H == 0) {
        if (t == 0) op(6, kk, cur);
        else if (t == 1) op(0, kk, cur);
        else if (t == 3) op(4, kk, cur);
        else if (t == 4) op(1, kk, cur);
      } else {
        if (t == 0 && s == 0 && tg < 3) {
          const int b = g * 32 + j;
          const char* src = tg == 0 ? (const char*)p.w2 + b * 7168 : (const char*)(tg == 1 ? p.cre : p.cim) + b * 2048;
          const u32 off = (tg == 0 ? (lane < 56 ? lane : 0) : (lane & 15)) * 128;
          asm volatile("s_mov_b32 m0, %2\n\ts_nop 0\n\tglobal_load_lds_dword %0, %1" ::"v"(off), "s"(src), "s"(lbase + DUMMY) : "memory", "m0");
        }
        if (t == 0) op(4, kk, cur);
        else if (t == 2) op(0, kk, cur);
        else if (t == 3) op(1, kk, cur);
      }
    });
    half<H>(F0, F1, L, ra(nxt, 1), acc, cs, ones, ((2 * s + 2) & 3) == own, [&](int t) {
      if (H == 0) {
        if (t == 0) op(2, kk, cur);
        else if (t == 2) op(5, kk, cur);
        else if (t == 3) op(3, kk, cur);
      } else {
        if (t == 0) op(5, kk, cur);
        else if (t == 1) op(2, kk, cur);
        else if (t == 2) op(3, kk, cur);
      }
    });
    cur = nxt;
    nxt = nxt == 2 * SLOT ? 0 : nxt + SLOT;
  }
  const Pw z = pw_setup(lane, w, g & 3);
  const u32 lw2 = lbase + w * PW;
  v4u gr;
#pragma unroll
  for (int s = 5; s < 7; s++) {
    WAITL0();
    DSTAMP(16 + 3 * s);
    WAITV(6);
    DSTAMP(17 + 3 * s);
    BAR();
    DSTAMP(18 + 3 * s);
    SCHED();
    half<H>(F1, F0, L, ra(((s + 1) % 3) * SLOT, 0), acc, cs, ones, ((2 * s + 1) & 3) == own, [&](int t) {
      if (H == 1 && t == 0) {
        if (s == 5)
          asm volatile("s_mov_b32 m0, %3\n\ts_nop 0\n\tbuffer_load_dwordx4 %0, %1, %2 offen sc1 lds" ::"v"(lane * 16), "s"(rs),
                       "s"(WS_SG + (g * 256 + 64 * tg) * 16), "s"(lbase + GR + tg * 1024)
                       : "memory", "m0");
        else gr = lds128(L, GR + tg * 1024 + lane * 16);
      }
      if (t == 1) { if (s == 5) glds2<unitb(0)>(z.vwe, z.vwo, sw2, p2_pair(r7, 0), lw2); else glds2<unitb(3)>(z.vwe, z.vwo, sw2, p2_pair(r7, 3), lw2); }
      if (H == 1 && t == 3 && s == 6) {
#pragma unroll 1
        for (u32 spins = 0; !allok(gr.y == tagLo && gr.w == tagHi) && spins < SPIN_CAP; spins++)
          asm volatile("s_sleep 1\n\tbuffer_load_dwordx4 %0, %1, %2, %3 offen sc1\n\ts_waitcnt vmcnt(0)"
                       : "=&v"(gr)
                       : "v"(lane * 16), "s"(rs), "s"(WS_SG + (g * 256 + 64 * tg) * 16)
                       : "memory");
        const u32 pos = GR + tg * 1024 + ((lane & 48) + 4 * ((lane >> 1) & 3) + 2 * (lane & 1) + ((lane >> 3) & 1)) * 4;
        *(LAS u32*)(L + pos) = gr.x;
        *(LAS u32*)(L + pos + 256) = gr.z;
      }
    });
    half<H>(F0, F1, L, ra(((s + 1) % 3) * SLOT, 1), acc, cs, ones, ((2 * s + 2) & 3) == own, [&](int t) {
      if (t == 0) { if (s == 5) glds2<unitb(1)>(z.vwe, z.vwo, sw2, p2_pair(r7, 1), lw2); else glds2<unitb(4)>(z.vwe, z.vwo, sw2, p2_pair(r7, 4), lw2); }
      if (t == 2) { if (s == 5) glds2<unitb(2)>(z.vwe, z.vwo, sw2, p2_pair(r7, 2), lw2); else glds2<unitb(5)>(z.vwe, z.vwo, sw2, p2_pair(r7, 5), lw2); }
    });
  }
  {
    const u32 cp = CSP + tg * 576;
    if (H == 0) {
      if (q == 0) { *(LAS float*)(L + cp + c * 4) = cs[0][0]; *(LAS float*)(L + cp + 64 + c * 4) = cs[1][0]; }
      if (c == 0) {
#pragma unroll
        for (int t = 2; t < 5; t++) *(LAS f4*)(L + cp + 64 * t + 16 * q) = cs[t];
      }
    } else if (c == 0) {
#pragma unroll
      for (int t = 0; t < 4; t++) *(LAS f4*)(L + cp + 320 + 64 * t + 16 * q) = cs[t];
    }
  }
  WAITL0();
  CSTAMP(37);
  BAR();
  CSTAMP(39);
  float mcol[4], rcol[4];
  f4 mrow[4], rrow[4], cp4[NT][4];
  {
#pragma unroll
    for (int t = 0; t < NT; t++) cs[t] = f4{0, 0, 0, 0};
    colsum<H>(F1, cs, ones);
    const u32 csa = CSP + (H == 0 ? 0 : 320) + 16 * q, cs1 = CSP + c * 4, gra = GR + tg * 1024 + (16 * r4 + (c & 3)) * 4, grr = GR + tg * 1024 + 64 * q;
#pragma unroll
    for (int t = 0; t < NT; t++) {
#pragma unroll
      for (int k = 0; k < 4; k++) {
        if (H == 0) { if (t < 2) cp4[t][k][0] = ldsf(L, cs1 + 64 * t + 576 * k); }
        else cp4[t][k] = ldsf4(L, csa + 64 * t + 576 * k);
      }
      if (t < 4) {
        if (H == 1) { mcol[t] = ldsf(L, gra + 16 * t); rcol[t] = ldsf(L, gra + 16 * t + 256); }
        else { mrow[t] = ldsf4(L, grr + 16 * t); rrow[t] = ldsf4(L, grr + 16 * t + 256); }
      }
      mmtile<H>(F1, acc[t], t);
#pragma unroll
      for (int k = 0; k < 4; k++) { SGB(0x8, 1); SGB(0x100, 2); }
      PIN4(acc[t]);
      SCHED();
      if (t == 0) glds2<unitb(6)>(z.vwe, z.vwo, sw2, p2_pair(r7, 6), lw2);
      if (t == 1) glds2<unitb(7)>(z.vwe, z.vwo, sw2, p2_pair(r7, 7), lw2);
      if (t == 2) glds2<unitb(8)>(z.vwe, z.vwo, sw2, p2_pair(r7, 8), lw2);
      if (t >= 1 && (H == 1 || t < 3)) {
        if (H == 0) cs[t - 1][0] += (cp4[t - 1][0][0] + cp4[t - 1][1][0]) + (cp4[t - 1][2][0] + cp4[t - 1][3][0]);
        else cs[t - 1] += (cp4[t - 1][0] + cp4[t - 1][1]) + (cp4[t - 1][2] + cp4[t - 1][3]);
        asm volatile("" : "+v"(cs[t - 1]));
      }
      SCHED();
    }
    if (H == 1) cs[NT - 1] += (cp4[NT - 1][0] + cp4[NT - 1][1]) + (cp4[NT - 1][2] + cp4[NT - 1][3]);
    else {
#pragma unroll
      for (int t = 2; t < 5; t++)
#pragma unroll
        for (int k = 0; k < 4; k++) cp4[t][k] = ldsf4(L, csa + 64 * t + 576 * k);
#pragma unroll
      for (int t = 0; t < 4; t++) { mcol[t] = ldsf(L, gra + 16 * t); rcol[t] = ldsf(L, gra + 16 * t + 256); }
      SCHED();
    }
  }
  STAMP(3);
  CSTAMP(15);
  __builtin_amdgcn_s_setprio(3);
  const int oct0 = 44 * (j >> 2) + 16 + 7 * (j & 3);
  const u32 hbs = WS_HB + (g * 4 + tg) * 4 * HB_TILE + oct0 * 256;
  auto r2 = [&](int k) {
    if (k == 0) { glds2<unitb(0)>(z.vwe, z.vwo, sw2, p2_pair(r7, 9), lw2); glds2<unitb(1)>(z.vwe, z.vwo, sw2, p2_pair(r7, 10), lw2); }
    if (k == 1) { glds2<unitb(2)>(z.vwe, z.vwo, sw2, p2_pair(r7, 11), lw2); glds2<unitb(3)>(z.vwe, z.vwo, sw2, p2_pair(r7, 12), lw2); }
    if (k == 2) { glds2<unitb(4)>(z.vwe, z.vwo, sw2, p2_pair(r7, 13), lw2); glds2<unitb(cunit(0, 0))>(z.vce, z.vco, scr, (g & 3) * 16384, lw2); }
    SCHED();
  };
  if (H == 1) {
    const u32 hv = c * 16 + (2 + (q >> 1) + 2 * (q & 1)) * 256;
#pragma unroll
    for (int i = 0; i < 4; i++) {
      const f2 nm = sp(-mcol[i]);
      const float nrl = rcol[i] * NL2E, ir2 = rcp(rcol[i] * rcol[i]);
      u32 v[4];
#pragma unroll
      for (int k = 0; k < 4; k++) {
        const int t = 2 * (k >> 1), h = k & 1;
        v[k] = swiglu2(fma2(nm, half2(cs[t], h), half2(acc[t][i], h)), fma2(nm, half2(cs[t + 1], h), half2(acc[t + 1][i], h)), nrl, ir2);
      }
      __builtin_amdgcn_raw_buffer_store_b128(swap16x2(v[0], v[1], v[2], v[3]), rs, hv + hbs + i * HB_TILE, 0, AUX_SC1);
      SCHED();
      if (i == 0) {
        CSTAMP(18);
        WAITL0();
        BAR();
        CSTAMP(19);
        WAITV(7);
        CSTAMP(16);
        rdw(wf[0], L, z, q, 0, 1); rdw(wf[1], L, z, q, 2, 3); rdw(wf[2], L, z, q, 4, 5);
        SCHED();
      } else {
        if (i == 1) WAITL0();
        if (i == 1) CSTAMP(21);
        if (i == 2) CSTAMP(23);
        if (i == 3) CSTAMP(20);
        r2(i - 1);
        if (i == 1) CSTAMP(22);
        if (i == 2) CSTAMP(24);
      }
    }
  } else {
#pragma unroll
    for (int k = 0; k < 8; k++) {
      const f2 m2 = half2(mrow[k >> 1], k & 1), r2v = half2(rrow[k >> 1], k & 1);
      sa.u[k].r = fma2(-m2, sp(cs[0][0]), half2(acc[0][k >> 1], k & 1)) * r2v;
      sa.u[k].i = fma2(-m2, sp(cs[1][0]), half2(acc[1][k >> 1], k & 1)) * r2v;
    }
    cf2 e2 = sa.u[0];
#pragma unroll
    for (int k = 1; k < 8; k++) e2 = cfma2(sa.a1, e2, sa.u[k]);
    {
      sa.e0 = cf{e2.r.x, e2.i.x};
      const cf e = cfma(sa.a8, sa.e0, cf{e2.r.y, e2.i.y});
      float er[4], ei[4];
      bcast4(e.r, er);
      bcast4(e.i, ei);
      const cf i0{er[0], ei[0]};
      const cf i1 = cfma(sa.a16, i0, cf{er[1], ei[1]});
      const cf i2 = cfma(sa.a16, i1, cf{er[2], ei[2]});
      const cf i3 = cfma(sa.a16, i2, cf{er[3], ei[3]});
      *(LAS v2u*)(L + XCH + (c * 4 + tg) * 8) = v2u{f2u(i3.r), f2u(i3.i)};
      sa.pq = csel(q & 2, csel(q & 1, i2, i1), csel(q & 1, i0, cf{0.f, 0.f}));
    }
    SCHED();
    CSTAMP(18);
    WAITL0();
    BAR();
    CSTAMP(19);
    {
      const f4 w01 = ldsf4(L, XCH + c * 32), w23 = ldsf4(L, XCH + c * 32 + 16);
      const cf p0{w01.x, w01.y};
      const cf p1 = cfma(sa.a64, p0, cf{w01.z, w01.w});
      const cf p2 = cfma(sa.a64, p1, cf{w23.x, w23.y});
      const cf p3 = cfma(sa.a64, p2, cf{w23.z, w23.w});
      const v4u o = {f2u(p3.r), tagLo, f2u(p3.i), tagHi};
      __builtin_amdgcn_raw_buffer_store_b128(o, rs, (q | tg) ? 0x7FFFFFF0 : WS_AG + (g * 512 + 16 * j + c) * 16, 0, AUX_SC1);
      sa.lw = csel(tg & 2, csel(tg & 1, p2, p1), csel(tg & 1, p0, cf{0.f, 0.f}));
    }
    SCHED();
    STAMP(8);
#pragma unroll
    for (int t = 2; t < 5; t++) {
      cs[t] += (cp4[t][0] + cp4[t][1]) + (cp4[t][2] + cp4[t][3]);
      asm volatile("" : "+v"(cs[t]));
    }
    WAITV(7);
    CSTAMP(16);
    rdw(wf[0], L, z, q, 0, 1); rdw(wf[1], L, z, q, 2, 3); rdw(wf[2], L, z, q, 4, 5);
    SCHED();
    u32 zx[2], zy[2];
#pragma unroll
    for (int ip = 0; ip < 2; ip++) {
      u32 px[2], py[2];
      f4 tm[2];
#pragma unroll
      for (int ii = 0; ii < 2; ii++) {
        const int i = 2 * ip + ii;
        const f2 nm = sp(-mcol[i]);
        const float nrl = rcol[i] * NL2E, ir2 = rcp(rcol[i] * rcol[i]);
        px[ii] = swiglu2(fma2(nm, cs[2].xy, acc[2][i].xy), fma2(nm, cs[3].xy, acc[3][i].xy), nrl, ir2);
        py[ii] = swiglu2(fma2(nm, cs[2].zw, acc[2][i].zw), fma2(nm, cs[3].zw, acc[3][i].zw), nrl, ir2);
        tm[ii].xy = fma2(nm, cs[4].xy, acc[4][i].xy);
        tm[ii].zw = fma2(nm, cs[4].zw, acc[4][i].zw);
      }
      __builtin_amdgcn_raw_buffer_store_b128(swap16x2(px[0], py[0], px[1], py[1]), rs, c * 16 + (q & 1) * HB_TILE + (q >> 1) * 256 + hbs + 2 * ip * HB_TILE, 0,
                                             AUX_SC1);
      SCHED();
      if (ip == 0) WAITL0();
      r2(ip);
      float rlo = rcol[2 * ip];
      asm volatile("" : "+v"(rlo));
      const float rsel = q < 2 ? rlo : rcol[2 * ip + 1];
      const float nrl = rsel * NL2E, ir2 = rcp(rsel * rsel);
      f4 gm, um;
#pragma unroll
      for (int r = 0; r < 4; r++) {
        u32 a = f2u(tm[0][r]), b = f2u(tm[1][r]);
        sw32(a, b);
        gm[r] = u2f(a);
        um[r] = u2f(b);
      }
      SWPAD2(gm, um);
      zx[ip] = swiglu2(gm.xy, um.xy, nrl, ir2);
      zy[ip] = swiglu2(gm.zw, um.zw, nrl, ir2);
    }
    __builtin_amdgcn_raw_buffer_store_b128(swap16x2(zx[0], zy[0], zx[1], zy[1]), rs, c * 16 + (2 * (q & 1) + (q >> 1)) * HB_TILE + hbs + 6 * 256, 0, AUX_SC1);
    SCHED();
    CSTAMP(20);
    r2(2);
  }
  STAMP(4);
  const int u = j >> 3, n = (j + NSH) & 7, xn = g & 3;
  const u64 tag = ((u64)tagHi << 32) | tagLo;
  GAS u64* flg = (GAS u64*)(p.ws + WS_FL) + ((g * 4 + tg) * 32 + j) * 4;
  const u32 wo = w * PW;
  const srd_t sci = mksrd((const char*)p.cim + (size_t)n * (64 * 1024) + w * 128, 64 * 1024 - w * 128);
  const int ti = w >> 1;
  v2u xv[2], dk;
  {
    const int dcol = 64 * n + 16 * (r4 ^ xn) + 4 * pp;
    u32 oo[2];
#pragma unroll
    for (int e = 0; e < 2; e++) {
      const int cc = 8 * (w & 1) + 4 * e + q;
      oo[e] = (64 * u + 16 * (cc >> 2) + 8 * (cc & 1) + 2 * ti + ((cc >> 1) & 1)) * 512 + dcol;
    }
    asm volatile("buffer_load_dwordx2 %0, %3, %5, 0 offen\n\tbuffer_load_dwordx2 %1, %4, %5, 0 offen\n\tglobal_load_dwordx2 %2, %6, %7"
                 : "=&v"(xv[0]), "=&v"(xv[1]), "=&v"(dk)
                 : "v"(oo[0] * 2), "v"(oo[1] * 2), "s"(sx), "v"(dcol * 2), "s"(p.dsk)
                 : "memory");
  }
  const u32 fso = WS_FL + ((g * 4 + u) * 32 + 4 * w) * 32;
  u32 l16 = lane * 16;
  v2u fv;
  auto rd36 = [&]() { rdw(wf[3], L, z, q, 6, 7); rdw(wf[4], L, z, q, 8, 0); rdw(wf[5], L, z, q, 1, 2); rdw(wf[6], L, z, q, 3, 4); SCHED(); };
  auto round3 = [&]() {
    WAITL0();
    glds2<unitb(cunit(0, 1))>(z.vce, z.vco, scr, (1 ^ xn) * 16384, lw2);
    glds2<unitb(cunit(0, 2))>(z.vce, z.vco, scr, (2 ^ xn) * 16384, lw2);
    glds2<unitb(cunit(0, 3))>(z.vce, z.vco, scr, (3 ^ xn) * 16384, lw2);
    glds2<unitb(cunit(1, 0))>(z.vce, z.vco, sci, (0 ^ xn) * 16384, lw2);
    glds2<unitb(cunit(1, 1))>(z.vce, z.vco, sci, (1 ^ xn) * 16384, lw2);
    flag_read(fv, rs, l16, fso);
    glds2<unitb(cunit(1, 2))>(z.vce, z.vco, sci, (2 ^ xn) * 16384, lw2);
    glds2<unitb(cunit(1, 3))>(z.vce, z.vco, sci, (3 ^ xn) * 16384, lw2);
    CSTAMP(28);
    asm volatile("s_waitcnt vmcnt(4)" : "+v"(fv)::"memory");
  };
  if (H == 0) {
    const u32 ao = WS_AG + (q * 512 + 16 * j + c) * 16;
    v4u g0, g1;
    asm volatile("buffer_load_dwordx4 %0, %2, %3, 0 offen sc1\n\tbuffer_load_dwordx4 %1, %2, %3, %4 offen sc1"
                 : "=&v"(g0), "=&v"(g1)
                 : "v"(ao), "s"(rs), "s"(4 * 512 * 16)
                 : "memory");
    WAITV(9);
    if (lane == 0) __hip_atomic_store(flg, tag, RLX_AGENT);
    STAMP(5);
    WAITV(6);
    rd36();
    const cf one{1.f, 0.f}, zero{0.f, 0.f};
    asm volatile("" : "+v"(sa.a16.r), "+v"(sa.a16.i), "+v"(sa.a64.r), "+v"(sa.a64.i));
    {
      const cf a128 = csq(sa.a64), a256 = csq(a128), a512 = csq(a256), a1024 = csq(a512);
      sa.cw = cmul(sa.c2, cmul(csel(q & 1, sa.a16, one), csel(q & 2, csq(sa.a16), one)));
      sa.wt = cmul(sa.cw, cmul(csel(tg & 1, sa.a64, one), csel(tg & 2, a128, one)));
      const int e0 = g - 1 - q, e1 = g - 5 - q;
      sa.wg1 = cmul(csel(e0 & 1, a256, one), csel(e0 & 2, a512, one));
      sa.wg0 = csel(e0 >= 0, cmul(sa.wg1, csel(e0 & 4, a1024, one)), zero);
      sa.wg1 = csel(e1 >= 0, sa.wg1, zero);
      asm volatile("" : "+v"(sa.wg0.r), "+v"(sa.wg0.i), "+v"(sa.wg1.r), "+v"(sa.wg1.i));
    }
    const cf wt8 = cmul(sa.wt, sa.a8);
    const cf k0 = cfma(sa.cw, sa.lw, cmul(sa.c2, sa.pq));
    const cf k1 = cfma(sa.a8, k0, cmul(sa.c2, sa.e0));
    cf2 h = cf2{f2{k0.r, k1.r}, f2{k0.i, k1.i}}, wt2 = cf2{f2{sa.wt.r, wt8.r}, f2{sa.wt.i, wt8.i}};
#pragma unroll
    for (int k = 0; k < 8; k++) {
      sa.u[k] = cmul2(sa.c2, sa.u[k]);
      asm volatile("" : "+v"(sa.u[k].r), "+v"(sa.u[k].i));
    }
    asm volatile("" : "+v"(h.r), "+v"(h.i), "+v"(wt2.r), "+v"(wt2.i));
    CSTAMP(25);
    asm volatile("s_waitcnt vmcnt(1)" : "+v"(g0), "+v"(g1), "+v"(xv[0]), "+v"(xv[1]), "+v"(dk)::"memory");
#pragma unroll 1
    for (u32 spins = 0; spins < SPIN_CAP; spins++) {
      if (allok((q >= g || (g0.y == tagLo && g0.w == tagHi)) && (q + 4 >= g || (g1.y == tagLo && g1.w == tagHi)))) break;
      asm volatile("s_sleep 1\n\tbuffer_load_dwordx4 %0, %2, %3, 0 offen sc1\n\tbuffer_load_dwordx4 %1, %2, %3, %4 offen sc1\n\ts_waitcnt vmcnt(0)"
                   : "=&v"(g0), "=&v"(g1)
                   : "v"(ao), "s"(rs), "s"(4 * 512 * 16)
                   : "memory");
    }
    {
      cf hin = cfma(sa.wg0, csel(q < g, cf{u2f(g0.x), u2f(g0.z)}, zero), cmul(sa.wg1, csel(q + 4 < g, cf{u2f(g1.x), u2f(g1.z)}, zero)));
      hin.r = sumx32(sumx16(hin.r));
      hin.i = sumx32(sumx16(hin.i));
      h = cfma2(hin, wt2, h);
    }
    STAMP(6);
    const u32 tb = TRB + tg * 1280;
    const u32 twr = tb + c * 40 + q * 8;
    const u32 trd = tb + (q >> 1) * 640 + (8 * (q & 1) + r4) * 40 + pp * 8;
    v4u o[4];
#pragma unroll
    for (int i = 0; i < 4; i++) {
      const cf2 ha = cfma2(sa.a1, h, sa.u[2 * i]);
      h = cfma2(sa.a1, ha, sa.u[2 * i + 1]);
      *(LAS v2u*)(L + twr) = v2u{pk2(ha.r), pk2(h.r)};
      *(LAS v2u*)(L + twr + 640) = v2u{pk2(-ha.i), pk2(-h.i)};
      asm volatile("" ::: "memory");
      o[i] = tr2(L, trd, 160);
      asm volatile("" ::: "memory");
    }
#pragma unroll
    for (int i = 0; i < 4; i++)
      __builtin_amdgcn_raw_buffer_store_b128(o[i], rs, lane * 16 + WS_HB + ((g * 4 + tg) * 4 + i) * HB_TILE + (11 * (j >> 2) + (j & 3)) * 1024, 0, AUX_SC1);
    CSTAMP(26);
    __builtin_amdgcn_s_setprio(1);
    round3();
    __builtin_amdgcn_raw_buffer_store_b64(v2u{tagLo, tagHi}, rs, l16 ? 0x7FFFFFF0 : WS_FL + (((g * 4 + tg) * 32 + j) * 4 + 2) * 8, 0, AUX_SC1);
  } else {
    WAITV(7);
    if (lane == 0) __hip_atomic_store(flg + 1, tag, RLX_AGENT);
    STAMP(5);
    __builtin_amdgcn_s_setprio(0);
    asm volatile("s_waitcnt vmcnt(1)" : "+v"(xv[0]), "+v"(xv[1]), "+v"(dk)::"memory");
    rd36();
    CSTAMP(26);
    round3();
  }
  STAMP(7);

  poll_flags(rs, l16, fso, tagLo, tagHi, 3, fv);
  STAMP(9);
  const u32 hs = WS_HB + (g * 4 + u) * 4 * HB_TILE + 11 * w * 1024;
  f4 ac2[4][4];
#pragma unroll
  for (int i = 0; i < 4; i++)
#pragma unroll
    for (int nn = 0; nn < 4; nn++) ac2[i][nn] = f4{0, 0, 0, 0};
  v4u hf[44];
  auto ldh = [&](int m) {
    const int i = m < 28 ? (m & 3) : ((m - 28) >> 2);
    const u32 so = hs + i * HB_TILE + (m < 28 ? 4 + rot7(m >> 2, r7) : ((m - 28) & 3)) * 1024;
    asm volatile("buffer_load_dwordx4 %0, %1, %2, %3 offen sc1" : "=v"(hf[m]) : "v"(l16), "s"(rs), "s"(so) : "memory");
  };
  u32 ub[4];
  int c2_ = 0, q2 = 0;
  auto ldc = [&](int s) {
    const u32 ca = ((2 * s + (q2 & 1)) ^ ((c2_ >> 1) & 7)) << 4;
#pragma unroll
    for (int nn = 0; nn < 4; nn++) wf[s][nn] = lds128(L, ub[nn] + ca);
  };
  __builtin_amdgcn_s_setprio(0);
#pragma unroll
  for (int m = 0; m < HI0; m++) ldh(m);
  CSTAMP(29);
  f4 sk[2];
  static_for<0, 44>([&](auto M) {
    constexpr int m = M.value;
    constexpr int i = m < 28 ? (m & 3) : ((m - 28) >> 2), ks = m < 28 ? (m >> 2) : ((m - 28) & 3);
    constexpr int nw = issued_upto(m - 1) - pos_h(m) - 1;
    static_assert(nw >= 0 && nw < 64, "vmcnt");
    asm volatile("s_waitcnt vmcnt(%1)" : "+v"(hf[m]) : "n"(nw) : "memory");
#pragma unroll
    for (int nn = 0; nn < 4; nn++) ac2[i][nn] = mfma(wf[ks][nn], hf[m], ac2[i][nn]);
    PIN4(ac2[i]);
    SCHED();
    if (m == HCHK) {
      STAMP(10);
      constexpr int nf = issued_upto(HCHK - 1) - issued_upto(HFL);
      asm volatile("s_waitcnt vmcnt(%1)" : "+v"(fv) : "n"(nf) : "memory");
      poll_flags(rs, l16, fso, tagLo, tagHi, 4, fv);
      STAMP(11);
    }
#pragma unroll
    for (int k = hiss(m - 1); k < hiss(m); k++) ldh(k);
    if (m == HFL) flag_read(fv, rs, l16, fso);
    if (m == 17) {
      asm volatile("" : "+v"(l16));
      c2_ = (l16 >> 4) & 15;
      q2 = l16 >> 8;
#pragma unroll
      for (int nn = 0; nn < 4; nn++) ub[nn] = wo + c2_ * 128 + ((q2 >> 1) ? unitb(cunit(1, nn)) : unitb(cunit(0, nn)));
    }
    if (m == 19) ldc(0);
    if (m == 21) ldc(1);
    if (m == 23) ldc(2);
    if (m == 25) ldc(3);
    if (m == 33) {
#pragma unroll
      for (int e = 0; e < 2; e++) {
        const int cc = 8 * (w & 1) + 4 * e + q2;
        const u32 sp_ = GR + u * 1024 + (16 * (cc >> 2) + 4 * ti + (cc & 3)) * 4;
        const float mm = ldsf(L, sp_), r = ldsf(L, sp_ + 256);
        const float xs[4] = {bf2f(xv[e].x), bfhi(xv[e].x), bf2f(xv[e].y), bfhi(xv[e].y)};
        const float ds[4] = {bf2f(dk.x), bfhi(dk.x), bf2f(dk.y), bfhi(dk.y)};
#pragma unroll
        for (int k = 0; k < 4; k++) sk[e][k] = xs[k] + rbf(rbf(rbf(xs[k] - mm) * r) * ds[k]);
        asm volatile("" : "+v"(sk[e]));
      }
    }
    if (m >= 28 && (m & 3) == 3) {
#pragma unroll
      for (int nn = 0; nn < 4; nn++)
        *(LAS f4*)(L + wo + unitb(2 * i + (nn >> 1)) + (nn & 1) * 1024 + q2 * 256 + ((c2_ ^ (4 * nn + q2)) << 4)) = ac2[i][nn];
    }
    SCHED();
  });
  STAMP(12);
  WAITL0();
  CSTAMP(33);
  BAR();
  CSTAMP(34);
  const int v0 = 2 * ti, v1 = v0 + 1;
  const u32 u0 = ((((v0 * 11) >> 5) + 2) % 3) * SLOT + (v0 % 3) * 2048, u1 = ((((v1 * 11) >> 5) + 2) % 3) * SLOT + (v1 % 3) * 2048;
  const srd_t so = mksrd(p.out + (size_t)g * (256 * 512), 256 * 512 * 4);
#pragma unroll
  for (int e = 0; e < 2; e++) {
    const int cc = 8 * (w & 1) + 4 * e + q2;
    const u32 rda = ((c2_ >> 3) ? u1 : u0) + ((c2_ >> 2) & 1) * 1024 + (c2_ & 3) * 256 + ((cc ^ c2_) << 4);
    f4 pv[8];
#pragma unroll
    for (int v = 0; v < 8; v++) pv[v] = ldsf4(L, v * PW + rda);
    const f4 s = (((pv[0] + pv[1]) + (pv[2] + pv[3])) + ((pv[4] + pv[5]) + (pv[6] + pv[7]))) + sk[e];
    const u32 o = (64 * u + 16 * (cc >> 2) + 8 * (cc & 1) + 2 * ti + ((cc >> 1) & 1)) * 512 + 64 * n + 16 * ((c2_ >> 2) ^ xn) + 4 * (c2_ & 3);
    __builtin_amdgcn_raw_buffer_store_b128(__builtin_bit_cast(v4u, s), so, o * 4, 0, AUX_SC1);
  }
  if (H == 0 && g == 7 && j == 0 && w == 0 && l16 == 0) __hip_atomic_store((GAS u64*)(p.ws + WS_EP), tag, RLX_AGENT);
  STAMP(13);
}

}

__global__ __launch_bounds__(512, 2) void s5_glu_fused(Args p) {
  __shared__ __attribute__((aligned(256))) char smem[LDS_BYTES];
  LAS char* L = (LAS char*)smem;
  const u32 lbase = (u32)(size_t)L;
  int lane = threadIdx.x & 63;
  const int w = __builtin_amdgcn_readfirstlane(threadIdx.x >> 6);
  const int b = blockIdx.x, xcd = b & 7, idx = b >> 3;
  const int g = (xcd & 1) * 4 + (idx & 3), j = (xcd >> 1) * 8 + (idx >> 2);
  const int rot = ROT == 0 ? ((idx >> 2) + 2 * (idx & 3)) & 7 : ROT == 1 ? 0 : ROT == 2 ? 4 * (((idx >> 2) + idx) & 1) : 2 * (((idx >> 2) + idx) & 3);
  u32 stampv = 0;
  STAMP(0);
  const srd_t sx = mksrd((const char*)p.x + (size_t)g * (256 * 1024), 256 * 1024);
  v2u ep;
  u32 pl0, pl1, pl2;
  v4u xr0, xr1;
  {
    const u32 zero = 0, o = (16 * j + (lane & 15)) * 2;
    asm volatile("global_load_dwordx2 %0, %1, %2 sc1" : "=v"(ep) : "v"(zero), "s"(p.ws + WS_EP) : "memory");
    asm volatile("global_load_ushort %0, %1, %2" : "=v"(pl0) : "v"(o), "s"(p.lre) : "memory");
    asm volatile("global_load_ushort %0, %1, %2" : "=v"(pl1) : "v"(o), "s"(p.lim) : "memory");
    asm volatile("global_load_ushort %0, %1, %2" : "=v"(pl2) : "v"(o), "s"(p.ldt) : "memory");
    const u32 ro = (lane >> 5) * 1024 + (lane & 31) * 16;
    const u32 so = w < 4 ? 0x7FFF0000u : (8 * j + 2 * (w & 3)) * 1024;
    asm volatile("buffer_load_dwordx4 %0, %1, %2, %3 offen" : "=v"(xr0) : "v"(ro), "s"(sx), "s"(so) : "memory");
    asm volatile("buffer_load_dwordx4 %0, %1, %2, %3 offen offset:512" : "=v"(xr1) : "v"(ro), "s"(sx), "s"(so) : "memory");
  }
  u32 vo[5], vs;
  stage_offsets(lane, w, vo, vs);
  const srd_t sb = mksrd((const char*)((w & 2) ? p.bim : p.bre) + (size_t)j * (16 * 1024), 16 * 1024);
  if (w < 4) glds<0>(vs, sb, rot * 128, lbase + SM_OFF + w * 1024, 0);
  glds<0>(vo[0], sx, rot * 128, lbase + w * 4096, 0);
  glds<1024>(vo[1], sx, rot * 128, lbase + w * 4096, 0);
  glds<2048>(vo[2], sx, rot * 128, lbase + w * 4096, 0);
  glds<3072>(vo[3], sx, rot * 128, lbase + w * 4096, 0);
  const srd_t s1 = mksrd((const char*)p.w1 + j * 112, 512 * 3584 - j * 112), s3 = mksrd((const char*)p.w3 + j * 112, 512 * 3584 - j * 112);
  glds56(vo[4], s1, rot * (64 * 3584), lbase + G_OFF + w * 896, 0);
  glds56(vo[4], s3, rot * (64 * 3584), lbase + G_OFF + w * 896, G3D);
  SCHED();
  const int n = (j + NSH) & 7, xn = g & 3, r7 = R7M == 0 ? rot7(n == 7 ? 0 : n, 2 * xn) : R7M == 1 ? (n == 7 ? 0 : n) : 0;
  const srd_t rs = mksrd(p.ws, WS_BYTES);
  const srd_t sw2 = mksrd((const char*)p.w2 + (size_t)w * (224 * 1024) + n * 128, 224 * 1024 - n * 128);
  const srd_t scr = mksrd((const char*)p.cre + (size_t)n * (64 * 1024) + w * 128, 64 * 1024 - w * 128);
  asm volatile("s_waitcnt vmcnt(6)" : "+v"(ep), "+v"(pl0), "+v"(pl1), "+v"(pl2), "+v"(xr0), "+v"(xr1)::"memory");
  const u64 tag = (((u64)rfl(ep.y) << 32) | rfl(ep.x)) + 1;
  const u32 tagLo = (u32)tag, tagHi = (u32)(tag >> 32);
  STAMP(1);
  cf a1{0.f, 0.f};
  if (w < 4) {
    const float dt = rbf(expf(bf2f(pl2)));
    float sn, cn;
    sincosf(dt * bf2f(pl1), &sn, &cn);
    const float e = expf(dt * bf2f(pl0));
    a1 = cf{e * cn, e * sn};
  } else {
    f2 v[8];
#pragma unroll
    for (int k = 0; k < 4; k++) { v[k] = f2{bf2f(xr0[k]), bfhi(xr0[k])}; v[4 + k] = f2{bf2f(xr1[k]), bfhi(xr1[k])}; }
    const f2 s = ((v[0] + v[1]) + (v[2] + v[3])) + ((v[4] + v[5]) + (v[6] + v[7]));
    const float m = sumx16(sum16(s.x + s.y)) * (1.f / 512.f);
    f2 s2 = {0.f, 0.f};
#pragma unroll
    for (int k = 0; k < 8; k++) { const f2 d = v[k] - sp(m); s2 = fma2(d, d, s2); }
    const float var = sumx16(sum16(s2.x + s2.y)) * (1.f / 512.f);
    const float sd = rbf(__builtin_amdgcn_sqrtf(rbf(rbf(var) + 1e-5f)));
    const v4u o = {f2u(rbf(m)), tagLo, f2u(rcp(sd)), tagHi};
    __builtin_amdgcn_raw_buffer_store_b128(o, rs, (lane & 31) ? 0x7FFFFFF0 : WS_SG + (g * 256 + 8 * j + 2 * (w & 3) + (lane >> 5)) * 16, 0, AUX_SC1);
  }
  asm volatile("" : "+v"(a1.r), "+v"(a1.i));
  int wb = w;
  asm volatile("" : "+s"(wb));
  if (wb >= 4) {
    asm volatile("" : "+v"(lane));
    phase1<1>(p, L, lbase, rs, sx, s1, s3, sw2, scr, sb, g, j, rot, r7, w, lane, tagLo, tagHi, 0, 0, cf{0.f, 0.f}, stampv);
    __builtin_amdgcn_endpgm();
  }
  phase1<0>(p, L, lbase, rs, sx, s1, s3, sw2, scr, sb, g, j, rot, r7, w, lane, tagLo, tagHi, pl0, pl1, a1, stampv);
}

extern "C" void kernel_launch(void* const* d_in, const int* in_sizes, int n_in, void* d_out, int out_size, void* d_ws, size_t ws_size,
                              hipStream_t stream) {
  Args p;
  p.x = (const u16*)d_in[0];
  p.lre = (const u16*)d_in[1];
  p.lim = (const u16*)d_in[2];
  p.bre = (const u16*)d_in[3];
  p.bim = (const u16*)d_in[4];
  p.cre = (const u16*)d_in[5];
  p.cim = (const u16*)d_in[6];
  p.ldt = (const u16*)d_in[7];
  p.dsk = (const u16*)d_in[8];
  p.w1 = (const u16*)d_in[10];
  p.w3 = (const u16*)d_in[11];
  p.w2 = (const u16*)d_in[12];
  p.out = (float*)d_out;
  p.ws = (unsigned char*)d_ws;
  s5_glu_fused<<<256, 512, 0, stream>>>(p);
}
```

```cpp
#include <hip/hip_runtime.h>
#include <stdint.h>

typedef unsigned short u16;
typedef unsigned int u32;
typedef unsigned long long u64;
typedef u32 v4u __attribute__((ext_vector_type(4)));
typedef u32 v2u __attribute__((ext_vector_type(2)));
typedef float f4 __attribute__((ext_vector_type(4)));
typedef float f2 __attribute__((ext_vector_type(2)));
typedef short v4s __attribute__((ext_vector_type(4)));
typedef __bf16 v8bf __attribute__((ext_vector_type(8)));
typedef __bf16 v2bf __attribute__((ext_vector_type(2)));
typedef __amdgpu_buffer_rsrc_t srd_t;
#define LAS __attribute__((address_space(3)))
#define GAS __attribute__((address_space(1)))
#define DI __device__ __forceinline__
#define RLX_AGENT __ATOMIC_RELAXED, __HIP_MEMORY_SCOPE_AGENT

#define STAMPS 0
#define WARM 1
#define HAUX 16
#define R7M 0
#define DST 0
#define PSLEEP "4"
#define NSH 0
#define PRE 0
#define TLBW 0
#define CODEPF 0
#define PST 0
#define BSPLIT 1
#define ROT 3

namespace {
constexpr int SM_OFF = 32768, G_OFF = 36864, G3D = 7184, SLOT = 51456, AUXO = 3 * SLOT;
constexpr int GR = AUXO, CST = AUXO + 2048, TRB = AUXO + 4096, XCH = 49152, DUMMY = AUXO + 9216;
constexpr int LDS_BYTES = 163840;
constexpr int PW = 6144;
constexpr int unitb(int v) { return ((v / 3 + 2) % 3) * SLOT + (v % 3) * 2048; }
#define SH_FL 0
#define SH_AG 0
#define SH_HB 0
constexpr int WS_EP = 0, WS_FL = 4096 + SH_FL, WS_AG = 65536 + SH_AG, WS_SG = 196608, WS_CS = 262144, WS_HB = (1 << 20) + SH_HB, HB_TILE = 88 * 1024;
constexpr int WS_BYTES = WS_HB + 128 * HB_TILE;
constexpr int WS_STAMP = 32 << 20, NSTAMP = 14;
constexpr int AUX_SC1 = 16;
constexpr u32 SPIN_CAP = 1u << 17;
constexpr float NL2E = -1.4426950408889634f;

DI float bf2f(u32 b) { return __builtin_bit_cast(float, b << 16); }
DI float bfhi(u32 b) { return __builtin_bit_cast(float, b & 0xFFFF0000u); }
DI float u2f(u32 b) { return __builtin_bit_cast(float, b); }
DI u32 f2u(float f) { return __builtin_bit_cast(u32, f); }
DI float rbf(float f) { return (float)(__bf16)f; }
DI u32 pk2(f2 a) { return __builtin_bit_cast(u32, __builtin_convertvector(a, v2bf)); }
DI float rcp(float x) { return __builtin_amdgcn_rcpf(x); }
DI float ffma(float a, float b, float c) { return __builtin_fmaf(a, b, c); }
DI f2 fma2(f2 a, f2 b, f2 c) { return __builtin_elementwise_fma(a, b, c); }
DI f2 sp(float a) { return f2{a, a}; }
DI f2 half2(f4 v, int h) { return h ? v.zw : v.xy; }
DI f4 mfma(v4u a, v4u b, f4 c) {
  return __builtin_amdgcn_mfma_f32_16x16x32_bf16(__builtin_bit_cast(v8bf, a), __builtin_bit_cast(v8bf, b), c, 0, 0, 0);
}
DI v4u lds128(LAS char* L, u32 a) { return *(LAS v4u*)(L + a); }
DI f4 ldsf4(LAS char* L, u32 a) { return *(LAS f4*)(L + a); }
DI float ldsf(LAS char* L, u32 a) { return *(LAS float*)(L + a); }
DI v4u tr2(LAS char* L, u32 a, int d) {
  v2u lo = __builtin_bit_cast(v2u, __builtin_amdgcn_ds_read_tr16_b64_v4i16((LAS v4s*)(L + a)));
  v2u hi = __builtin_bit_cast(v2u, __builtin_amdgcn_ds_read_tr16_b64_v4i16((LAS v4s*)(L + a + d)));
  return v4u{lo.x, lo.y, hi.x, hi.y};
}
DI srd_t mksrd(const void* p, int bytes) { return __builtin_amdgcn_make_buffer_rsrc((void*)p, 0, bytes, 0x00020000); }
DI u32 rfl(u32 x) { return __builtin_amdgcn_readfirstlane(x); }
#pragma clang diagnostic ignored "-Winline-asm"
template <int OFF> DI void glds(u32 voff, srd_t srd, u32 soff, u32 la, u32 lb) {
  asm volatile("s_add_u32 m0, %3, %4\n\ts_nop 0\n\tbuffer_load_dwordx4 %0, %1, %2 offen offset:%5 lds" ::"v"(voff), "s"(srd), "s"(soff), "s"(la), "s"(lb),
               "n"(OFF)
               : "memory", "m0", "scc");
}
DI void glds56(u32 voff, srd_t srd, u32 soff, u32 la, u32 lb) {
  asm volatile("s_add_u32 m0, %3, %4\n\ts_mov_b32 exec_hi, 0xffffff\n\tbuffer_load_dwordx4 %0, %1, %2 offen lds\n\ts_mov_b32 exec_hi, -1" ::"v"(voff),
               "s"(srd), "s"(soff), "s"(la), "s"(lb)
               : "memory", "m0", "scc");
}
template <int LB> DI void glds2(u32 ve, u32 vo, srd_t srd, u32 soff, u32 la) {
  asm volatile("s_add_u32 m0, %4, %5\n\ts_nop 0\n\tbuffer_load_dwordx4 %0, %2, %3 offen lds\n\tbuffer_load_dwordx4 %1, %2, %3 offen offset:1024 lds" ::"v"(ve),
               "v"(vo), "s"(srd), "s"(soff), "s"(rfl(la)), "n"(LB)
               : "memory", "m0", "scc");
}
#define WAITV(n) asm volatile("s_waitcnt vmcnt(" #n ")" ::: "memory")
#define WAITL0() do { asm volatile("" ::: "memory"); __builtin_amdgcn_s_waitcnt(0xC07F); asm volatile("" ::: "memory"); } while (0)
#define BAR() __builtin_amdgcn_s_barrier()
#define SCHED() __builtin_amdgcn_sched_barrier(0)
#define SGB(m, n) __builtin_amdgcn_sched_group_barrier(m, n, 0)
#define PIN4(a) asm volatile("" : "+v"((a)[0]), "+v"((a)[1]), "+v"((a)[2]), "+v"((a)[3]))

template <int V> struct IC { static constexpr int value = V; };
template <int B, int E, class F> DI void static_for(F f) {
  if constexpr (B < E) {
    f(IC<B>{});
    static_for<B + 1, E>(f);
  }
}
DI bool allok(bool c) { return __builtin_amdgcn_ballot_w64(!c) == 0; }
struct cf { float r, i; };
DI float afma(float a, float b, float c) { float r; asm("v_fma_f32 %0, %1, %2, %3" : "=v"(r) : "v"(a), "v"(b), "v"(c)); return r; }
DI float anfma(float a, float b, float c) { float r; asm("v_fma_f32 %0, -%1, %2, %3" : "=v"(r) : "v"(a), "v"(b), "v"(c)); return r; }
DI cf cmul(cf a, cf b) { return cf{anfma(a.i, b.i, a.r * b.r), ffma(a.r, b.i, a.i * b.r)}; }
DI cf cfma(cf a, cf b, cf c) { return cf{afma(a.r, b.r, anfma(a.i, b.i, c.r)), ffma(a.r, b.i, ffma(a.i, b.r, c.i))}; }
DI cf csq(cf a) { return cf{anfma(a.i, a.i, a.r * a.r), 2.f * a.r * a.i}; }
DI cf csel(bool p, cf a, cf b) { return cf{p ? a.r : b.r, p ? a.i : b.i}; }
struct cf2 { f2 r, i; };
DI cf2 cfma2(cf a, cf2 b, cf2 c) { return cf2{fma2(sp(a.r), b.r, fma2(sp(-a.i), b.i, c.r)), fma2(sp(a.r), b.i, fma2(sp(a.i), b.r, c.i))}; }
DI cf2 cmul2(cf a, cf2 b) { return cf2{fma2(sp(a.r), b.r, sp(-a.i) * b.i), fma2(sp(a.r), b.i, sp(a.i) * b.r)}; }
template <int CTRL> DI float dpp(float x) { return u2f(__builtin_amdgcn_mov_dpp(f2u(x), CTRL, 0xf, 0xf, true)); }
DI float sum16(float v) {
  v += dpp<0xB1>(v); v += dpp<0x4E>(v); v += dpp<0x141>(v); v += dpp<0x140>(v);
  return v;
}
DI void sw16(u32& a, u32& b) { auto r = __builtin_amdgcn_permlane16_swap(a, b, false, false); a = r[0]; b = r[1]; }
DI void sw32(u32& a, u32& b) { auto r = __builtin_amdgcn_permlane32_swap(a, b, false, false); a = r[0]; b = r[1]; }
#define SWPAD2(a, b) asm volatile("s_nop 1" : "+v"(a), "+v"(b))
#define SWPAD4(a, b, c, d) asm volatile("s_nop 1" : "+v"(a), "+v"(b), "+v"(c), "+v"(d))
DI float sumx16(float v) { u32 a = f2u(v), b = a; sw16(a, b); SWPAD2(a, b); return u2f(a) + u2f(b); }
DI float sumx32(float v) { u32 a = f2u(v), b = a; sw32(a, b); SWPAD2(a, b); return u2f(a) + u2f(b); }
DI void bcast4(float v, float (&o)[4]) {
  u32 a = f2u(v), b = a;
  sw16(a, b);
  SWPAD2(a, b);
  u32 a2 = a, b2 = b;
  sw32(a, a2);
  sw32(b, b2);
  SWPAD4(a, b, a2, b2);
  o[0] = u2f(a); o[1] = u2f(b); o[2] = u2f(a2); o[3] = u2f(b2);
}
DI v4u swap16x2(u32 ax, u32 ay, u32 bx, u32 by) {
  sw16(ax, bx);
  sw16(ay, by);
  v4u o = {ax, ay, bx, by};
  asm volatile("s_nop 1" : "+v"(o));
  return o;
}

struct Args {
  const u16 *x, *lre, *lim, *bre, *bim, *cre, *cim, *ldt, *dsk, *w1, *w3, *w2;
  float* out;
  unsigned char* ws;
};

#define STAMP(k) do { } while (0)
#define CSTAMP(k) do { } while (0)
#define DSTAMP(k) do { } while (0)
#define PSTAMP(k) do { } while (0)

DI u32 swiglu2(f2 tg, f2 tu, float nrl, float ir2) {
  const f2 a = tg * sp(nrl);
  const f2 e = {__builtin_amdgcn_exp2f(a.x), __builtin_amdgcn_exp2f(a.y)};
  const f2 d = fma2(e, sp(ir2), sp(ir2));
  const f2 r = {rcp(d.x), rcp(d.y)};
  return pk2(tg * tu * r);
}

template <int H> struct Fr { v4u x[4]; v4u w[H == 0 ? 5 : 4]; };
struct Ra { u32 xa, sa, ga, gm; };

template <int H> DI void ldpiece(Fr<H>& F, LAS char* L, const Ra& a, int k) {
  if (k == 0) { F.x[0] = lds128(L, a.xa); F.x[1] = lds128(L, a.xa + 256); F.x[2] = lds128(L, a.xa + 512); }
  if (H == 0) {
    if (k == 1) { F.x[3] = lds128(L, a.xa + 768); F.w[0] = lds128(L, a.sa); F.w[1] = lds128(L, a.sa + 2048); }
    if (k == 2) { F.w[2] = tr2(L, a.ga, 112); F.w[3] = tr2(L, a.ga + G3D, 112); }
    if (k == 3) F.w[4] = tr2(L, a.gm, 112);
  } else {
    if (k == 0) F.x[3] = lds128(L, a.xa + 768);
    if (k == 1) { F.w[0] = tr2(L, a.ga + 32, 112); F.w[1] = tr2(L, a.ga + G3D + 32, 112); }
    if (k == 2) { F.w[2] = tr2(L, a.ga + 64, 112); F.w[3] = tr2(L, a.ga + G3D + 64, 112); }
  }
}
template <int H> DI void mmtile(const Fr<H>& F, f4 (&acc)[4], int t) {
#pragma unroll
  for (int i = 0; i < 4; i++) {
    if (H == 0 && t < 2) acc[i] = mfma(F.x[i], F.w[t], acc[i]);
    else acc[i] = mfma(F.w[t], F.x[i], acc[i]);
  }
}
template <int H, class I>
DI void half(const Fr<H>& Fc, Fr<H>& Fn, LAS char* L, const Ra& a, f4 (&acc)[H == 0 ? 5 : 4][4], I iss) {
  constexpr int NT = H == 0 ? 5 : 4;
#pragma unroll
  for (int t = 0; t < NT; t++) {
    ldpiece<H>(Fn, L, a, t);
    mmtile<H>(Fc, acc[t], t);
#pragma unroll
    for (int k = 0; k < 4; k++) { SGB(0x8, 1); SGB(0x100, 1); }
    PIN4(acc[t]);
    SCHED();
    iss(t);
    SCHED();
  }
}

DI int rot7(int s, int r) { const int k = s + r; return k >= 7 ? k - 7 : k; }
DI u32 p2_pair(int r, int spn) { return (2 * rot7(spn >> 1, r) + (spn & 1)) * 16384; }
constexpr int cunit(int m, int nn) { return m == 0 ? (nn == 0 ? 5 : nn == 3 ? 0 : 5 + nn) : 1 + nn; }

struct Sa {
  cf2 u[8];
  cf a1, a8, a16, a64, pq, lw, e0, c2, cw, wt, wg0, wg1;
};
struct Pw {
  u32 vwe, vwo, vce, vco, tl[4];
};
DI Pw pw_setup(int lane, int w, int xn) {
  Pw z;
  const int q = lane >> 4, r4 = (lane & 15) >> 2, pp = lane & 3;
  z.vwe = (lane >> 3) * 1024 + (((lane & 7) ^ (2 * (q & 1))) << 4);
  z.vwo = (z.vwe ^ 64) + 8192 - 1024;
  z.vce = (lane >> 3) * 1024 + (((lane & 7) ^ q) << 4);
  z.vco = (z.vce ^ 64) + 8192 - 1024;
#pragma unroll
  for (int nn = 0; nn < 4; nn++)
    z.tl[nn] = w * PW + (q & 1) * 1024 + r4 * 128 + ((((2 * (nn ^ xn)) ^ (2 * (((r4 >> 1) & 1) + 2 * (q & 1)))) + (pp >> 1)) << 4) + (pp & 1) * 8;
  return z;
}
DI void rdw(v4u (&wf)[4], LAS char* L, const Pw& z, int q, int ua, int ub) {
  const u32 o = (q >> 1) ? unitb(ub) : unitb(ua);
#pragma unroll
  for (int nn = 0; nn < 4; nn++) wf[nn] = tr2(L, o + z.tl[nn], 512);
}

#define HW 8
constexpr int HI0 = HW, HCHK = 28 - HW, HFL = HCHK - HW - 1;
constexpr int hiss(int m) { return m + 1 + HW < 44 ? m + 1 + HW : 44; }
constexpr int ncopy(int m) { return 0; }
constexpr int nother(int m) { return ncopy(m) + (m == HFL ? 1 : 0); }
constexpr int issued_upto(int m) {
  int n = hiss(m);
  for (int k = 0; k <= m; k++) n += nother(k);
  return n;
}
constexpr int pos_h(int m) {
  int n = m;
  for (int k = 0; k < 44; k++) if (hiss(k) <= m) n += nother(k);
  return n;
}
static_assert(hiss(HCHK - 1) == 28, "schedule");

DI void flag_read(v2u& v, srd_t rs, u32 l16, u32 so) {
  asm volatile("buffer_load_dwordx2 %0, %1, %2, %3 offen sc1" : "=&v"(v) : "v"((l16 & 0xF0) >> 1), "s"(rs), "s"(so) : "memory");
}
DI void poll_flags(srd_t rs, u32 l16, u32 so, u32 tagLo, u32 tagHi, int mask, v2u v) {
  const bool need = (mask >> ((l16 >> 4) & 3)) & 1;
#pragma unroll 1
  for (u32 spins = 0; !allok(!need || (v.x == tagLo && v.y == tagHi)) && spins < SPIN_CAP; spins++)
    asm volatile("s_sleep " PSLEEP "\n\tbuffer_load_dwordx2 %0, %1, %2, %3 offen sc1\n\ts_waitcnt vmcnt(0)"
                 : "=&v"(v)
                 : "v"((l16 & 0xF0) >> 1), "s"(rs), "s"(so)
                 : "memory");
  asm volatile("" ::: "memory");
}

DI void stage_offsets(int lane, int w, u32 (&vo)[5], u32& vs) {
#pragma unroll
  for (int e = 0; e < 4; e++) vo[e] = (32 * w + 8 * e + (lane >> 3)) * 1024 + (((lane & 7) ^ ((4 * w + e) & 7)) << 4) - e * 1024;
  const int gpos = (lane * 37) >> 8, gch = lane - gpos * 7;
  vo[4] = (8 * w + 4 * (gpos & 1) + (gpos >> 1)) * 3584 + gch * 16;
  vs = (8 * (w & 1) + (lane >> 3)) * 1024 + (((lane & 7) ^ ((4 * (w & 1) + (lane >> 4)) & 7)) << 4);
}

template <int H>
DI void phase1(const Args& p, LAS char* L, u32 lbase, int g, int j, int rot, int w, int lane, u32 stampv) {
  constexpr int NT = H == 0 ? 5 : 4;
  u32 vo[5], vs;
  stage_offsets(lane, w, vo, vs);
  asm volatile("" : "+v"(vo[0]), "+v"(vo[1]), "+v"(vo[2]), "+v"(vo[3]), "+v"(vo[4]), "+v"(vs));
  SCHED();
  const srd_t sx = mksrd((const char*)p.x + (size_t)g * (256 * 1024), 256 * 1024);
  const srd_t sb = mksrd((const char*)((w & 2) ? p.bim : p.bre) + (size_t)j * (16 * 1024), 16 * 1024);
  const srd_t s1 = mksrd((const char*)p.w1 + j * 112, 512 * 3584 - j * 112), s3 = mksrd((const char*)p.w3 + j * 112, 512 * 3584 - j * 112);
  Sa sa;
  v4u wf[7][4];
  const int tg = w & 3, c = lane & 15, q = lane >> 4, r4 = c >> 2, pp = lane & 3;
  const u32 ld0 = lbase + w * 4096, ld4 = lbase + G_OFF + w * 896, ld5 = ld4 + G3D, lds_s = lbase + SM_OFF + w * 1024;
  auto op = [&](int i, u32 kk, u32 slot) {
    if (i == 0) glds<0>(vo[0], sx, kk * 128, ld0, slot);
    else if (i == 1) glds<1024>(vo[1], sx, kk * 128, ld0, slot);
    else if (i == 2) glds<2048>(vo[2], sx, kk * 128, ld0, slot);
    else if (i == 3) glds<3072>(vo[3], sx, kk * 128, ld0, slot);
    else if (i == 4) glds56(vo[4], s1, kk * (64 * 3584), ld4, slot);
    else if (i == 5) glds56(vo[4], s3, kk * (64 * 3584), ld5, slot);
    else if (H == 0) glds<0>(vs, sb, kk * 128, lds_s, slot);
  };
  v2u ep;
  u32 pl0 = 0, pl1 = 0, pl2 = 0;
  v4u xr0 = {0, 0, 0, 0}, xr1 = {0, 0, 0, 0};
  {
    const u32 zero = 0;
    asm volatile("global_load_dwordx2 %0, %1, %2 sc1" : "=v"(ep) : "v"(zero), "s"(p.ws + WS_EP) : "memory");
    if (H == 0) {
      const u32 o = (16 * j + c) * 2;
      asm volatile("global_load_ushort %0, %1, %2" : "=v"(pl0) : "v"(o), "s"(p.lre) : "memory");
      asm volatile("global_load_ushort %0, %1, %2" : "=v"(pl1) : "v"(o), "s"(p.lim) : "memory");
      asm volatile("global_load_ushort %0, %1, %2" : "=v"(pl2) : "v"(o), "s"(p.ldt) : "memory");
      const u32 ro = (8 * j + 2 * tg + (lane >> 5)) * 1024 + (lane & 31) * 16;
      asm volatile("buffer_load_dwordx4 %0, %1, %2, 0 offen" : "=v"(xr0) : "v"(ro), "s"(sx) : "memory");
      asm volatile("buffer_load_dwordx4 %0, %1, %2, 0 offen offset:512" : "=v"(xr1) : "v"(ro), "s"(sx) : "memory");
    }
  }
  PSTAMP(4);
  op(6, rot, 0);
#pragma unroll
  for (int i = 0; i < 6; i++) op(i, rot, 0);
  SCHED();
  PSTAMP(5);
  int jl = j;
  asm volatile("" : "+s"(jl));
  const int r7 = rot7(((jl + NSH) & 7) == 7 ? 0 : ((jl + NSH) & 7), 2 * (g & 3));
  const srd_t rs = mksrd(p.ws, WS_BYTES);
  const srd_t sw2 = mksrd((const char*)p.w2 + (size_t)w * (224 * 1024) + ((jl + NSH) & 7) * 128, 224 * 1024 - ((jl + NSH) & 7) * 128);
  const srd_t scr = mksrd((const char*)p.cre + (size_t)((jl + NSH) & 7) * (64 * 1024) + w * 128, 64 * 1024 - w * 128);
  u32 tagLo = 0, tagHi = 0;
  if (H == 0) {
    asm volatile("s_waitcnt vmcnt(7)" : "+v"(ep), "+v"(pl0), "+v"(pl1), "+v"(pl2), "+v"(xr0), "+v"(xr1)::"memory");
    PSTAMP(6);
    const u64 tg64 = (((u64)rfl(ep.y) << 32) | rfl(ep.x)) + 1;
    tagLo = (u32)tg64; tagHi = (u32)(tg64 >> 32);
    f2 v[8];
#pragma unroll
    for (int k = 0; k < 4; k++) { v[k] = f2{bf2f(xr0[k]), bfhi(xr0[k])}; v[4 + k] = f2{bf2f(xr1[k]), bfhi(xr1[k])}; }
    const f2 sm = ((v[0] + v[1]) + (v[2] + v[3])) + ((v[4] + v[5]) + (v[6] + v[7]));
    const float m = sumx16(sum16(sm.x + sm.y)) * (1.f / 512.f);
    f2 s2 = {0.f, 0.f};
#pragma unroll
    for (int k = 0; k < 8; k++) { const f2 d = v[k] - sp(m); s2 = fma2(d, d, s2); }
    const float var = sumx16(sum16(s2.x + s2.y)) * (1.f / 512.f);
    const float sd = rbf(__builtin_amdgcn_sqrtf(rbf(rbf(var) + 1e-5f)));
    const v4u o = {f2u(rbf(m)), tagLo, f2u(rcp(sd)), tagHi};
    __builtin_amdgcn_raw_buffer_store_b128(o, rs, (lane & 31) ? 0x7FFFFFF0 : WS_SG + (g * 256 + 8 * j + 2 * tg + (lane >> 5)) * 16, 0, AUX_SC1);
    SCHED();
  }
  PSTAMP(8);
  op(6, (rot + 1) & 7, SLOT);
#pragma unroll
  for (int i = 0; i < 6; i++) op(i, (rot + 1) & 7, SLOT);
  if (H == 0) {
    const float lamr = bf2f(pl0), lami = bf2f(pl1), dt = rbf(expf(bf2f(pl2)));
    float sn, cn;
    sincosf(dt * lami, &sn, &cn);
    const float e = expf(dt * lamr);
    sa.a1 = cf{e * cn, e * sn};
    sa.a8 = csq(csq(csq(sa.a1)));
    sa.a16 = csq(sa.a8);
    sa.a64 = csq(csq(sa.a16));
    const float nr = sa.a1.r - 1.f, ni = sa.a1.i, den = 2.f / (lamr * lamr + lami * lami);
    sa.c2 = cf{(nr * lamr + ni * lami) * den, (ni * lamr - nr * lami) * den};
    asm volatile("" : "+v"(sa.a1.r), "+v"(sa.a1.i), "+v"(sa.a8.r), "+v"(sa.a8.i), "+v"(sa.a16.r), "+v"(sa.a16.i), "+v"(sa.a64.r), "+v"(sa.a64.i),
                 "+v"(sa.c2.r), "+v"(sa.c2.i));
    SCHED();
  }
  const u32 xa0 = (64 * tg + 16 * r4 + 8 * (c & 1) + ((c >> 1) & 1)) * 128 + ((q ^ (2 * r4 + (c & 1))) << 4);
  const u32 sa0 = SM_OFF + c * 128 + ((q ^ ((c >> 1) & 7)) << 4);
  const u32 ga0 = G_OFF + q * 896 + r4 * 224 + 8 * pp;
  const u32 gm0 = ga0 + 96 + (pp >= 2 ? G3D - 16 : 0);
  auto ra = [&](u32 slot, int ks) { return Ra{slot + (xa0 ^ (ks * 64)), slot + (sa0 ^ (ks * 64)), slot + ga0 + ks * 3584, slot + gm0 + ks * 3584}; };
  f4 acc[NT][4];
#pragma unroll
  for (int t = 0; t < NT; t++)
#pragma unroll
    for (int i = 0; i < 4; i++) acc[t][i] = f4{0, 0, 0, 0};
  PSTAMP(10);
  if (H == 1) WAITV(6); else WAITV(8);
  PSTAMP(11);
  BAR();
  STAMP(2);
  CSTAMP(14);
  Fr<H> F0, F1;
  {
    const Ra a = ra(0, 0);
#pragma unroll
    for (int t = 0; t < NT; t++) ldpiece<H>(F0, L, a, t);
    SCHED();
  }
  half<H>(F0, F1, L, ra(0, 1), acc, [&](int t) {
    if (t == 0) op(6, (rot + 2) & 7, 2 * SLOT);
    if (t < 3) { op(2 * t, (rot + 2) & 7, 2 * SLOT); op(2 * t + 1, (rot + 2) & 7, 2 * SLOT); }
  });
  if (H == 1) {
    asm volatile("s_waitcnt vmcnt(12)" : "+v"(ep)::"memory");
    const u64 tg64 = (((u64)rfl(ep.y) << 32) | rfl(ep.x)) + 1;
    tagLo = (u32)tg64; tagHi = (u32)(tg64 >> 32);
  }
  if (H == 0 && tg < 3) {
    const int b = g * 32 + j;
    const char* src = tg == 0 ? (const char*)p.w2 + b * 7168 : (const char*)(tg == 1 ? p.cre : p.cim) + b * 2048;
    const u32 off = (tg == 0 ? (lane < 56 ? lane : 0) : (lane & 15)) * 128;
    asm volatile("s_mov_b32 m0, %2\n\ts_nop 0\n\tglobal_load_lds_dword %0, %1" ::"v"(off), "s"(src), "s"(lbase + DUMMY) : "memory", "m0");
  }
  const u64 tag = ((u64)tagHi << 32) | tagLo;
  u32 cur = 0, nxt = SLOT;
  SCHED();
#pragma unroll 1
  for (int s = 0; s < 5; s++) {
    const Ra an0 = ra(nxt, 0);
    const u32 kk = (rot + s + 3) & 7;
    WAITL0();
    DSTAMP(16 + 3 * s);
    WAITV(6);
    DSTAMP(17 + 3 * s);
    BAR();
    DSTAMP(18 + 3 * s);
    SCHED();
    half<H>(F1, F0, L, an0, acc, [&](int t) {
      if (H == 0) {
        if (t == 0) op(6, kk, cur);
        else if (t == 1) op(0, kk, cur);
        else if (t == 3) op(4, kk, cur);
        else if (t == 4) op(1, kk, cur);
      } else {
        if (t == 0) op(4, kk, cur);
        else if (t == 2) op(0, kk, cur);
        else if (t == 3) op(1, kk, cur);
      }
    });
    half<H>(F0, F1, L, ra(nxt, 1), acc, [&](int t) {
      if (H == 0) {
        if (t == 0) op(2, kk, cur);
        else if (t == 2) op(5, kk, cur);
        else if (t == 3) op(3, kk, cur);
      } else {
        if (t == 0) op(5, kk, cur);
        else if (t == 1) op(2, kk, cur);
        else if (t == 2) op(3, kk, cur);
      }
    });
    if (H == 0) {
      if (s == (g >> 2)) {
        const v4u ones = {0x3F803F80u, 0x3F803F80u, 0x3F803F80u, 0x3F803F80u};
        const u32 a1 = nxt + (tg < 3 ? ga0 + 32 * tg : gm0), a2 = tg < 3 ? a1 + G3D : a1, a3 = nxt + (tg & 1) * 2048;
        const v4u f0 = tr2(L, a1, 112), f1 = tr2(L, a1 + 3584, 112), f2_ = tr2(L, a2, 112), f3 = tr2(L, a2 + 3584, 112);
        const v4u f4_ = lds128(L, a3 + sa0), f5 = lds128(L, a3 + (sa0 ^ 64));
        const f4 z4 = {0, 0, 0, 0};
        const f4 r1 = mfma(ones, f1, mfma(ones, f0, z4)), r2 = mfma(ones, f3, mfma(ones, f2_, z4)), r3 = mfma(ones, f5, mfma(ones, f4_, z4));
        const int t1 = tg == 0 ? 2 : tg == 3 ? 4 : 3 + 2 * tg;
        const int T = q == 0 ? t1 : q == 1 ? t1 + 1 : (tg & 1);
        const bool on = q == 0 || (q == 1 && tg < 3) || (q == 2 && tg < 2);
        const float v = q == 0 ? r1[0] : q == 1 ? r2[0] : r3[0];
        __builtin_amdgcn_raw_buffer_store_b64(v2u{f2u(v), tagLo}, rs, on ? WS_CS + ((j * 144 + T * 16 + c) * 8 + g) * 8 : 0x7FFFFFF0, 0, AUX_SC1);
      }
      SCHED();
    }
    cur = nxt;
    nxt = nxt == 2 * SLOT ? 0 : nxt + SLOT;
  }
  const Pw z = pw_setup(lane, w, g & 3);
  const u32 lw2 = lbase + w * PW;
  v4u gr, gc[3];
  const u32 gso = WS_SG + (g * 256 + 64 * tg) * 16, cso = WS_CS + (j * 9 + tg) * 1024, cso8 = WS_CS + (j * 9 + 8) * 1024;
#define GATHER(PRE_, POST_)                                                                                                              \
  asm volatile(PRE_ "buffer_load_dwordx4 %0, %4, %5, %6 offen sc1\n\tbuffer_load_dwordx4 %1, %4, %5, %7 offen sc1\n\t"                    \
                    "buffer_load_dwordx4 %2, %4, %5, %8 offen sc1\n\tbuffer_load_dwordx4 %3, %4, %5, %9 offen sc1" POST_                  \
               : "=&v"(gr), "=&v"(gc[0]), "=&v"(gc[1]), "=&v"(gc[2])                                                                     \
               : "v"(lane * 16), "s"(rs), "s"(gso), "s"(cso), "s"(cso + 4096), "s"(cso8)                                                 \
               : "memory")
  float mcol[4], rcol[4];
  f4 mrow[4], rrow[4], cs[NT];
  const u32 gra = GR + tg * 256 + (16 * r4 + (c & 3)) * 4, grr = GR + tg * 256 + 64 * q;
#pragma unroll
  for (int s = 5; s < 7; s++) {
    WAITL0();
    DSTAMP(16 + 3 * s);
    WAITV(6);
    DSTAMP(17 + 3 * s);
    BAR();
    DSTAMP(18 + 3 * s);
    SCHED();
    half<H>(F1, F0, L, ra(((s + 1) % 3) * SLOT, 0), acc, [&](int t) {
      if (H == 0 && t == 0 && s == 5) GATHER("", "");
      if (t == 1) { if (s == 5) glds2<unitb(0)>(z.vwe, z.vwo, sw2, p2_pair(r7, 0), lw2); else glds2<unitb(3)>(z.vwe, z.vwo, sw2, p2_pair(r7, 3), lw2); }
    });
    half<H>(F0, F1, L, ra(((s + 1) % 3) * SLOT, 1), acc, [&](int t) {
      if (t == 0) { if (s == 5) glds2<unitb(1)>(z.vwe, z.vwo, sw2, p2_pair(r7, 1), lw2); else glds2<unitb(4)>(z.vwe, z.vwo, sw2, p2_pair(r7, 4), lw2); }
      if (t == 2) { if (s == 5) glds2<unitb(2)>(z.vwe, z.vwo, sw2, p2_pair(r7, 2), lw2); else glds2<unitb(5)>(z.vwe, z.vwo, sw2, p2_pair(r7, 5), lw2); }
    });
    if (H == 0 && s == 5) {
      asm volatile("s_waitcnt vmcnt(6)" : "+v"(gr), "+v"(gc[0]), "+v"(gc[1]), "+v"(gc[2])::"memory");
#pragma unroll 1
      for (u32 spins = 0; spins < SPIN_CAP; spins++) {
        const u32 bad = (gr.y ^ tagLo) | (gr.w ^ tagHi) | (gc[0].y ^ tagLo) | (gc[0].w ^ tagLo) | (gc[1].y ^ tagLo) | (gc[1].w ^ tagLo) |
                        (gc[2].y ^ tagLo) | (gc[2].w ^ tagLo);
        if (allok(bad == 0)) break;
        GATHER("s_sleep 1\n\t", "\n\ts_waitcnt vmcnt(0)");
      }
      const u32 pos = GR + tg * 256 + ((lane & 48) + 4 * ((lane >> 1) & 3) + 2 * (lane & 1) + ((lane >> 3) & 1)) * 4;
      *(LAS u32*)(L + pos) = gr.x;
      *(LAS u32*)(L + pos + 1024) = gr.z;
      SCHED();
    }
    if (H == 0 && s == 6) {
#pragma unroll
      for (int k = 0; k < 3; k++) {
        float v = u2f(gc[k].x) + u2f(gc[k].z);
        v += dpp<0xB1>(v);
        v += dpp<0x4E>(v);
        *(LAS float*)(L + CST + (k < 2 ? tg + 4 * k : 8) * 64 + (lane >> 2) * 4) = v;
      }
      SCHED();
    }
  }
  WAITL0();
  DSTAMP(37);
  BAR();
  DSTAMP(39);
#pragma unroll
  for (int t = 0; t < NT; t++) {
    if (H == 0 && t < 2) cs[t][0] = ldsf(L, CST + 64 * t + c * 4);
    else cs[t] = ldsf4(L, CST + (H == 0 ? 0 : 320) + 64 * t + 16 * q);
    if (t < 4) {
      mcol[t] = ldsf(L, gra + 16 * t);
      rcol[t] = ldsf(L, gra + 16 * t + 1024);
      if (H == 0) { mrow[t] = ldsf4(L, grr + 16 * t); rrow[t] = ldsf4(L, grr + 16 * t + 1024); }
    }
    mmtile<H>(F1, acc[t], t);
#pragma unroll
    for (int k = 0; k < 4; k++) { SGB(0x8, 1); SGB(0x100, 2); }
    PIN4(acc[t]);
    SCHED();
    if (t == 0) glds2<unitb(6)>(z.vwe, z.vwo, sw2, p2_pair(r7, 6), lw2);
    if (t == 1) glds2<unitb(7)>(z.vwe, z.vwo, sw2, p2_pair(r7, 7), lw2);
    if (t == 2) glds2<unitb(8)>(z.vwe, z.vwo, sw2, p2_pair(r7, 8), lw2);
    SCHED();
  }
  STAMP(3);
  CSTAMP(15);
  __builtin_amdgcn_s_setprio(3);
  const int oct0 = 44 * (j >> 2) + 16 + 7 * (j & 3);
  const u32 hbs = WS_HB + (g * 4 + tg) * 4 * HB_TILE + oct0 * 256;
  auto r2 = [&](int k) {
    if (k == 0) { glds2<unitb(0)>(z.vwe, z.vwo, sw2, p2_pair(r7, 9), lw2); glds2<unitb(1)>(z.vwe, z.vwo, sw2, p2_pair(r7, 10), lw2); }
    if (k == 1) { glds2<unitb(2)>(z.vwe, z.vwo, sw2, p2_pair(r7, 11), lw2); glds2<unitb(3)>(z.vwe, z.vwo, sw2, p2_pair(r7, 12), lw2); }
    if (k == 2) { glds2<unitb(4)>(z.vwe, z.vwo, sw2, p2_pair(r7, 13), lw2); glds2<unitb(cunit(0, 0))>(z.vce, z.vco, scr, (g & 3) * 16384, lw2); }
    SCHED();
  };
  if (H == 1) {
    const u32 hv = c * 16 + (2 + (q >> 1) + 2 * (q & 1)) * 256;
#pragma unroll
    for (int i = 0; i < 4; i++) {
      const f2 nm = sp(-mcol[i]);
      const float nrl = rcol[i] * NL2E, ir2 = rcp(rcol[i] * rcol[i]);
      u32 v[4];
#pragma unroll
      for (int k = 0; k < 4; k++) {
        const int t = 2 * (k >> 1), h = k & 1;
        v[k] = swiglu2(fma2(nm, half2(cs[t], h), half2(acc[t][i], h)), fma2(nm, half2(cs[t + 1], h), half2(acc[t + 1][i], h)), nrl, ir2);
      }
      __builtin_amdgcn_raw_buffer_store_b128(swap16x2(v[0], v[1], v[2], v[3]), rs, hv + hbs + i * HB_TILE, 0, AUX_SC1);
      SCHED();
      if (i == 0) {
        CSTAMP(18);
        WAITL0();
        BAR();
        CSTAMP(19);
        WAITV(7);
        CSTAMP(16);
        rdw(wf[0], L, z, q, 0, 1); rdw(wf[1], L, z, q, 2, 3); rdw(wf[2], L, z, q, 4, 5);
        SCHED();
      } else {
        if (i == 1) WAITL0();
        if (i == 1) CSTAMP(21);
        if (i == 2) CSTAMP(23);
        if (i == 3) CSTAMP(20);
        r2(i - 1);
        if (i == 1) CSTAMP(22);
        if (i == 2) CSTAMP(24);
      }
    }
  } else {
#pragma unroll
    for (int k = 0; k < 8; k++) {
      const f2 m2 = half2(mrow[k >> 1], k & 1), r2v = half2(rrow[k >> 1], k & 1);
      sa.u[k].r = fma2(-m2, sp(cs[0][0]), half2(acc[0][k >> 1], k & 1)) * r2v;
      sa.u[k].i = fma2(-m2, sp(cs[1][0]), half2(acc[1][k >> 1], k & 1)) * r2v;
    }
    cf2 e2 = sa.u[0];
#pragma unroll
    for (int k = 1; k < 8; k++) e2 = cfma2(sa.a1, e2, sa.u[k]);
    {
      sa.e0 = cf{e2.r.x, e2.i.x};
      const cf e = cfma(sa.a8, sa.e0, cf{e2.r.y, e2.i.y});
      float er[4], ei[4];
      bcast4(e.r, er);
      bcast4(e.i, ei);
      const cf i0{er[0], ei[0]};
      const cf i1 = cfma(sa.a16, i0, cf{er[1], ei[1]});
      const cf i2 = cfma(sa.a16, i1, cf{er[2], ei[2]});
      const cf i3 = cfma(sa.a16, i2, cf{er[3], ei[3]});
      *(LAS v2u*)(L + XCH + (c * 4 + tg) * 8) = v2u{f2u(i3.r), f2u(i3.i)};
      sa.pq = csel(q & 2, csel(q & 1, i2, i1), csel(q & 1, i0, cf{0.f, 0.f}));
    }
    SCHED();
    CSTAMP(18);
    WAITL0();
    BAR();
    CSTAMP(19);
    {
      const f4 w01 = ldsf4(L, XCH + c * 32), w23 = ldsf4(L, XCH + c * 32 + 16);
      const cf p0{w01.x, w01.y};
      const cf p1 = cfma(sa.a64, p0, cf{w01.z, w01.w});
      const cf p2 = cfma(sa.a64, p1, cf{w23.x, w23.y});
      const cf p3 = cfma(sa.a64, p2, cf{w23.z, w23.w});
      const v4u o = {f2u(p3.r), tagLo, f2u(p3.i), tagHi};
      __builtin_amdgcn_raw_buffer_store_b128(o, rs, (q | tg) ? 0x7FFFFFF0 : WS_AG + (g * 512 + 16 * j + c) * 16, 0, AUX_SC1);
      sa.lw = csel(tg & 2, csel(tg & 1, p2, p1), csel(tg & 1, p0, cf{0.f, 0.f}));
    }
    SCHED();
    STAMP(8);
    WAITV(7);
    CSTAMP(16);
    rdw(wf[0], L, z, q, 0, 1); rdw(wf[1], L, z, q, 2, 3); rdw(wf[2], L, z, q, 4, 5);
    SCHED();
    u32 zx[2], zy[2];
#pragma unroll
    for (int ip = 0; ip < 2; ip++) {
      u32 px[2], py[2];
      f4 tm[2];
#pragma unroll
      for (int ii = 0; ii < 2; ii++) {
        const int i = 2 * ip + ii;
        const f2 nm = sp(-mcol[i]);
        const float nrl = rcol[i] * NL2E, ir2 = rcp(rcol[i] * rcol[i]);
        px[ii] = swiglu2(fma2(nm, cs[2].xy, acc[2][i].xy), fma2(nm, cs[3].xy, acc[3][i].xy), nrl, ir2);
        py[ii] = swiglu2(fma2(nm, cs[2].zw, acc[2][i].zw), fma2(nm, cs[3].zw, acc[3][i].zw), nrl, ir2);
        tm[ii].xy = fma2(nm, cs[4].xy, acc[4][i].xy);
        tm[ii].zw = fma2(nm, cs[4].zw, acc[4][i].zw);
      }
      __builtin_amdgcn_raw_buffer_store_b128(swap16x2(px[0], py[0], px[1], py[1]), rs, c * 16 + (q & 1) * HB_TILE + (q >> 1) * 256 + hbs + 2 * ip * HB_TILE, 0,
                                             AUX_SC1);
      SCHED();
      if (ip == 0) WAITL0();
      r2(ip);
      float rlo = rcol[2 * ip];
      asm volatile("" : "+v"(rlo));
      const float rsel = q < 2 ? rlo : rcol[2 * ip + 1];
      const float nrl = rsel * NL2E, ir2 = rcp(rsel * rsel);
      f4 gm, um;
#pragma unroll
      for (int r = 0; r < 4; r++) {
        u32 a = f2u(tm[0][r]), b = f2u(tm[1][r]);
        sw32(a, b);
        gm[r] = u2f(a);
        um[r] = u2f(b);
      }
      SWPAD2(gm, um);
      zx[ip] = swiglu2(gm.xy, um.xy, nrl, ir2);
      zy[ip] = swiglu2(gm.zw, um.zw, nrl, ir2);
    }
    __builtin_amdgcn_raw_buffer_store_b128(swap16x2(zx[0], zy[0], zx[1], zy[1]), rs, c * 16 + (2 * (q & 1) + (q >> 1)) * HB_TILE + hbs + 6 * 256, 0, AUX_SC1);
    SCHED();
    CSTAMP(20);
    r2(2);
  }
  STAMP(4);
  const int u = j >> 3, n = (j + NSH) & 7, xn = g & 3;
  GAS u64* flg = (GAS u64*)(p.ws + WS_FL) + ((g * 4 + tg) * 32 + j) * 4;
  const u32 wo = w * PW;
  const srd_t sci = mksrd((const char*)p.cim + (size_t)n * (64 * 1024) + w * 128, 64 * 1024 - w * 128);
  const int ti = w >> 1;
  v2u xv[2], dk;
  {
    const int dcol = 64 * n + 16 * (r4 ^ xn) + 4 * pp;
    u32 oo[2];
#pragma unroll
    for (int e = 0; e < 2; e++) {
      const int cc = 8 * (w & 1) + 4 * e + q;
      oo[e] = (64 * u + 16 * (cc >> 2) + 8 * (cc & 1) + 2 * ti + ((cc >> 1) & 1)) * 512 + dcol;
    }
    asm volatile("buffer_load_dwordx2 %0, %3, %5, 0 offen\n\tbuffer_load_dwordx2 %1, %4, %5, 0 offen\n\tglobal_load_dwordx2 %2, %6, %7"
                 : "=&v"(xv[0]), "=&v"(xv[1]), "=&v"(dk)
                 : "v"(oo[0] * 2), "v"(oo[1] * 2), "s"(sx), "v"(dcol * 2), "s"(p.dsk)
                 : "memory");
  }
  const u32 fso = WS_FL + ((g * 4 + u) * 32 + 4 * w) * 32;
  u32 l16 = lane * 16;
  v2u fv;
  auto rd36 = [&]() { rdw(wf[3], L, z, q, 6, 7); rdw(wf[4], L, z, q, 8, 0); rdw(wf[5], L, z, q, 1, 2); rdw(wf[6], L, z, q, 3, 4); SCHED(); };
  auto round3 = [&]() {
    WAITL0();
    glds2<unitb(cunit(0, 1))>(z.vce, z.vco, scr, (1 ^ xn) * 16384, lw2);
    glds2<unitb(cunit(0, 2))>(z.vce, z.vco, scr, (2 ^ xn) * 16384, lw2);
    glds2<unitb(cunit(0, 3))>(z.vce, z.vco, scr, (3 ^ xn) * 16384, lw2);
    glds2<unitb(cunit(1, 0))>(z.vce, z.vco, sci, (0 ^ xn) * 16384, lw2);
    glds2<unitb(cunit(1, 1))>(z.vce, z.vco, sci, (1 ^ xn) * 16384, lw2);
    flag_read(fv, rs, l16, fso);
    glds2<unitb(cunit(1, 2))>(z.vce, z.vco, sci, (2 ^ xn) * 16384, lw2);
    glds2<unitb(cunit(1, 3))>(z.vce, z.vco, sci, (3 ^ xn) * 16384, lw2);
    CSTAMP(28);
    asm volatile("s_waitcnt vmcnt(4)" : "+v"(fv)::"memory");
  };
  if (H == 0) {
    const u32 ao = WS_AG + (q * 512 + 16 * j + c) * 16;
    v4u g0, g1;
    asm volatile("buffer_load_dwordx4 %0, %2, %3, 0 offen sc1\n\tbuffer_load_dwordx4 %1, %2, %3, %4 offen sc1"
                 : "=&v"(g0), "=&v"(g1)
                 : "v"(ao), "s"(rs), "s"(4 * 512 * 16)
                 : "memory");
    WAITV(9);
    if (lane == 0) __hip_atomic_store(flg, tag, RLX_AGENT);
    STAMP(5);
    WAITV(6);
    rd36();
    const cf one{1.f, 0.f}, zero{0.f, 0.f};
    asm volatile("" : "+v"(sa.a16.r), "+v"(sa.a16.i), "+v"(sa.a64.r), "+v"(sa.a64.i));
    {
      const cf a128 = csq(sa.a64), a256 = csq(a128), a512 = csq(a256), a1024 = csq(a512);
      sa.cw = cmul(sa.c2, cmul(csel(q & 1, sa.a16, one), csel(q & 2, csq(sa.a16), one)));
      sa.wt = cmul(sa.cw, cmul(csel(tg & 1, sa.a64, one), csel(tg & 2, a128, one)));
      const int e0 = g - 1 - q, e1 = g - 5 - q;
      sa.wg1 = cmul(csel(e0 & 1, a256, one), csel(e0 & 2, a512, one));
      sa.wg0 = csel(e0 >= 0, cmul(sa.wg1, csel(e0 & 4, a1024, one)), zero);
      sa.wg1 = csel(e1 >= 0, sa.wg1, zero);
      asm volatile("" : "+v"(sa.wg0.r), "+v"(sa.wg0.i), "+v"(sa.wg1.r), "+v"(sa.wg1.i));
    }
    const cf wt8 = cmul(sa.wt, sa.a8);
    const cf k0 = cfma(sa.cw, sa.lw, cmul(sa.c2, sa.pq));
    const cf k1 = cfma(sa.a8, k0, cmul(sa.c2, sa.e0));
    cf2 h = cf2{f2{k0.r, k1.r}, f2{k0.i, k1.i}}, wt2 = cf2{f2{sa.wt.r, wt8.r}, f2{sa.wt.i, wt8.i}};
#pragma unroll
    for (int k = 0; k < 8; k++) {
      sa.u[k] = cmul2(sa.c2, sa.u[k]);
      asm volatile("" : "+v"(sa.u[k].r), "+v"(sa.u[k].i));
    }
    asm volatile("" : "+v"(h.r), "+v"(h.i), "+v"(wt2.r), "+v"(wt2.i));
    CSTAMP(25);
    asm volatile("s_waitcnt vmcnt(1)" : "+v"(g0), "+v"(g1), "+v"(xv[0]), "+v"(xv[1]), "+v"(dk)::"memory");
#pragma unroll 1
    for (u32 spins = 0; spins < SPIN_CAP; spins++) {
      if (allok((q >= g || (g0.y == tagLo && g0.w == tagHi)) && (q + 4 >= g || (g1.y == tagLo && g1.w == tagHi)))) break;
      asm volatile("s_sleep 1\n\tbuffer_load_dwordx4 %0, %2, %3, 0 offen sc1\n\tbuffer_load_dwordx4 %1, %2, %3, %4 offen sc1\n\ts_waitcnt vmcnt(0)"
                   : "=&v"(g0), "=&v"(g1)
                   : "v"(ao), "s"(rs), "s"(4 * 512 * 16)
                   : "memory");
    }
    {
      cf hin = cfma(sa.wg0, csel(q < g, cf{u2f(g0.x), u2f(g0.z)}, zero), cmul(sa.wg1, csel(q + 4 < g, cf{u2f(g1.x), u2f(g1.z)}, zero)));
      hin.r = sumx32(sumx16(hin.r));
      hin.i = sumx32(sumx16(hin.i));
      h = cfma2(hin, wt2, h);
    }
    STAMP(6);
    const u32 tb = TRB + tg * 1280;
    const u32 twr = tb + c * 40 + q * 8;
    const u32 trd = tb + (q >> 1) * 640 + (8 * (q & 1) + r4) * 40 + pp * 8;
    v4u o[4];
#pragma unroll
    for (int i = 0; i < 4; i++) {
      const cf2 ha = cfma2(sa.a1, h, sa.u[2 * i]);
      h = cfma2(sa.a1, ha, sa.u[2 * i + 1]);
      *(LAS v2u*)(L + twr) = v2u{pk2(ha.r), pk2(h.r)};
      *(LAS v2u*)(L + twr + 640) = v2u{pk2(-ha.i), pk2(-h.i)};
      asm volatile("" ::: "memory");
      o[i] = tr2(L, trd, 160);
      asm volatile("" ::: "memory");
    }
#pragma unroll
    for (int i = 0; i < 4; i++)
      __builtin_amdgcn_raw_buffer_store_b128(o[i], rs, lane * 16 + WS_HB + ((g * 4 + tg) * 4 + i) * HB_TILE + (11 * (j >> 2) + (j & 3)) * 1024, 0, AUX_SC1);
    CSTAMP(26);
    __builtin_amdgcn_s_setprio(1);
    round3();
    __builtin_amdgcn_raw_buffer_store_b64(v2u{tagLo, tagHi}, rs, l16 ? 0x7FFFFFF0 : WS_FL + (((g * 4 + tg) * 32 + j) * 4 + 2) * 8, 0, AUX_SC1);
  } else {
    WAITV(7);
    if (lane == 0) __hip_atomic_store(flg + 1, tag, RLX_AGENT);
    STAMP(5);
    __builtin_amdgcn_s_setprio(0);
    asm volatile("s_waitcnt vmcnt(1)" : "+v"(xv[0]), "+v"(xv[1]), "+v"(dk)::"memory");
    rd36();
    CSTAMP(26);
    round3();
  }
  STAMP(7);

  poll_flags(rs, l16, fso, tagLo, tagHi, 3, fv);
  STAMP(9);
  const u32 hs = WS_HB + (g * 4 + u) * 4 * HB_TILE + 11 * w * 1024;
  f4 ac2[4][4];
#pragma unroll
  for (int i = 0; i < 4; i++)
#pragma unroll
    for (int nn = 0; nn < 4; nn++) ac2[i][nn] = f4{0, 0, 0, 0};
  v4u hf[44];
  auto ldh = [&](int m) {
    const int i = m < 28 ? (m & 3) : ((m - 28) >> 2);
    const u32 so = hs + i * HB_TILE + (m < 28 ? 4 + rot7(m >> 2, r7) : ((m - 28) & 3)) * 1024;
    asm volatile("buffer_load_dwordx4 %0, %1, %2, %3 offen sc1" : "=v"(hf[m]) : "v"(l16), "s"(rs), "s"(so) : "memory");
  };
  u32 ub[4];
  int c2_ = 0, q2 = 0;
  auto ldc = [&](int s) {
    const u32 ca = ((2 * s + (q2 & 1)) ^ ((c2_ >> 1) & 7)) << 4;
#pragma unroll
    for (int nn = 0; nn < 4; nn++) wf[s][nn] = lds128(L, ub[nn] + ca);
  };
  __builtin_amdgcn_s_setprio(0);
#pragma unroll
  for (int m = 0; m < HI0; m++) ldh(m);
  CSTAMP(29);
  f4 sk[2];
  static_for<0, 44>([&](auto M) {
    constexpr int m = M.value;
    constexpr int i = m < 28 ? (m & 3) : ((m - 28) >> 2), ks = m < 28 ? (m >> 2) : ((m - 28) & 3);
    constexpr int nw = issued_upto(m - 1) - pos_h(m) - 1;
    static_assert(nw >= 0 && nw < 64, "vmcnt");
    asm volatile("s_waitcnt vmcnt(%1)" : "+v"(hf[m]) : "n"(nw) : "memory");
#pragma unroll
    for (int nn = 0; nn < 4; nn++) ac2[i][nn] = mfma(wf[ks][nn], hf[m], ac2[i][nn]);
    PIN4(ac2[i]);
    SCHED();
    if (m == HCHK) {
      STAMP(10);
      constexpr int nf = issued_upto(HCHK - 1) - issued_upto(HFL);
      asm volatile("s_waitcnt vmcnt(%1)" : "+v"(fv) : "n"(nf) : "memory");
      poll_flags(rs, l16, fso, tagLo, tagHi, 4, fv);
      STAMP(11);
    }
#pragma unroll
    for (int k = hiss(m - 1); k < hiss(m); k++) ldh(k);
    if (m == HFL) flag_read(fv, rs, l16, fso);
    if (m == 17) {
      asm volatile("" : "+v"(l16));
      c2_ = (l16 >> 4) & 15;
      q2 = l16 >> 8;
#pragma unroll
      for (int nn = 0; nn < 4; nn++) ub[nn] = wo + c2_ * 128 + ((q2 >> 1) ? unitb(cunit(1, nn)) : unitb(cunit(0, nn)));
    }
    if (m == 19) ldc(0);
    if (m == 21) ldc(1);
    if (m == 23) ldc(2);
    if (m == 25) ldc(3);
    if (m == 33) {
#pragma unroll
      for (int e = 0; e < 2; e++) {
        const int cc = 8 * (w & 1) + 4 * e + q2;
        const u32 sp_ = GR + u * 256 + (16 * (cc >> 2) + 4 * ti + (cc & 3)) * 4;
        const float mm = ldsf(L, sp_), r = ldsf(L, sp_ + 1024);
        const float xs[4] = {bf2f(xv[e].x), bfhi(xv[e].x), bf2f(xv[e].y), bfhi(xv[e].y)};
        const float ds[4] = {bf2f(dk.x), bfhi(dk.x), bf2f(dk.y), bfhi(dk.y)};
#pragma unroll
        for (int k = 0; k < 4; k++) sk[e][k] = xs[k] + rbf(rbf(rbf(xs[k] - mm) * r) * ds[k]);
        asm volatile("" : "+v"(sk[e]));
      }
    }
    if (m >= 28 && (m & 3) == 3) {
#pragma unroll
      for (int nn = 0; nn < 4; nn++)
        *(LAS f4*)(L + wo + unitb(2 * i + (nn >> 1)) + (nn & 1) * 1024 + q2 * 256 + ((c2_ ^ (4 * nn + q2)) << 4)) = ac2[i][nn];
    }
    SCHED();
  });
  STAMP(12);
  WAITL0();
  CSTAMP(33);
  BAR();
  CSTAMP(34);
  const int v0 = 2 * ti, v1 = v0 + 1;
  const u32 u0 = ((((v0 * 11) >> 5) + 2) % 3) * SLOT + (v0 % 3) * 2048, u1 = ((((v1 * 11) >> 5) + 2) % 3) * SLOT + (v1 % 3) * 2048;
  const srd_t so = mksrd(p.out + (size_t)g * (256 * 512), 256 * 512 * 4);
#pragma unroll
  for (int e = 0; e < 2; e++) {
    const int cc = 8 * (w & 1) + 4 * e + q2;
    const u32 rda = ((c2_ >> 3) ? u1 : u0) + ((c2_ >> 2) & 1) * 1024 + (c2_ & 3) * 256 + ((cc ^ c2_) << 4);
    f4 pv[8];
#pragma unroll
    for (int v = 0; v < 8; v++) pv[v] = ldsf4(L, v * PW + rda);
    const f4 s = (((pv[0] + pv[1]) + (pv[2] + pv[3])) + ((pv[4] + pv[5]) + (pv[6] + pv[7]))) + sk[e];
    const u32 o = (64 * u + 16 * (cc >> 2) + 8 * (cc & 1) + 2 * ti + ((cc >> 1) & 1)) * 512 + 64 * n + 16 * ((c2_ >> 2) ^ xn) + 4 * (c2_ & 3);
    __builtin_amdgcn_raw_buffer_store_b128(__builtin_bit_cast(v4u, s), so, o * 4, 0, AUX_SC1);
  }
  if (H == 0 && g == 7 && j == 0 && w == 0 && l16 == 0) __hip_atomic_store((GAS u64*)(p.ws + WS_EP), tag, RLX_AGENT);
  STAMP(13);
}

}

__global__ __launch_bounds__(512, 2) void s5_glu_fused(Args p) {
  __shared__ __attribute__((aligned(256))) char smem[LDS_BYTES];
  LAS char* L = (LAS char*)smem;
  const u32 lbase = (u32)(size_t)L;
  int lane = threadIdx.x & 63;
  const int w = __builtin_amdgcn_readfirstlane(threadIdx.x >> 6);
  const int b = blockIdx.x, xcd = b & 7, idx = b >> 3;
  const int g = (xcd & 1) * 4 + (idx & 3), j = (xcd >> 1) * 8 + (idx >> 2);
  const int rot = 2 * (((idx >> 2) + idx) & 3);
  u32 stampv = 0;
  STAMP(0);
  int wb = w;
  asm volatile("" : "+s"(wb));
  if (wb >= 4) {
    asm volatile("" : "+v"(lane));
    phase1<1>(p, L, lbase, g, j, rot, w, lane, stampv);
    __builtin_amdgcn_endpgm();
  }
  phase1<0>(p, L, lbase, g, j, rot, w, lane, stampv);
}

extern "C" void kernel_launch(void* const* d_in, const int* in_sizes, int n_in, void* d_out, int out_size, void* d_ws, size_t ws_size,
                              hipStream_t stream) {
  Args p;
  p.x = (const u16*)d_in[0];
  p.lre = (const u16*)d_in[1];
  p.lim = (const u16*)d_in[2];
  p.bre = (const u16*)d_in[3];
  p.bim = (const u16*)d_in[4];
  p.cre = (const u16*)d_in[5];
  p.cim = (const u16*)d_in[6];
  p.ldt = (const u16*)d_in[7];
  p.dsk = (const u16*)d_in[8];
  p.w1 = (const u16*)d_in[10];
  p.w3 = (const u16*)d_in[11];
  p.w2 = (const u16*)d_in[12];
  p.out = (float*)d_out;
  p.ws = (unsigned char*)d_ws;
  s5_glu_fused<<<256, 512, 0, stream>>>(p);
}
```

```cpp
#include <hip/hip_runtime.h>
#include <stdint.h>

typedef unsigned short u16;
typedef unsigned int u32;
typedef unsigned long long u64;
typedef u32 v4u __attribute__((ext_vector_type(4)));
typedef u32 v2u __attribute__((ext_vector_type(2)));
typedef float f4 __attribute__((ext_vector_type(4)));
typedef float f2 __attribute__((ext_vector_type(2)));
typedef short v4s __attribute__((ext_vector_type(4)));
typedef __bf16 v8bf __attribute__((ext_vector_type(8)));
typedef __bf16 v2bf __attribute__((ext_vector_type(2)));
typedef __amdgpu_buffer_rsrc_t srd_t;
#define LAS __attribute__((address_space(3)))
#define GAS __attribute__((address_space(1)))
#define DI __device__ __forceinline__
#define RLX_AGENT __ATOMIC_RELAXED, __HIP_MEMORY_SCOPE_AGENT

#define STAMPS 0
#define WARM 1
#define HAUX 16
#define R7M 0
#define DST 0
#define PSLEEP "4"
#define NSH 0
#define PRE 0
#define TLBW 0
#define CODEPF 0
#define PST 0
#define SPRIO 1
#define BSPLIT 1
#define ROT 3

namespace {
constexpr int SM_OFF = 32768, G_OFF = 36864, G3D = 7184, SLOT = 51456, AUXO = 3 * SLOT;
constexpr int GR = AUXO, CST = AUXO + 2048, TRB = AUXO + 4096, XCH = 49152, DUMMY = AUXO + 9216;
constexpr int LDS_BYTES = 163840;
constexpr int PW = 6144;
constexpr int unitb(int v) { return ((v / 3 + 2) % 3) * SLOT + (v % 3) * 2048; }
#define SH_FL 0
#define SH_AG 0
#define SH_HB 0
constexpr int WS_EP = 0, WS_FL = 4096 + SH_FL, WS_AG = 65536 + SH_AG, WS_SG = 196608, WS_CS = 262144, WS_HB = (1 << 20) + SH_HB, HB_TILE = 88 * 1024;
constexpr int WS_BYTES = WS_HB + 128 * HB_TILE;
constexpr int WS_STAMP = 32 << 20, NSTAMP = 14;
constexpr int AUX_SC1 = 16;
constexpr u32 SPIN_CAP = 1u << 17;
constexpr float NL2E = -1.4426950408889634f;

DI float bf2f(u32 b) { return __builtin_bit_cast(float, b << 16); }
DI float bfhi(u32 b) { return __builtin_bit_cast(float, b & 0xFFFF0000u); }
DI float u2f(u32 b) { return __builtin_bit_cast(float, b); }
DI u32 f2u(float f) { return __builtin_bit_cast(u32, f); }
DI float rbf(float f) { return (float)(__bf16)f; }
DI u32 pk2(f2 a) { return __builtin_bit_cast(u32, __builtin_convertvector(a, v2bf)); }
DI float rcp(float x) { return __builtin_amdgcn_rcpf(x); }
DI float ffma(float a, float b, float c) { return __builtin_fmaf(a, b, c); }
DI f2 fma2(f2 a, f2 b, f2 c) { return __builtin_elementwise_fma(a, b, c); }
DI f2 sp(float a) { return f2{a, a}; }
DI f2 half2(f4 v, int h) { return h ? v.zw : v.xy; }
DI f4 mfma(v4u a, v4u b, f4 c) {
  return __builtin_amdgcn_mfma_f32_16x16x32_bf16(__builtin_bit_cast(v8bf, a), __builtin_bit_cast(v8bf, b), c, 0, 0, 0);
}
DI v4u lds128(LAS char* L, u32 a) { return *(LAS v4u*)(L + a); }
DI f4 ldsf4(LAS char* L, u32 a) { return *(LAS f4*)(L + a); }
DI float ldsf(LAS char* L, u32 a) { return *(LAS float*)(L + a); }
DI v4u tr2(LAS char* L, u32 a, int d) {
  v2u lo = __builtin_bit_cast(v2u, __builtin_amdgcn_ds_read_tr16_b64_v4i16((LAS v4s*)(L + a)));
  v2u hi = __builtin_bit_cast(v2u, __builtin_amdgcn_ds_read_tr16_b64_v4i16((LAS v4s*)(L + a + d)));
  return v4u{lo.x, lo.y, hi.x, hi.y};
}
DI srd_t mksrd(const void* p, int bytes) { return __builtin_amdgcn_make_buffer_rsrc((void*)p, 0, bytes, 0x00020000); }
DI u32 rfl(u32 x) { return __builtin_amdgcn_readfirstlane(x); }
#pragma clang diagnostic ignored "-Winline-asm"
template <int OFF> DI void glds(u32 voff, srd_t srd, u32 soff, u32 la, u32 lb) {
  asm volatile("s_add_u32 m0, %3, %4\n\ts_nop 0\n\tbuffer_load_dwordx4 %0, %1, %2 offen offset:%5 lds" ::"v"(voff), "s"(srd), "s"(soff), "s"(la), "s"(lb),
               "n"(OFF)
               : "memory", "m0", "scc");
}
DI void glds56(u32 voff, srd_t srd, u32 soff, u32 la, u32 lb) {
  asm volatile("s_add_u32 m0, %3, %4\n\ts_mov_b32 exec_hi, 0xffffff\n\tbuffer_load_dwordx4 %0, %1, %2 offen lds\n\ts_mov_b32 exec_hi, -1" ::"v"(voff),
               "s"(srd), "s"(soff), "s"(la), "s"(lb)
               : "memory", "m0", "scc");
}
template <int LB> DI void glds2(u32 ve, u32 vo, srd_t srd, u32 soff, u32 la) {
  asm volatile("s_add_u32 m0, %4, %5\n\ts_nop 0\n\tbuffer_load_dwordx4 %0, %2, %3 offen lds\n\tbuffer_load_dwordx4 %1, %2, %3 offen offset:1024 lds" ::"v"(ve),
               "v"(vo), "s"(srd), "s"(soff), "s"(rfl(la)), "n"(LB)
               : "memory", "m0", "scc");
}
#define WAITV(n) asm volatile("s_waitcnt vmcnt(" #n ")" ::: "memory")
#define WAITL0() do { asm volatile("" ::: "memory"); __builtin_amdgcn_s_waitcnt(0xC07F); asm volatile("" ::: "memory"); } while (0)
#define BAR() __builtin_amdgcn_s_barrier()
#define SCHED() __builtin_amdgcn_sched_barrier(0)
#define SGB(m, n) __builtin_amdgcn_sched_group_barrier(m, n, 0)
#define PIN4(a) asm volatile("" : "+v"((a)[0]), "+v"((a)[1]), "+v"((a)[2]), "+v"((a)[3]))

template <int V> struct IC { static constexpr int value = V; };
template <int B, int E, class F> DI void static_for(F f) {
  if constexpr (B < E) {
    f(IC<B>{});
    static_for<B + 1, E>(f);
  }
}
DI bool allok(bool c) { return __builtin_amdgcn_ballot_w64(!c) == 0; }
struct cf { float r, i; };
DI float afma(float a, float b, float c) { float r; asm("v_fma_f32 %0, %1, %2, %3" : "=v"(r) : "v"(a), "v"(b), "v"(c)); return r; }
DI float anfma(float a, float b, float c) { float r; asm("v_fma_f32 %0, -%1, %2, %3" : "=v"(r) : "v"(a), "v"(b), "v"(c)); return r; }
DI cf cmul(cf a, cf b) { return cf{anfma(a.i, b.i, a.r * b.r), ffma(a.r, b.i, a.i * b.r)}; }
DI cf cfma(cf a, cf b, cf c) { return cf{afma(a.r, b.r, anfma(a.i, b.i, c.r)), ffma(a.r, b.i, ffma(a.i, b.r, c.i))}; }
DI cf csq(cf a) { return cf{anfma(a.i, a.i, a.r * a.r), 2.f * a.r * a.i}; }
DI cf csel(bool p, cf a, cf b) { return cf{p ? a.r : b.r, p ? a.i : b.i}; }
struct cf2 { f2 r, i; };
DI cf2 cfma2(cf a, cf2 b, cf2 c) { return cf2{fma2(sp(a.r), b.r, fma2(sp(-a.i), b.i, c.r)), fma2(sp(a.r), b.i, fma2(sp(a.i), b.r, c.i))}; }
DI cf2 cmul2(cf a, cf2 b) { return cf2{fma2(sp(a.r), b.r, sp(-a.i) * b.i), fma2(sp(a.r), b.i, sp(a.i) * b.r)}; }
template <int CTRL> DI float dpp(float x) { return u2f(__builtin_amdgcn_mov_dpp(f2u(x), CTRL, 0xf, 0xf, true)); }
DI float sum16(float v) {
  v += dpp<0xB1>(v); v += dpp<0x4E>(v); v += dpp<0x141>(v); v += dpp<0x140>(v);
  return v;
}
DI void sw16(u32& a, u32& b) { auto r = __builtin_amdgcn_permlane16_swap(a, b, false, false); a = r[0]; b = r[1]; }
DI void sw32(u32& a, u32& b) { auto r = __builtin_amdgcn_permlane32_swap(a, b, false, false); a = r[0]; b = r[1]; }
#define SWPAD2(a, b) asm volatile("s_nop 1" : "+v"(a), "+v"(b))
#define SWPAD4(a, b, c, d) asm volatile("s_nop 1" : "+v"(a), "+v"(b), "+v"(c), "+v"(d))
DI float sumx16(float v) { u32 a = f2u(v), b = a; sw16(a, b); SWPAD2(a, b); return u2f(a) + u2f(b); }
DI float sumx32(float v) { u32 a = f2u(v), b = a; sw32(a, b); SWPAD2(a, b); return u2f(a) + u2f(b); }
DI void bcast4(float v, float (&o)[4]) {
  u32 a = f2u(v), b = a;
  sw16(a, b);
  SWPAD2(a, b);
  u32 a2 = a, b2 = b;
  sw32(a, a2);
  sw32(b, b2);
  SWPAD4(a, b, a2, b2);
  o[0] = u2f(a); o[1] = u2f(b); o[2] = u2f(a2); o[3] = u2f(b2);
}
DI v4u swap16x2(u32 ax, u32 ay, u32 bx, u32 by) {
  sw16(ax, bx);
  sw16(ay, by);
  v4u o = {ax, ay, bx, by};
  asm volatile("s_nop 1" : "+v"(o));
  return o;
}

struct Args {
  const u16 *x, *lre, *lim, *bre, *bim, *cre, *cim, *ldt, *dsk, *w1, *w3, *w2;
  float* out;
  unsigned char* ws;
};

#define STAMP(k) do { } while (0)
#define CSTAMP(k) do { } while (0)
#define DSTAMP(k) do { } while (0)
#define PSTAMP(k) do { } while (0)

DI u32 swiglu2(f2 tg, f2 tu, float nrl, float ir2) {
  const f2 a = tg * sp(nrl);
  const f2 e = {__builtin_amdgcn_exp2f(a.x), __builtin_amdgcn_exp2f(a.y)};
  const f2 d = fma2(e, sp(ir2), sp(ir2));
  const f2 r = {rcp(d.x), rcp(d.y)};
  return pk2(tg * tu * r);
}

template <int H> struct Fr { v4u x[4]; v4u w[H == 0 ? 5 : 4]; };
struct Ra { u32 xa, sa, ga, gm; };

template <int H> DI void ldpiece(Fr<H>& F, LAS char* L, const Ra& a, int k) {
  if (k == 0) { F.x[0] = lds128(L, a.xa); F.x[1] = lds128(L, a.xa + 256); F.x[2] = lds128(L, a.xa + 512); }
  if (H == 0) {
    if (k == 1) { F.x[3] = lds128(L, a.xa + 768); F.w[0] = lds128(L, a.sa); F.w[1] = lds128(L, a.sa + 2048); }
    if (k == 2) { F.w[2] = tr2(L, a.ga, 112); F.w[3] = tr2(L, a.ga + G3D, 112); }
    if (k == 3) F.w[4] = tr2(L, a.gm, 112);
  } else {
    if (k == 0) F.x[3] = lds128(L, a.xa + 768);
    if (k == 1) { F.w[0] = tr2(L, a.ga + 32, 112); F.w[1] = tr2(L, a.ga + G3D + 32, 112); }
    if (k == 2) { F.w[2] = tr2(L, a.ga + 64, 112); F.w[3] = tr2(L, a.ga + G3D + 64, 112); }
  }
}
template <int H> DI void mmtile(const Fr<H>& F, f4 (&acc)[4], int t) {
#pragma unroll
  for (int i = 0; i < 4; i++) {
    if (H == 0 && t < 2) acc[i] = mfma(F.x[i], F.w[t], acc[i]);
    else acc[i] = mfma(F.w[t], F.x[i], acc[i]);
  }
}
template <int H, class I>
DI void half(const Fr<H>& Fc, Fr<H>& Fn, LAS char* L, const Ra& a, f4 (&acc)[H == 0 ? 5 : 4][4], I iss) {
  constexpr int NT = H == 0 ? 5 : 4;
#pragma unroll
  for (int t = 0; t < NT; t++) {
    ldpiece<H>(Fn, L, a, t);
    mmtile<H>(Fc, acc[t], t);
#pragma unroll
    for (int k = 0; k < 4; k++) { SGB(0x8, 1); SGB(0x100, 1); }
    PIN4(acc[t]);
    SCHED();
    iss(t);
    SCHED();
  }
}

DI int rot7(int s, int r) { const int k = s + r; return k >= 7 ? k - 7 : k; }
DI u32 p2_pair(int r, int spn) { return (2 * rot7(spn >> 1, r) + (spn & 1)) * 16384; }
constexpr int cunit(int m, int nn) { return m == 0 ? (nn == 0 ? 5 : nn == 3 ? 6 : nn - 1) : (nn == 0 ? 7 : 1 + nn); }

struct Sa {
  cf2 u[8];
  cf a1, a8, a16, a64, pq, lw, e0, c2, cw, wt, wg0, wg1;
};
struct Pw {
  u32 vwe, vwo, vce, vco, tl[4];
};
DI Pw pw_setup(int lane, int w, int xn) {
  Pw z;
  const int q = lane >> 4, r4 = (lane & 15) >> 2, pp = lane & 3;
  z.vwe = (lane >> 3) * 1024 + (((lane & 7) ^ (2 * (q & 1))) << 4);
  z.vwo = (z.vwe ^ 64) + 8192 - 1024;
  z.vce = (lane >> 3) * 1024 + (((lane & 7) ^ q) << 4);
  z.vco = (z.vce ^ 64) + 8192 - 1024;
#pragma unroll
  for (int nn = 0; nn < 4; nn++)
    z.tl[nn] = w * PW + (q & 1) * 1024 + r4 * 128 + ((((2 * (nn ^ xn)) ^ (2 * (((r4 >> 1) & 1) + 2 * (q & 1)))) + (pp >> 1)) << 4) + (pp & 1) * 8;
  return z;
}
DI void rdw(v4u (&wf)[4], LAS char* L, const Pw& z, int q, int ua, int ub) {
  const u32 o = (q >> 1) ? unitb(ub) : unitb(ua);
#pragma unroll
  for (int nn = 0; nn < 4; nn++) wf[nn] = tr2(L, o + z.tl[nn], 512);
}

#define HW 8
constexpr int HI0 = HW, HCHK = 28 - HW, HFL = HCHK - 6;
#define HG 1
constexpr int hiss(int m) {
  const int n = m < HCHK ? m + 1 + HW : 28 + HG * (m - HCHK + 1);
  return n < 44 ? n : 44;
}
constexpr int ncopy(int m) { return 0; }
constexpr int nother(int m) { return ncopy(m); }
constexpr int issued_upto(int m) {
  int n = hiss(m);
  for (int k = 0; k <= m; k++) n += nother(k);
  return n;
}
constexpr int pos_h(int m) {
  int n = m;
  for (int k = 0; k < 44; k++) if (hiss(k) <= m) n += nother(k);
  return n;
}
static_assert(hiss(HCHK - 1) == 28, "schedule");

typedef u32 s16u __attribute__((ext_vector_type(16)));
typedef u32 s8u __attribute__((ext_vector_type(8)));
template <class V, int N> DI bool flags_match(V v, u32 tagLo, u32 tagHi) {
  u32 x = 0;
#pragma unroll
  for (int k = 0; k < N; k += 2) x |= (v[k] ^ tagLo) | (v[k + 1] ^ tagHi);
  return x == 0;
}
DI bool flags_vec(srd_t rs, u32 l16, u32 so, u32 tagLo, u32 tagHi, u32 amask) {
  v2u v;
  asm volatile("buffer_load_dwordx2 %0, %1, %2, %3 offen sc1\n\ts_waitcnt vmcnt(0)" : "=&v"(v) : "v"((l16 >> 1) & amask), "s"(rs), "s"(so) : "memory");
  return allok(v.x == tagLo && v.y == tagHi);
}
DI void glu_read(s16u& v, const unsigned char* fp) { asm volatile("s_load_dwordx16 %0, %1, 0x0 glc" : "=&s"(v) : "s"(fp) : "memory"); }
DI void poll_glu(s16u v, const unsigned char* fp, srd_t rs, u32 l16, u32 so, u32 tagLo, u32 tagHi) {
#pragma unroll 1
  for (u32 spins = 1; spins < SPIN_CAP; spins++) {
    if (flags_match<s16u, 16>(v, tagLo, tagHi)) break;
    if ((spins & 7) == 0 && flags_vec(rs, l16, so, tagLo, tagHi, 0x38)) break;
    asm volatile("s_load_dwordx16 %0, %1, 0x0 glc\n\ts_waitcnt lgkmcnt(0)" : "=&s"(v) : "s"(fp) : "memory");
  }
  asm volatile("" ::: "memory");
}
DI void ssm_read(s8u& v, const unsigned char* fp) { asm volatile("s_load_dwordx8 %0, %1, 0x40 glc" : "=&s"(v) : "s"(fp) : "memory"); }
DI void poll_ssm(s8u v, const unsigned char* fp, srd_t rs, u32 l16, u32 so, u32 tagLo, u32 tagHi) {
#pragma unroll 1
  for (u32 spins = 1; spins < SPIN_CAP; spins++) {
    if (flags_match<s8u, 8>(v, tagLo, tagHi)) break;
    if ((spins & 7) == 0 && flags_vec(rs, l16, so + 64, tagLo, tagHi, 0x18)) break;
    __builtin_amdgcn_s_sleep(2);
    asm volatile("s_load_dwordx8 %0, %1, 0x40 glc\n\ts_waitcnt lgkmcnt(0)" : "=&s"(v) : "s"(fp) : "memory");
  }
  asm volatile("" ::: "memory");
}

DI void stage_offsets(int lane, int w, u32 (&vo)[5], u32& vs) {
#pragma unroll
  for (int e = 0; e < 4; e++) vo[e] = (32 * w + 8 * e + (lane >> 3)) * 1024 + (((lane & 7) ^ ((4 * w + e) & 7)) << 4) - e * 1024;
  const int gpos = (lane * 37) >> 8, gch = lane - gpos * 7;
  vo[4] = (8 * w + 4 * (gpos & 1) + (gpos >> 1)) * 3584 + gch * 16;
  vs = (8 * (w & 1) + (lane >> 3)) * 1024 + (((lane & 7) ^ ((4 * (w & 1) + (lane >> 4)) & 7)) << 4);
}

template <int H>
DI void phase1(const Args& p, LAS char* L, u32 lbase, srd_t rs, srd_t sx, srd_t s1, srd_t s3, srd_t sw2, srd_t scr, srd_t sb, int g, int j, int rot, int r7, int w,
               int lane, u32 stampv) {
  constexpr int NT = H == 0 ? 5 : 4;
  u32 vo[5], vs;
  stage_offsets(lane, w, vo, vs);
  Sa sa;
  v4u wf[7][4];
  const int tg = w & 3, c = lane & 15, q = lane >> 4, r4 = c >> 2, pp = lane & 3;
  const u32 ld0 = lbase + w * 4096, ld4 = lbase + G_OFF + w * 896, ld5 = ld4 + G3D, lds_s = lbase + SM_OFF + w * 1024;
  auto op = [&](int i, u32 kk, u32 slot) {
    if (i == 0) glds<0>(vo[0], sx, kk * 128, ld0, slot);
    else if (i == 1) glds<1024>(vo[1], sx, kk * 128, ld0, slot);
    else if (i == 2) glds<2048>(vo[2], sx, kk * 128, ld0, slot);
    else if (i == 3) glds<3072>(vo[3], sx, kk * 128, ld0, slot);
    else if (i == 4) glds56(vo[4], s1, kk * (64 * 3584), ld4, slot);
    else if (i == 5) glds56(vo[4], s3, kk * (64 * 3584), ld5, slot);
    else if (H == 0) glds<0>(vs, sb, kk * 128, lds_s, slot);
  };
  v2u ep;
  u32 pl0 = 0, pl1 = 0, pl2 = 0;
  v4u xr0 = {0, 0, 0, 0}, xr1 = {0, 0, 0, 0};
  {
    const u32 zero = 0;
    asm volatile("global_load_dwordx2 %0, %1, %2 sc1" : "=v"(ep) : "v"(zero), "s"(p.ws + WS_EP) : "memory");
    if (H == 0) {
      const u32 o = (16 * j + c) * 2;
      asm volatile("global_load_ushort %0, %1, %2" : "=v"(pl0) : "v"(o), "s"(p.lre) : "memory");
      asm volatile("global_load_ushort %0, %1, %2" : "=v"(pl1) : "v"(o), "s"(p.lim) : "memory");
      asm volatile("global_load_ushort %0, %1, %2" : "=v"(pl2) : "v"(o), "s"(p.ldt) : "memory");
      const u32 ro = (8 * j + 2 * tg + (lane >> 5)) * 1024 + (lane & 31) * 16;
      asm volatile("buffer_load_dwordx4 %0, %1, %2, 0 offen" : "=v"(xr0) : "v"(ro), "s"(sx) : "memory");
      asm volatile("buffer_load_dwordx4 %0, %1, %2, 0 offen offset:512" : "=v"(xr1) : "v"(ro), "s"(sx) : "memory");
    }
  }
  u32 tagLo = 0, tagHi = 0;
  if (H == 0) {
    asm volatile("s_waitcnt vmcnt(0)" : "+v"(ep), "+v"(pl0), "+v"(pl1), "+v"(pl2), "+v"(xr0), "+v"(xr1)::"memory");
    PSTAMP(6);
    const u64 tg64 = (((u64)rfl(ep.y) << 32) | rfl(ep.x)) + 1;
    tagLo = (u32)tg64; tagHi = (u32)(tg64 >> 32);
    f2 v[8];
#pragma unroll
    for (int k = 0; k < 4; k++) { v[k] = f2{bf2f(xr0[k]), bfhi(xr0[k])}; v[4 + k] = f2{bf2f(xr1[k]), bfhi(xr1[k])}; }
    const f2 sm = ((v[0] + v[1]) + (v[2] + v[3])) + ((v[4] + v[5]) + (v[6] + v[7]));
    const float m = sumx16(sum16(sm.x + sm.y)) * (1.f / 512.f);
    f2 s2 = {0.f, 0.f};
#pragma unroll
    for (int k = 0; k < 8; k++) { const f2 d = v[k] - sp(m); s2 = fma2(d, d, s2); }
    const float var = sumx16(sum16(s2.x + s2.y)) * (1.f / 512.f);
    const float sd = rbf(__builtin_amdgcn_sqrtf(rbf(rbf(var) + 1e-5f)));
    const v4u o = {f2u(rbf(m)), tagLo, f2u(rcp(sd)), tagHi};
    __builtin_amdgcn_raw_buffer_store_b128(o, rs, (lane & 31) ? 0x7FFFFFF0 : WS_SG + (g * 256 + 8 * j + 2 * tg + (lane >> 5)) * 16, 0, AUX_SC1);
    SCHED();
  }
  PSTAMP(8);
  op(6, (rot + 1) & 7, SLOT);
#pragma unroll
  for (int i = 0; i < 6; i++) op(i, (rot + 1) & 7, SLOT);
  if (H == 0) {
    const float lamr = bf2f(pl0), lami = bf2f(pl1), dt = rbf(expf(bf2f(pl2)));
    float sn, cn;
    sincosf(dt * lami, &sn, &cn);
    const float e = expf(dt * lamr);
    sa.a1 = cf{e * cn, e * sn};
    sa.a8 = csq(csq(csq(sa.a1)));
    sa.a16 = csq(sa.a8);
    sa.a64 = csq(csq(sa.a16));
    const float nr = sa.a1.r - 1.f, ni = sa.a1.i, den = 2.f / (lamr * lamr + lami * lami);
    sa.c2 = cf{(nr * lamr + ni * lami) * den, (ni * lamr - nr * lami) * den};
    asm volatile("" : "+v"(sa.a1.r), "+v"(sa.a1.i), "+v"(sa.a8.r), "+v"(sa.a8.i), "+v"(sa.a16.r), "+v"(sa.a16.i), "+v"(sa.a64.r), "+v"(sa.a64.i),
                 "+v"(sa.c2.r), "+v"(sa.c2.i));
    SCHED();
  }
  const u32 xa0 = (64 * tg + 16 * r4 + 8 * (c & 1) + ((c >> 1) & 1)) * 128 + ((q ^ (2 * r4 + (c & 1))) << 4);
  const u32 sa0 = SM_OFF + c * 128 + ((q ^ ((c >> 1) & 7)) << 4);
  const u32 ga0 = G_OFF + q * 896 + r4 * 224 + 8 * pp;
  const u32 gm0 = ga0 + 96 + (pp >= 2 ? G3D - 16 : 0);
  auto ra = [&](u32 slot, int ks) { return Ra{slot + (xa0 ^ (ks * 64)), slot + (sa0 ^ (ks * 64)), slot + ga0 + ks * 3584, slot + gm0 + ks * 3584}; };
  f4 acc[NT][4];
#pragma unroll
  for (int t = 0; t < NT; t++)
#pragma unroll
    for (int i = 0; i < 4; i++) acc[t][i] = f4{0, 0, 0, 0};
  PSTAMP(10);
  if (H == 1) WAITV(7);
  PSTAMP(11);
  BAR();
  STAMP(2);
  CSTAMP(14);
  Fr<H> F0, F1;
  {
    const Ra a = ra(0, 0);
#pragma unroll
    for (int t = 0; t < NT; t++) ldpiece<H>(F0, L, a, t);
    SCHED();
  }
  half<H>(F0, F1, L, ra(0, 1), acc, [&](int t) {
    if (t == 0) op(6, (rot + 2) & 7, 2 * SLOT);
    if (t < 3) { op(2 * t, (rot + 2) & 7, 2 * SLOT); op(2 * t + 1, (rot + 2) & 7, 2 * SLOT); }
  });
  if (H == 1) {
    asm volatile("s_waitcnt vmcnt(12)" : "+v"(ep)::"memory");
    const u64 tg64 = (((u64)rfl(ep.y) << 32) | rfl(ep.x)) + 1;
    tagLo = (u32)tg64; tagHi = (u32)(tg64 >> 32);
  }
  if (H == 0 && tg < 3) {
    const int b = g * 32 + j;
    const char* src = tg == 0 ? (const char*)p.w2 + b * 7168 : (const char*)(tg == 1 ? p.cre : p.cim) + b * 2048;
    const u32 off = (tg == 0 ? (lane < 56 ? lane : 0) : (lane & 15)) * 128;
    asm volatile("s_mov_b32 m0, %2\n\ts_nop 0\n\tglobal_load_lds_dword %0, %1" ::"v"(off), "s"(src), "s"(lbase + DUMMY) : "memory", "m0");
  }
  const u64 tag = ((u64)tagHi << 32) | tagLo;
  u32 cur = 0, nxt = SLOT;
  SCHED();
#pragma unroll 1
  for (int s = 0; s < 5; s++) {
    const Ra an0 = ra(nxt, 0);
    const u32 kk = (rot + s + 3) & 7;
    WAITL0();
    DSTAMP(16 + 3 * s);
    WAITV(6);
    DSTAMP(17 + 3 * s);
    BAR();
    DSTAMP(18 + 3 * s);
    SCHED();
    half<H>(F1, F0, L, an0, acc, [&](int t) {
      if (H == 0) {
        if (t == 0) op(6, kk, cur);
        else if (t == 1) op(0, kk, cur);
        else if (t == 3) op(4, kk, cur);
        else if (t == 4) op(1, kk, cur);
      } else {
        if (t == 0) op(4, kk, cur);
        else if (t == 2) op(0, kk, cur);
        else if (t == 3) op(1, kk, cur);
      }
    });
    half<H>(F0, F1, L, ra(nxt, 1), acc, [&](int t) {
      if (H == 0) {
        if (t == 0) op(2, kk, cur);
        else if (t == 2) op(5, kk, cur);
        else if (t == 3) op(3, kk, cur);
      } else {
        if (t == 0) op(5, kk, cur);
        else if (t == 1) op(2, kk, cur);
        else if (t == 2) op(3, kk, cur);
      }
    });
    if (H == 0) {
      if (s == (g >> 2)) {
        const v4u ones = {0x3F803F80u, 0x3F803F80u, 0x3F803F80u, 0x3F803F80u};
        const u32 a1 = nxt + (tg < 3 ? ga0 + 32 * tg : gm0), a2 = tg < 3 ? a1 + G3D : a1, a3 = nxt + (tg & 1) * 2048;
        const v4u f0 = tr2(L, a1, 112), f1 = tr2(L, a1 + 3584, 112), f2_ = tr2(L, a2, 112), f3 = tr2(L, a2 + 3584, 112);
        const v4u f4_ = lds128(L, a3 + sa0), f5 = lds128(L, a3 + (sa0 ^ 64));
        const f4 z4 = {0, 0, 0, 0};
        const f4 r1 = mfma(ones, f1, mfma(ones, f0, z4)), r2 = mfma(ones, f3, mfma(ones, f2_, z4)), r3 = mfma(ones, f5, mfma(ones, f4_, z4));
        const int t1 = tg == 0 ? 2 : tg == 3 ? 4 : 3 + 2 * tg;
        const int T = q == 0 ? t1 : q == 1 ? t1 + 1 : (tg & 1);
        const bool on = q == 0 || (q == 1 && tg < 3) || (q == 2 && tg < 2);
        const float v = q == 0 ? r1[0] : q == 1 ? r2[0] : r3[0];
        __builtin_amdgcn_raw_buffer_store_b64(v2u{f2u(v), tagLo}, rs, on ? WS_CS + ((j * 144 + T * 16 + c) * 8 + g) * 8 : 0x7FFFFFF0, 0, AUX_SC1);
      }
      SCHED();
    }
    cur = nxt;
    nxt = nxt == 2 * SLOT ? 0 : nxt + SLOT;
  }
  const Pw z = pw_setup(lane, w, g & 3);
  const u32 lw2 = lbase + w * PW;
  v4u gr, gc[3];
  const u32 gso = WS_SG + (g * 256 + 64 * tg) * 16, cso = WS_CS + (j * 9 + tg) * 1024, cso8 = WS_CS + (j * 9 + 8) * 1024;
#define GATHER(PRE_, POST_)                                                                                                              \
  asm volatile(PRE_ "buffer_load_dwordx4 %0, %4, %5, %6 offen sc1\n\tbuffer_load_dwordx4 %1, %4, %5, %7 offen sc1\n\t"                    \
                    "buffer_load_dwordx4 %2, %4, %5, %8 offen sc1\n\tbuffer_load_dwordx4 %3, %4, %5, %9 offen sc1" POST_                  \
               : "=&v"(gr), "=&v"(gc[0]), "=&v"(gc[1]), "=&v"(gc[2])                                                                     \
               : "v"(lane * 16), "s"(rs), "s"(gso), "s"(cso), "s"(cso + 4096), "s"(cso8)                                                 \
               : "memory")
  float mcol[4], rcol[4];
  f4 mrow[4], rrow[4], cs[NT];
  const u32 gra = GR + tg * 256 + (16 * r4 + (c & 3)) * 4, grr = GR + tg * 256 + 64 * q;
  auto w2p = [&](auto P, auto U) { glds2<unitb(U.value)>(z.vwe, z.vwo, sw2, p2_pair(r7, P.value), lw2); };
#define W2P(p_, u_) w2p(IC<p_>{}, IC<u_>{})
  WAITL0();
  DSTAMP(31);
  WAITV(6);
  DSTAMP(32);
  BAR();
  DSTAMP(33);
  SCHED();
  half<H>(F1, F0, L, ra(0, 0), acc, [&](int t) {
    if (H == 0 && t == 0) GATHER("", "");
    if (t == 1) W2P(0, 0);
    if (t == 2) W2P(1, 1);
    if (t == 3) W2P(2, 2);
  });
  half<H>(F0, F1, L, ra(0, 1), acc, [&](int t) {});
  if (H == 0) {
    asm volatile("s_waitcnt vmcnt(6)" : "+v"(gr), "+v"(gc[0]), "+v"(gc[1]), "+v"(gc[2])::"memory");
#pragma unroll 1
    for (u32 spins = 0; spins < SPIN_CAP; spins++) {
      const u32 bad = (gr.y ^ tagLo) | (gr.w ^ tagHi) | (gc[0].y ^ tagLo) | (gc[0].w ^ tagLo) | (gc[1].y ^ tagLo) | (gc[1].w ^ tagLo) |
                      (gc[2].y ^ tagLo) | (gc[2].w ^ tagLo);
      if (allok(bad == 0)) break;
      GATHER("s_sleep 1\n\t", "\n\ts_waitcnt vmcnt(0)");
    }
    const u32 pos = GR + tg * 256 + ((lane & 48) + 4 * ((lane >> 1) & 3) + 2 * (lane & 1) + ((lane >> 3) & 1)) * 4;
    *(LAS u32*)(L + pos) = gr.x;
    *(LAS u32*)(L + pos + 1024) = gr.z;
    SCHED();
  }
  WAITL0();
  DSTAMP(34);
  WAITV(2);
  DSTAMP(35);
  BAR();
  DSTAMP(36);
  SCHED();
  half<H>(F1, F0, L, ra(SLOT, 0), acc, [&](int t) {
    if (t == 0) W2P(3, 3);
    if (t == 1) W2P(4, 4);
    if (t == 2) W2P(5, 5);
    if (t == 3) rdw(wf[0], L, z, q, 0, 1);
  });
  half<H>(F0, F1, L, ra(SLOT, 1), acc, [&](int t) {
    if (t == 1) { asm volatile("s_waitcnt lgkmcnt(%0)" ::"n"(H == 0 ? 6 : 8) : "memory"); W2P(6, 0); }
    if (t == 2) W2P(7, 1);
  });
  if (H == 0) {
#pragma unroll
    for (int k = 0; k < 3; k++) {
      float v = u2f(gc[k].x) + u2f(gc[k].z);
      v += dpp<0xB1>(v);
      v += dpp<0x4E>(v);
      *(LAS float*)(L + CST + (k < 2 ? tg + 4 * k : 8) * 64 + (lane >> 2) * 4) = v;
    }
    SCHED();
  }
  WAITL0();
  DSTAMP(37);
  WAITV(4);
  BAR();
  DSTAMP(39);
#pragma unroll
  for (int t = 0; t < NT; t++) {
    if (H == 0 && t < 2) cs[t][0] = ldsf(L, CST + 64 * t + c * 4);
    else cs[t] = ldsf4(L, CST + (H == 0 ? 0 : 320) + 64 * t + 16 * q);
    if (t < 4) {
      mcol[t] = ldsf(L, gra + 16 * t);
      rcol[t] = ldsf(L, gra + 16 * t + 1024);
      if (H == 0) { mrow[t] = ldsf4(L, grr + 16 * t); rrow[t] = ldsf4(L, grr + 16 * t + 1024); }
    }
    mmtile<H>(F1, acc[t], t);
#pragma unroll
    for (int k = 0; k < 4; k++) { SGB(0x8, 1); SGB(0x100, 2); }
    PIN4(acc[t]);
    SCHED();
    if (t == 0) W2P(8, 6);
    if (t == 1) W2P(9, 7);
    if (t == 2) W2P(10, 8);
    if (t == (H == 0 ? 4 : 1)) rdw(wf[1], L, z, q, 2, 3);
    if (t == (H == 0 ? 4 : 2)) rdw(wf[2], L, z, q, 4, 5);
    SCHED();
  }
  STAMP(3);
  CSTAMP(15);
  __builtin_amdgcn_s_setprio(3);
  const int oct0 = 44 * (j >> 2) + 16 + 7 * (j & 3);
  const u32 hbs = WS_HB + (g * 4 + tg) * 4 * HB_TILE + oct0 * 256;
  const int u = j >> 3, n = (j + NSH) & 7, xn = g & 3;
  const u32 wo = w * PW;
  const srd_t sci = mksrd((const char*)p.cim + (size_t)n * (64 * 1024) + w * 128, 64 * 1024 - w * 128);
  const u32 fso = WS_FL + ((g * 4 + u) * 32 + 4 * w) * 32, fmine = WS_FL + ((g * 4 + tg) * 8 + (j >> 2)) * 128;
  u32 l16 = lane * 16;
  auto cp = [&](auto K) { glds2<unitb(cunit(K.value >> 2, K.value & 3))>(z.vce, z.vco, K.value < 4 ? scr : sci, ((K.value & 3) ^ xn) * 16384, lw2); };
#define CP(k_) cp(IC<k_>{})
  auto setflag = [&](int k) { __builtin_amdgcn_raw_buffer_store_b64(v2u{tagLo, tagHi}, rs, l16 ? 0x7FFFFFF0 : fmine + (k < 2 ? 2 * (j & 3) + k : 8 + (j & 3)) * 8, 0, AUX_SC1); };
  v2u xv[2], dk;
  auto ldx = [&]() {
    const int dcol = 64 * n + 16 * (r4 ^ xn) + 4 * pp;
    const u32 oo = (64 * u + 16 * tg + 8 * (q & 1) + 2 * H + (q >> 1)) * 512 + dcol;
    asm volatile("buffer_load_dwordx2 %0, %3, %4, 0 offen\n\tbuffer_load_dwordx2 %1, %3, %4, %7 offen\n\tglobal_load_dwordx2 %2, %5, %6"
                 : "=&v"(xv[0]), "=&v"(xv[1]), "=&v"(dk)
                 : "v"(oo * 2), "s"(sx), "v"(dcol * 2), "s"(p.dsk), "s"(4096)
                 : "memory");
  };
  if (H == 1) {
    const u32 hv = c * 16 + (2 + (q >> 1) + 2 * (q & 1)) * 256 + hbs;
    v4u o[4];
    WAITL0();
    W2P(11, 2); W2P(12, 3);
    SCHED();
#pragma unroll
    for (int i = 0; i < 4; i++) {
      const f2 nm = sp(-mcol[i]);
      const float nrl = rcol[i] * NL2E, ir2 = rcp(rcol[i] * rcol[i]);
      u32 v[4];
#pragma unroll
      for (int k = 0; k < 4; k++) {
        const int t = 2 * (k >> 1), h = k & 1;
        v[k] = swiglu2(fma2(nm, half2(cs[t], h), half2(acc[t][i], h)), fma2(nm, half2(cs[t + 1], h), half2(acc[t + 1][i], h)), nrl, ir2);
      }
      o[i] = swap16x2(v[0], v[1], v[2], v[3]);
      SCHED();
      if (i == 0) {
        CSTAMP(18);
        BAR();
        CSTAMP(19);
        W2P(13, 4); CP(0);
      } else {
        if (i == 1) __builtin_amdgcn_raw_buffer_store_b128(o[0], rs, hv, 0, AUX_SC1);
        __builtin_amdgcn_raw_buffer_store_b128(o[i], rs, hv + i * HB_TILE, 0, AUX_SC1);
      }
      if (i == 1) {
        WAITV(12);
        rdw(wf[3], L, z, q, 0, 1); rdw(wf[4], L, z, q, 6, 7);
      }
      if (i == 2) { WAITL0(); CP(1); CP(2); CP(3); CP(4); }
      SCHED();
    }
    STAMP(4);
    ldx();
    WAITV(17);
    rdw(wf[5], L, z, q, 8, 2); rdw(wf[6], L, z, q, 3, 4);
    WAITL0();
    CP(5); CP(6); CP(7);
    CSTAMP(28);
    asm volatile("s_waitcnt vmcnt(6)" : "+v"(xv[0]), "+v"(xv[1]), "+v"(dk)::"memory");
    setflag(1);
    STAMP(5);
  } else {
#pragma unroll
    for (int k = 0; k < 8; k++) {
      const f2 m2 = half2(mrow[k >> 1], k & 1), r2v = half2(rrow[k >> 1], k & 1);
      sa.u[k].r = fma2(-m2, sp(cs[0][0]), half2(acc[0][k >> 1], k & 1)) * r2v;
      sa.u[k].i = fma2(-m2, sp(cs[1][0]), half2(acc[1][k >> 1], k & 1)) * r2v;
    }
    cf2 e2 = sa.u[0];
#pragma unroll
    for (int k = 1; k < 8; k++) e2 = cfma2(sa.a1, e2, sa.u[k]);
    {
      sa.e0 = cf{e2.r.x, e2.i.x};
      const cf e = cfma(sa.a8, sa.e0, cf{e2.r.y, e2.i.y});
      float er[4], ei[4];
      bcast4(e.r, er);
      bcast4(e.i, ei);
      const cf i0{er[0], ei[0]};
      const cf i1 = cfma(sa.a16, i0, cf{er[1], ei[1]});
      const cf i2 = cfma(sa.a16, i1, cf{er[2], ei[2]});
      const cf i3 = cfma(sa.a16, i2, cf{er[3], ei[3]});
      *(LAS v2u*)(L + XCH + (c * 4 + tg) * 8) = v2u{f2u(i3.r), f2u(i3.i)};
      sa.pq = csel(q & 2, csel(q & 1, i2, i1), csel(q & 1, i0, cf{0.f, 0.f}));
    }
    SCHED();
    CSTAMP(18);
    WAITL0();
    BAR();
    CSTAMP(19);
    {
      const f4 w01 = ldsf4(L, XCH + c * 32), w23 = ldsf4(L, XCH + c * 32 + 16);
      const cf p0{w01.x, w01.y};
      const cf p1 = cfma(sa.a64, p0, cf{w01.z, w01.w});
      const cf p2 = cfma(sa.a64, p1, cf{w23.x, w23.y});
      const cf p3 = cfma(sa.a64, p2, cf{w23.z, w23.w});
      const v4u o = {f2u(p3.r), tagLo, f2u(p3.i), tagHi};
      __builtin_amdgcn_raw_buffer_store_b128(o, rs, (q | tg) ? 0x7FFFFFF0 : WS_AG + (g * 512 + 16 * j + c) * 16, 0, AUX_SC1);
      sa.lw = csel(tg & 2, csel(tg & 1, p2, p1), csel(tg & 1, p0, cf{0.f, 0.f}));
    }
    SCHED();
    STAMP(8);
    W2P(11, 2); W2P(12, 3); W2P(13, 4); CP(0);
    SCHED();
    u32 zx[2], zy[2];
#pragma unroll
    for (int ip = 0; ip < 2; ip++) {
      u32 px[2], py[2];
      f4 tm[2];
#pragma unroll
      for (int ii = 0; ii < 2; ii++) {
        const int i = 2 * ip + ii;
        const f2 nm = sp(-mcol[i]);
        const float nrl = rcol[i] * NL2E, ir2 = rcp(rcol[i] * rcol[i]);
        px[ii] = swiglu2(fma2(nm, cs[2].xy, acc[2][i].xy), fma2(nm, cs[3].xy, acc[3][i].xy), nrl, ir2);
        py[ii] = swiglu2(fma2(nm, cs[2].zw, acc[2][i].zw), fma2(nm, cs[3].zw, acc[3][i].zw), nrl, ir2);
        tm[ii].xy = fma2(nm, cs[4].xy, acc[4][i].xy);
        tm[ii].zw = fma2(nm, cs[4].zw, acc[4][i].zw);
      }
      __builtin_amdgcn_raw_buffer_store_b128(swap16x2(px[0], py[0], px[1], py[1]), rs, c * 16 + (q & 1) * HB_TILE + (q >> 1) * 256 + hbs + 2 * ip * HB_TILE, 0,
                                             AUX_SC1);
      SCHED();
      if (ip == 0) {
        WAITV(12);
        rdw(wf[3], L, z, q, 0, 1); rdw(wf[4], L, z, q, 6, 7);
        SCHED();
      }
      float rlo = rcol[2 * ip];
      asm volatile("" : "+v"(rlo));
      const float rsel = q < 2 ? rlo : rcol[2 * ip + 1];
      const float nrl = rsel * NL2E, ir2 = rcp(rsel * rsel);
      f4 gm, um;
#pragma unroll
      for (int r = 0; r < 4; r++) {
        u32 a = f2u(tm[0][r]), b = f2u(tm[1][r]);
        sw32(a, b);
        gm[r] = u2f(a);
        um[r] = u2f(b);
      }
      SWPAD2(gm, um);
      zx[ip] = swiglu2(gm.xy, um.xy, nrl, ir2);
      zy[ip] = swiglu2(gm.zw, um.zw, nrl, ir2);
    }
    __builtin_amdgcn_raw_buffer_store_b128(swap16x2(zx[0], zy[0], zx[1], zy[1]), rs, c * 16 + (2 * (q & 1) + (q >> 1)) * HB_TILE + hbs + 6 * 256, 0, AUX_SC1);
    STAMP(4);
    ldx();
    const u32 ao = WS_AG + (q * 512 + 16 * j + c) * 16;
    v4u g0, g1;
    asm volatile("buffer_load_dwordx4 %0, %2, %3, 0 offen sc1\n\tbuffer_load_dwordx4 %1, %2, %3, %4 offen sc1"
                 : "=&v"(g0), "=&v"(g1)
                 : "v"(ao), "s"(rs), "s"(4 * 512 * 16)
                 : "memory");
    const cf one{1.f, 0.f}, zero{0.f, 0.f};
    asm volatile("" : "+v"(sa.a16.r), "+v"(sa.a16.i), "+v"(sa.a64.r), "+v"(sa.a64.i));
    {
      const cf a128 = csq(sa.a64), a256 = csq(a128), a512 = csq(a256), a1024 = csq(a512);
      sa.cw = cmul(sa.c2, cmul(csel(q & 1, sa.a16, one), csel(q & 2, csq(sa.a16), one)));
      sa.wt = cmul(sa.cw, cmul(csel(tg & 1, sa.a64, one), csel(tg & 2, a128, one)));
      const int e0 = g - 1 - q, e1 = g - 5 - q;
      sa.wg1 = cmul(csel(e0 & 1, a256, one), csel(e0 & 2, a512, one));
      sa.wg0 = csel(e0 >= 0, cmul(sa.wg1, csel(e0 & 4, a1024, one)), zero);
      sa.wg1 = csel(e1 >= 0, sa.wg1, zero);
      asm volatile("" : "+v"(sa.wg0.r), "+v"(sa.wg0.i), "+v"(sa.wg1.r), "+v"(sa.wg1.i));
    }
    const cf wt8 = cmul(sa.wt, sa.a8);
    const cf k0 = cfma(sa.cw, sa.lw, cmul(sa.c2, sa.pq));
    const cf k1 = cfma(sa.a8, k0, cmul(sa.c2, sa.e0));
    cf2 h = cf2{f2{k0.r, k1.r}, f2{k0.i, k1.i}}, wt2 = cf2{f2{sa.wt.r, wt8.r}, f2{sa.wt.i, wt8.i}};
#pragma unroll
    for (int k = 0; k < 8; k++) {
      sa.u[k] = cmul2(sa.c2, sa.u[k]);
      asm volatile("" : "+v"(sa.u[k].r), "+v"(sa.u[k].i));
    }
    asm volatile("" : "+v"(h.r), "+v"(h.i), "+v"(wt2.r), "+v"(wt2.i));
    CSTAMP(25);
    WAITV(5);
    setflag(0);
    STAMP(5);
    rdw(wf[5], L, z, q, 8, 2); rdw(wf[6], L, z, q, 3, 4);
    asm volatile("s_waitcnt vmcnt(1)" : "+v"(g0), "+v"(g1), "+v"(xv[0]), "+v"(xv[1]), "+v"(dk)::"memory");
#pragma unroll 1
    for (u32 spins = 0; spins < SPIN_CAP; spins++) {
      if (allok((q >= g || (g0.y == tagLo && g0.w == tagHi)) && (q + 4 >= g || (g1.y == tagLo && g1.w == tagHi)))) break;
      asm volatile("s_sleep 1\n\tbuffer_load_dwordx4 %0, %2, %3, 0 offen sc1\n\tbuffer_load_dwordx4 %1, %2, %3, %4 offen sc1\n\ts_waitcnt vmcnt(0)"
                   : "=&v"(g0), "=&v"(g1)
                   : "v"(ao), "s"(rs), "s"(4 * 512 * 16)
                   : "memory");
    }
    {
      cf hin = cfma(sa.wg0, csel(q < g, cf{u2f(g0.x), u2f(g0.z)}, zero), cmul(sa.wg1, csel(q + 4 < g, cf{u2f(g1.x), u2f(g1.z)}, zero)));
      hin.r = sumx32(sumx16(hin.r));
      hin.i = sumx32(sumx16(hin.i));
      h = cfma2(hin, wt2, h);
    }
    STAMP(6);
    const u32 tb = TRB + tg * 1280;
    const u32 twr = tb + c * 40 + q * 8;
    const u32 trd = tb + (q >> 1) * 640 + (8 * (q & 1) + r4) * 40 + pp * 8;
    v4u o[4];
#pragma unroll
    for (int i = 0; i < 4; i++) {
      const cf2 ha = cfma2(sa.a1, h, sa.u[2 * i]);
      h = cfma2(sa.a1, ha, sa.u[2 * i + 1]);
      *(LAS v2u*)(L + twr) = v2u{pk2(ha.r), pk2(h.r)};
      *(LAS v2u*)(L + twr + 640) = v2u{pk2(-ha.i), pk2(-h.i)};
      asm volatile("" ::: "memory");
      o[i] = tr2(L, trd, 160);
      asm volatile("" ::: "memory");
    }
#pragma unroll
    for (int i = 0; i < 4; i++)
      __builtin_amdgcn_raw_buffer_store_b128(o[i], rs, lane * 16 + WS_HB + ((g * 4 + tg) * 4 + i) * HB_TILE + (11 * (j >> 2) + (j & 3)) * 1024, 0, AUX_SC1);
    CSTAMP(26);
    WAITL0();
    CP(1); CP(2); CP(3);
    WAITV(6);
    setflag(2);
    __builtin_amdgcn_s_setprio(1);
    CP(4); CP(5); CP(6); CP(7);
    CSTAMP(28);
  }
  f4 sk[2];
  float smm[2], srr[2];
#pragma unroll
  for (int e = 0; e < 2; e++) {
    const u32 sp_ = GR + u * 256 + (16 * tg + 4 * (2 * e + H) + q) * 4;
    smm[e] = ldsf(L, sp_);
    srr[e] = ldsf(L, sp_ + 1024);
  }
  asm volatile("" : "+v"(smm[0]), "+v"(smm[1]), "+v"(srr[0]), "+v"(srr[1]));
  s16u gfv;
  glu_read(gfv, p.ws + fso);
  asm volatile("" : "+v"(xv[0]), "+v"(xv[1]));
#pragma unroll
  for (int e = 0; e < 2; e++) {
    const float mm = smm[e], r = srr[e];
    const float xs[4] = {bf2f(xv[e].x), bfhi(xv[e].x), bf2f(xv[e].y), bfhi(xv[e].y)};
    const float ds[4] = {bf2f(dk.x), bfhi(dk.x), bf2f(dk.y), bfhi(dk.y)};
#pragma unroll
    for (int k = 0; k < 4; k++) sk[e][k] = xs[k] + rbf(rbf(rbf(xs[k] - mm) * r) * ds[k]);
    asm volatile("" : "+v"(sk[e]));
  }
  asm volatile("s_waitcnt lgkmcnt(0)" : "+s"(gfv)::"memory");
  STAMP(7);

  __builtin_amdgcn_s_setprio(0);
  poll_glu(gfv, p.ws + fso, rs, l16, fso, tagLo, tagHi);
  s8u sfv;
  STAMP(9);
  const u32 hs = WS_HB + (g * 4 + u) * 4 * HB_TILE + 11 * w * 1024;
  f4 ac2[4][4];
#pragma unroll
  for (int i = 0; i < 4; i++)
#pragma unroll
    for (int nn = 0; nn < 4; nn++) ac2[i][nn] = f4{0, 0, 0, 0};
  v4u hf[44];
  auto ldh = [&](int m) {
    const int i = m < 28 ? (m & 3) : ((m - 28) >> 2);
    const u32 so = hs + i * HB_TILE + (m < 28 ? 4 + rot7(m >> 2, r7) : ((m - 28) & 3)) * 1024;
    asm volatile("buffer_load_dwordx4 %0, %1, %2, %3 offen sc1" : "=v"(hf[m]) : "v"(l16), "s"(rs), "s"(so) : "memory");
  };
  u32 ub[4];
  int c2_ = 0, q2 = 0;
  auto ldc = [&](int s) {
    const u32 ca = ((2 * s + (q2 & 1)) ^ ((c2_ >> 1) & 7)) << 4;
#pragma unroll
    for (int nn = 0; nn < 4; nn++) wf[s][nn] = lds128(L, ub[nn] + ca);
  };
  auto wrt = [&](int i) {
#pragma unroll
    for (int nn = 0; nn < 4; nn++)
      *(LAS f4*)(L + wo + unitb(2 * i + (nn >> 1)) + (nn & 1) * 1024 + q2 * 256 + ((c2_ ^ (4 * nn + q2)) << 4)) = ac2[i][nn];
  };
  const srd_t so = mksrd(p.out + (size_t)g * (256 * 512), 256 * 512 * 4);
  f4 pv[8];
  auto redrd = [&](int e) {
    const u32 rda = ((c2_ >> 3) ? unitb(4 * e + 2 * H + 1) : unitb(4 * e + 2 * H)) + ((c2_ >> 2) & 1) * 1024 + (c2_ & 3) * 256 + (((4 * tg + q2) ^ c2_) << 4);
#pragma unroll
    for (int v = 0; v < 8; v++) pv[v] = ldsf4(L, v * PW + rda);
  };
  auto redst = [&](int e) {
    const f4 s = (((pv[0] + pv[1]) + (pv[2] + pv[3])) + ((pv[4] + pv[5]) + (pv[6] + pv[7]))) + sk[e];
    const u32 o = (64 * u + 16 * tg + 8 * (q2 & 1) + 2 * (2 * e + H) + (q2 >> 1)) * 512 + 64 * n + 16 * ((c2_ >> 2) ^ xn) + 4 * (c2_ & 3);
    __builtin_amdgcn_raw_buffer_store_b128(__builtin_bit_cast(v4u, s), so, o * 4, 0, AUX_SC1);
  };
  __builtin_amdgcn_s_setprio(SPRIO ? H : 0);
#pragma unroll
  for (int m = 0; m < HI0; m++) ldh(m);
  CSTAMP(29);
  static_for<0, 44>([&](auto M) {
    constexpr int m = M.value;
    constexpr int i = m < 28 ? (m & 3) : ((m - 28) >> 2), ks = m < 28 ? (m >> 2) : ((m - 28) & 3);
    constexpr int nw = issued_upto(m - 1) - pos_h(m) - 1 + (m > 40 ? 1 : 0);
    static_assert(nw >= 0 && nw < 64 && hiss(37) == 44, "vmcnt");
    asm volatile("s_waitcnt vmcnt(%1)" : "+v"(hf[m]) : "n"(nw) : "memory");
#pragma unroll
    for (int nn = 0; nn < 4; nn++) ac2[i][nn] = mfma(wf[ks][nn], hf[m], ac2[i][nn]);
    PIN4(ac2[i]);
    SCHED();
    if (m == HCHK) {
      STAMP(10);
      asm volatile("s_waitcnt lgkmcnt(0)" : "+s"(sfv)::"memory");
      poll_ssm(sfv, p.ws + fso, rs, l16, fso, tagLo, tagHi);
      STAMP(11);
    }
#pragma unroll
    for (int k = hiss(m - 1); k < hiss(m); k++) ldh(k);
    if (m == HFL) ssm_read(sfv, p.ws + fso);
    if (m == 17) {
      asm volatile("" : "+v"(l16));
      c2_ = (l16 >> 4) & 15;
      q2 = l16 >> 8;
#pragma unroll
      for (int nn = 0; nn < 4; nn++) ub[nn] = wo + c2_ * 128 + ((q2 >> 1) ? unitb(cunit(1, nn)) : unitb(cunit(0, nn)));
    }
    if (m == 19) ldc(0);
    if (m == 21) ldc(1);
    if (m == 23) ldc(2);
    if (m == 25) ldc(3);
    if (m == 32) wrt(0);
    if (m == 36) wrt(1);
    if (m == 38) { WAITL0(); BAR(); redrd(0); }
    if (m == 40) { redst(0); wrt(2); }
    if (m == 43) wrt(3);
    SCHED();
  });
  STAMP(12);
  WAITL0();
  CSTAMP(33);
  BAR();
  CSTAMP(34);
  redrd(1);
  redst(1);
  if (H == 0 && g == 7 && j == 0 && w == 0 && l16 == 0) __hip_atomic_store((GAS u64*)(p.ws + WS_EP), tag, RLX_AGENT);
  STAMP(13);
}

}

__global__ __launch_bounds__(512, 2) void s5_glu_fused(Args p) {
  __shared__ __attribute__((aligned(256))) char smem[LDS_BYTES];
  LAS char* L = (LAS char*)smem;
  const u32 lbase = (u32)(size_t)L;
  int lane = threadIdx.x & 63;
  const int w = __builtin_amdgcn_readfirstlane(threadIdx.x >> 6);
  const int b = blockIdx.x, xcd = b & 7, idx = b >> 3;
  const int g = (xcd & 1) * 4 + (idx & 3), j = (xcd >> 1) * 8 + (idx >> 2);
  const int rot = 2 * (((idx >> 2) + idx) & 3);
  u32 stampv = 0;
  STAMP(0);
  const srd_t sx = mksrd((const char*)p.x + (size_t)g * (256 * 1024), 256 * 1024);
  PSTAMP(4);
  u32 vo[5], vs;
  stage_offsets(lane, w, vo, vs);
  const srd_t sb = mksrd((const char*)((w & 2) ? p.bim : p.bre) + (size_t)j * (16 * 1024), 16 * 1024);
  if (w < 4) glds<0>(vs, sb, rot * 128, lbase + SM_OFF + w * 1024, 0);
  glds<0>(vo[0], sx, rot * 128, lbase + w * 4096, 0);
  glds<1024>(vo[1], sx, rot * 128, lbase + w * 4096, 0);
  glds<2048>(vo[2], sx, rot * 128, lbase + w * 4096, 0);
  glds<3072>(vo[3], sx, rot * 128, lbase + w * 4096, 0);
  const srd_t s1 = mksrd((const char*)p.w1 + j * 112, 512 * 3584 - j * 112), s3 = mksrd((const char*)p.w3 + j * 112, 512 * 3584 - j * 112);
  glds56(vo[4], s1, rot * (64 * 3584), lbase + G_OFF + w * 896, 0);
  glds56(vo[4], s3, rot * (64 * 3584), lbase + G_OFF + w * 896, G3D);
  SCHED();
  PSTAMP(5);
  const int n = (j + NSH) & 7, xn = g & 3, r7 = R7M == 0 ? rot7(n == 7 ? 0 : n, 2 * xn) : R7M == 1 ? (n == 7 ? 0 : n) : 0;
  const srd_t rs = mksrd(p.ws, WS_BYTES);
  const srd_t sw2 = mksrd((const char*)p.w2 + (size_t)w * (224 * 1024) + n * 128, 224 * 1024 - n * 128);
  const srd_t scr = mksrd((const char*)p.cre + (size_t)n * (64 * 1024) + w * 128, 64 * 1024 - w * 128);
  int wb = w;
  asm volatile("" : "+s"(wb));
  if (wb >= 4) {
    asm volatile("" : "+v"(lane));
    phase1<1>(p, L, lbase, rs, sx, s1, s3, sw2, scr, sb, g, j, rot, r7, w, lane, stampv);
    __builtin_amdgcn_endpgm();
  }
  phase1<0>(p, L, lbase, rs, sx, s1, s3, sw2, scr, sb, g, j, rot, r7, w, lane, stampv);
}

extern "C" void kernel_launch(void* const* d_in, const int* in_sizes, int n_in, void* d_out, int out_size, void* d_ws, size_t ws_size,
                              hipStream_t stream) {
  Args p;
  p.x = (const u16*)d_in[0];
  p.lre = (const u16*)d_in[1];
  p.lim = (const u16*)d_in[2];
  p.bre = (const u16*)d_in[3];
  p.bim = (const u16*)d_in[4];
  p.cre = (const u16*)d_in[5];
  p.cim = (const u16*)d_in[6];
  p.ldt = (const u16*)d_in[7];
  p.dsk = (const u16*)d_in[8];
  p.w1 = (const u16*)d_in[10];
  p.w3 = (const u16*)d_in[11];
  p.w2 = (const u16*)d_in[12];
  p.out = (float*)d_out;
  p.ws = (unsigned char*)d_ws;
  s5_glu_fused<<<256, 512, 0, stream>>>(p);
}
```

```cpp
#include <hip/hip_runtime.h>
#include <stdint.h>

typedef unsigned short u16;
typedef unsigned int u32;
typedef unsigned long long u64;
typedef u32 v4u __attribute__((ext_vector_type(4)));
typedef u32 v2u __attribute__((ext_vector_type(2)));
typedef float f4 __attribute__((ext_vector_type(4)));
typedef float f2 __attribute__((ext_vector_type(2)));
typedef short v4s __attribute__((ext_vector_type(4)));
typedef __bf16 v8bf __attribute__((ext_vector_type(8)));
typedef __bf16 v2bf __attribute__((ext_vector_type(2)));
typedef __amdgpu_buffer_rsrc_t srd_t;
#define LAS __attribute__((address_space(3)))
#define GAS __attribute__((address_space(1)))
#define DI __device__ __forceinline__
#define RLX_AGENT __ATOMIC_RELAXED, __HIP_MEMORY_SCOPE_AGENT

#define STAMPS 0
#define WARM 1
#define HAUX 16
#define R7M 1
#define DST 0
#define PSLEEP "4"
#define NSH 0
#define PRE 0
#define TLBW 0
#define CODEPF 28
#define PST 0
#define SPRIO 1
#define BSPLIT 1
#define ROT 3

namespace {
constexpr int SM_OFF = 32768, G_OFF = 36864, G3D = 7184, SLOT = 51456, AUXO = 3 * SLOT;
constexpr int GR = AUXO, CST = AUXO + 2048, TRB = AUXO + 4096, XCH = 49152, DUMMY = AUXO + 9216;
constexpr int LDS_BYTES = 163840;
constexpr int PW = 6144;
constexpr int unitb(int v) { return ((v / 3 + 2) % 3) * SLOT + (v % 3) * 2048; }
#define SH_FL 0
#define SH_AG 0
#define SH_HB 0
constexpr int WS_EP = 0, WS_FL = 4096 + SH_FL, WS_AG = 65536 + SH_AG, WS_SG = 196608, WS_CS = 262144, WS_HB = (1 << 20) + SH_HB, HB_TILE = 88 * 1024;
constexpr int WS_BYTES = WS_HB + 128 * HB_TILE;
constexpr int WS_STAMP = 32 << 20, NSTAMP = 14;
constexpr int AUX_SC1 = 16;
constexpr u32 SPIN_CAP = 1u << 17;
constexpr float NL2E = -1.4426950408889634f;

DI float bf2f(u32 b) { return __builtin_bit_cast(float, b << 16); }
DI float bfhi(u32 b) { return __builtin_bit_cast(float, b & 0xFFFF0000u); }
DI float u2f(u32 b) { return __builtin_bit_cast(float, b); }
DI u32 f2u(float f) { return __builtin_bit_cast(u32, f); }
DI float rbf(float f) { return (float)(__bf16)f; }
DI u32 pk2(f2 a) { return __builtin_bit_cast(u32, __builtin_convertvector(a, v2bf)); }
DI float rcp(float x) { return __builtin_amdgcn_rcpf(x); }
DI float ffma(float a, float b, float c) { return __builtin_fmaf(a, b, c); }
DI f2 fma2(f2 a, f2 b, f2 c) { return __builtin_elementwise_fma(a, b, c); }
DI f2 sp(float a) { return f2{a, a}; }
DI f2 half2(f4 v, int h) { return h ? v.zw : v.xy; }
DI f4 mfma(v4u a, v4u b, f4 c) {
  return __builtin_amdgcn_mfma_f32_16x16x32_bf16(__builtin_bit_cast(v8bf, a), __builtin_bit_cast(v8bf, b), c, 0, 0, 0);
}
DI v4u lds128(LAS char* L, u32 a) { return *(LAS v4u*)(L + a); }
DI f4 ldsf4(LAS char* L, u32 a) { return *(LAS f4*)(L + a); }
DI float ldsf(LAS char* L, u32 a) { return *(LAS float*)(L + a); }
DI v4u tr2(LAS char* L, u32 a, int d) {
  v2u lo = __builtin_bit_cast(v2u, __builtin_amdgcn_ds_read_tr16_b64_v4i16((LAS v4s*)(L + a)));
  v2u hi = __builtin_bit_cast(v2u, __builtin_amdgcn_ds_read_tr16_b64_v4i16((LAS v4s*)(L + a + d)));
  return v4u{lo.x, lo.y, hi.x, hi.y};
}
DI srd_t mksrd(const void* p, int bytes) { return __builtin_amdgcn_make_buffer_rsrc((void*)p, 0, bytes, 0x00020000); }
DI u32 rfl(u32 x) { return __builtin_amdgcn_readfirstlane(x); }
#pragma clang diagnostic ignored "-Winline-asm"
template <int OFF> DI void glds(u32 voff, srd_t srd, u32 soff, u32 la, u32 lb) {
  asm volatile("s_add_u32 m0, %3, %4\n\ts_nop 0\n\tbuffer_load_dwordx4 %0, %1, %2 offen offset:%5 lds" ::"v"(voff), "s"(srd), "s"(soff), "s"(la), "s"(lb),
               "n"(OFF)
               : "memory", "m0", "scc");
}
DI void glds56(u32 voff, srd_t srd, u32 soff, u32 la, u32 lb) {
  asm volatile("s_add_u32 m0, %3, %4\n\ts_mov_b32 exec_hi, 0xffffff\n\tbuffer_load_dwordx4 %0, %1, %2 offen lds\n\ts_mov_b32 exec_hi, -1" ::"v"(voff),
               "s"(srd), "s"(soff), "s"(la), "s"(lb)
               : "memory", "m0", "scc");
}
template <int LB> DI void glds2(u32 ve, u32 vo, srd_t srd, u32 soff, u32 la) {
  asm volatile("s_add_u32 m0, %4, %5\n\ts_nop 0\n\tbuffer_load_dwordx4 %0, %2, %3 offen lds\n\tbuffer_load_dwordx4 %1, %2, %3 offen offset:1024 lds" ::"v"(ve),
               "v"(vo), "s"(srd), "s"(soff), "s"(rfl(la)), "n"(LB)
               : "memory", "m0", "scc");
}
#define WAITV(n) asm volatile("s_waitcnt vmcnt(" #n ")" ::: "memory")
#define WAITL0() do { asm volatile("" ::: "memory"); __builtin_amdgcn_s_waitcnt(0xC07F); asm volatile("" ::: "memory"); } while (0)
#define BAR() __builtin_amdgcn_s_barrier()
#define SCHED() __builtin_amdgcn_sched_barrier(0)
#define SGB(m, n) __builtin_amdgcn_sched_group_barrier(m, n, 0)
#define PIN4(a) asm volatile("" : "+v"((a)[0]), "+v"((a)[1]), "+v"((a)[2]), "+v"((a)[3]))

template <int V> struct IC { static constexpr int value = V; };
template <int B, int E, class F> DI void static_for(F f) {
  if constexpr (B < E) {
    f(IC<B>{});
    static_for<B + 1, E>(f);
  }
}
DI bool allok(bool c) { return __builtin_amdgcn_ballot_w64(!c) == 0; }
struct cf { float r, i; };
DI float afma(float a, float b, float c) { float r; asm("v_fma_f32 %0, %1, %2, %3" : "=v"(r) : "v"(a), "v"(b), "v"(c)); return r; }
DI float anfma(float a, float b, float c) { float r; asm("v_fma_f32 %0, -%1, %2, %3" : "=v"(r) : "v"(a), "v"(b), "v"(c)); return r; }
DI cf cmul(cf a, cf b) { return cf{anfma(a.i, b.i, a.r * b.r), ffma(a.r, b.i, a.i * b.r)}; }
DI cf cfma(cf a, cf b, cf c) { return cf{afma(a.r, b.r, anfma(a.i, b.i, c.r)), ffma(a.r, b.i, ffma(a.i, b.r, c.i))}; }
DI cf csq(cf a) { return cf{anfma(a.i, a.i, a.r * a.r), 2.f * a.r * a.i}; }
DI cf csel(bool p, cf a, cf b) { return cf{p ? a.r : b.r, p ? a.i : b.i}; }
struct cf2 { f2 r, i; };
DI cf2 cfma2(cf a, cf2 b, cf2 c) { return cf2{fma2(sp(a.r), b.r, fma2(sp(-a.i), b.i, c.r)), fma2(sp(a.r), b.i, fma2(sp(a.i), b.r, c.i))}; }
DI cf2 cmul2(cf a, cf2 b) { return cf2{fma2(sp(a.r), b.r, sp(-a.i) * b.i), fma2(sp(a.r), b.i, sp(a.i) * b.r)}; }
template <int CTRL> DI float dpp(float x) { return u2f(__builtin_amdgcn_mov_dpp(f2u(x), CTRL, 0xf, 0xf, true)); }
DI float sum16(float v) {
  v += dpp<0xB1>(v); v += dpp<0x4E>(v); v += dpp<0x141>(v); v += dpp<0x140>(v);
  return v;
}
DI void sw16(u32& a, u32& b) { auto r = __builtin_amdgcn_permlane16_swap(a, b, false, false); a = r[0]; b = r[1]; }
DI void sw32(u32& a, u32& b) { auto r = __builtin_amdgcn_permlane32_swap(a, b, false, false); a = r[0]; b = r[1]; }
#define SWPAD2(a, b) asm volatile("s_nop 1" : "+v"(a), "+v"(b))
#define SWPAD4(a, b, c, d) asm volatile("s_nop 1" : "+v"(a), "+v"(b), "+v"(c), "+v"(d))
DI float sumx16(float v) { u32 a = f2u(v), b = a; sw16(a, b); SWPAD2(a, b); return u2f(a) + u2f(b); }
DI float sumx32(float v) { u32 a = f2u(v), b = a; sw32(a, b); SWPAD2(a, b); return u2f(a) + u2f(b); }
DI void bcast4(float v, float (&o)[4]) {
  u32 a = f2u(v), b = a;
  sw16(a, b);
  SWPAD2(a, b);
  u32 a2 = a, b2 = b;
  sw32(a, a2);
  sw32(b, b2);
  SWPAD4(a, b, a2, b2);
  o[0] = u2f(a); o[1] = u2f(b); o[2] = u2f(a2); o[3] = u2f(b2);
}
DI v4u swap16x2(u32 ax, u32 ay, u32 bx, u32 by) {
  sw16(ax, bx);
  sw16(ay, by);
  v4u o = {ax, ay, bx, by};
  asm volatile("s_nop 1" : "+v"(o));
  return o;
}

struct Args {
  const u16 *x, *lre, *lim, *bre, *bim, *cre, *cim, *ldt, *dsk, *w1, *w3, *w2;
  float* out;
  unsigned char* ws;
};

#define STAMP(k) do { } while (0)
#define CSTAMP(k) do { } while (0)
#define DSTAMP(k) do { } while (0)
#define PSTAMP(k) do { } while (0)

DI u32 swiglu2(f2 tg, f2 tu, float nrl, float ir2) {
  const f2 a = tg * sp(nrl);
  const f2 e = {__builtin_amdgcn_exp2f(a.x), __builtin_amdgcn_exp2f(a.y)};
  const f2 d = fma2(e, sp(ir2), sp(ir2));
  const f2 r = {rcp(d.x), rcp(d.y)};
  return pk2(tg * tu * r);
}

template <int H> struct Fr { v4u x[4]; v4u w[H == 0 ? 5 : 4]; };
struct Ra { u32 xa, sa, ga, gm; };

template <int H> DI void ldpiece(Fr<H>& F, LAS char* L, const Ra& a, int k) {
  if (k == 0) { F.x[0] = lds128(L, a.xa); F.x[1] = lds128(L, a.xa + 256); F.x[2] = lds128(L, a.xa + 512); }
  if (H == 0) {
    if (k == 1) { F.x[3] = lds128(L, a.xa + 768); F.w[0] = lds128(L, a.sa); F.w[1] = lds128(L, a.sa + 2048); }
    if (k == 2) { F.w[2] = tr2(L, a.ga, 112); F.w[3] = tr2(L, a.ga + G3D, 112); }
    if (k == 3) F.w[4] = tr2(L, a.gm, 112);
  } else {
    if (k == 0) F.x[3] = lds128(L, a.xa + 768);
    if (k == 1) { F.w[0] = tr2(L, a.ga + 32, 112); F.w[1] = tr2(L, a.ga + G3D + 32, 112); }
    if (k == 2) { F.w[2] = tr2(L, a.ga + 64, 112); F.w[3] = tr2(L, a.ga + G3D + 64, 112); }
  }
}
template <int H> DI void mmtile(const Fr<H>& F, f4 (&acc)[4], int t) {
#pragma unroll
  for (int i = 0; i < 4; i++) {
    if (H == 0 && t < 2) acc[i] = mfma(F.x[i], F.w[t], acc[i]);
    else acc[i] = mfma(F.w[t], F.x[i], acc[i]);
  }
}
template <int H, class I>
DI void half(const Fr<H>& Fc, Fr<H>& Fn, LAS char* L, const Ra& a, f4 (&acc)[H == 0 ? 5 : 4][4], I iss) {
  constexpr int NT = H == 0 ? 5 : 4;
#pragma unroll
  for (int t = 0; t < NT; t++) {
    ldpiece<H>(Fn, L, a, t);
    mmtile<H>(Fc, acc[t], t);
#pragma unroll
    for (int k = 0; k < 4; k++) { SGB(0x8, 1); SGB(0x100, 1); }
    PIN4(acc[t]);
    SCHED();
    iss(t);
    SCHED();
  }
}

DI int rot7(int s, int r) { const int k = s + r; return k >= 7 ? k - 7 : k; }
DI u32 p2_pair(int r, int spn) { return (2 * rot7(spn >> 1, r) + (spn & 1)) * 16384; }
constexpr int cunit(int m, int nn) { return m == 0 ? (nn == 0 ? 5 : nn == 3 ? 6 : nn - 1) : (nn == 0 ? 7 : 1 + nn); }

struct Sa {
  cf2 u[8];
  cf a1, a8, a16, a64, pq, lw, e0, c2, cw, wt, wg0, wg1;
};
struct Pw {
  u32 vwe, vwo, vce, vco, tl[4];
};
DI Pw pw_setup(int lane, int w, int xn) {
  Pw z;
  const int q = lane >> 4, r4 = (lane & 15) >> 2, pp = lane & 3;
  z.vwe = (lane >> 3) * 1024 + (((lane & 7) ^ (2 * (q & 1))) << 4);
  z.vwo = (z.vwe ^ 64) + 8192 - 1024;
  z.vce = (lane >> 3) * 1024 + (((lane & 7) ^ q) << 4);
  z.vco = (z.vce ^ 64) + 8192 - 1024;
#pragma unroll
  for (int nn = 0; nn < 4; nn++)
    z.tl[nn] = w * PW + (q & 1) * 1024 + r4 * 128 + ((((2 * (nn ^ xn)) ^ (2 * (((r4 >> 1) & 1) + 2 * (q & 1)))) + (pp >> 1)) << 4) + (pp & 1) * 8;
  return z;
}
DI void rdw(v4u (&wf)[4], LAS char* L, const Pw& z, int q, int ua, int ub) {
  const u32 o = (q >> 1) ? unitb(ub) : unitb(ua);
#pragma unroll
  for (int nn = 0; nn < 4; nn++) wf[nn] = tr2(L, o + z.tl[nn], 512);
}

#define HW 8
constexpr int HI0 = HW, HCHK = 28 - HW, HFL = HCHK - 6;
#define HG 1
constexpr int hiss(int m) {
  const int n = m < HCHK ? m + 1 + HW : 28 + HG * (m - HCHK + 1);
  return n < 44 ? n : 44;
}
constexpr int ncopy(int m) { return 0; }
constexpr int nother(int m) { return ncopy(m); }
constexpr int issued_upto(int m) {
  int n = hiss(m);
  for (int k = 0; k <= m; k++) n += nother(k);
  return n;
}
constexpr int pos_h(int m) {
  int n = m;
  for (int k = 0; k < 44; k++) if (hiss(k) <= m) n += nother(k);
  return n;
}
static_assert(hiss(HCHK - 1) == 28, "schedule");

typedef u32 s16u __attribute__((ext_vector_type(16)));
typedef u32 s8u __attribute__((ext_vector_type(8)));
template <class V, int N> DI bool flags_match(V v, u32 tagLo, u32 tagHi) {
  u32 x = 0;
#pragma unroll
  for (int k = 0; k < N; k += 2) x |= (v[k] ^ tagLo) | (v[k + 1] ^ tagHi);
  return x == 0;
}
DI bool flags_vec(srd_t rs, u32 l16, u32 so, u32 tagLo, u32 tagHi, u32 amask) {
  v2u v;
  asm volatile("buffer_load_dwordx2 %0, %1, %2, %3 offen sc1\n\ts_waitcnt vmcnt(0)" : "=&v"(v) : "v"((l16 >> 1) & amask), "s"(rs), "s"(so) : "memory");
  return allok(v.x == tagLo && v.y == tagHi);
}
DI void glu_read(s16u& v, const unsigned char* fp) { asm volatile("s_load_dwordx16 %0, %1, 0x0 glc" : "=&s"(v) : "s"(fp) : "memory"); }
DI void poll_glu(s16u v, const unsigned char* fp, srd_t rs, u32 l16, u32 so, u32 tagLo, u32 tagHi) {
#pragma unroll 1
  for (u32 spins = 1; spins < SPIN_CAP; spins++) {
    if (flags_match<s16u, 16>(v, tagLo, tagHi)) break;
    if ((spins & 7) == 0 && flags_vec(rs, l16, so, tagLo, tagHi, 0x38)) break;
    asm volatile("s_load_dwordx16 %0, %1, 0x0 glc\n\ts_waitcnt lgkmcnt(0)" : "=&s"(v) : "s"(fp) : "memory");
  }
  asm volatile("" ::: "memory");
}
DI void ssm_read(s8u& v, const unsigned char* fp) { asm volatile("s_load_dwordx8 %0, %1, 0x40 glc" : "=&s"(v) : "s"(fp) : "memory"); }
DI void poll_ssm(s8u v, const unsigned char* fp, srd_t rs, u32 l16, u32 so, u32 tagLo, u32 tagHi) {
#pragma unroll 1
  for (u32 spins = 1; spins < SPIN_CAP; spins++) {
    if (flags_match<s8u, 8>(v, tagLo, tagHi)) break;
    if ((spins & 7) == 0 && flags_vec(rs, l16, so + 64, tagLo, tagHi, 0x18)) break;
    __builtin_amdgcn_s_sleep(2);
    asm volatile("s_load_dwordx8 %0, %1, 0x40 glc\n\ts_waitcnt lgkmcnt(0)" : "=&s"(v) : "s"(fp) : "memory");
  }
  asm volatile("" ::: "memory");
}

DI void stage_offsets(int lane, int w, u32 (&vo)[5], u32& vs) {
#pragma unroll
  for (int e = 0; e < 4; e++) vo[e] = (32 * w + 8 * e + (lane >> 3)) * 1024 + (((lane & 7) ^ ((4 * w + e) & 7)) << 4) - e * 1024;
  const int gpos = (lane * 37) >> 8, gch = lane - gpos * 7;
  vo[4] = (8 * w + 4 * (gpos & 1) + (gpos >> 1)) * 3584 + gch * 16;
  vs = (8 * (w & 1) + (lane >> 3)) * 1024 + (((lane & 7) ^ ((4 * (w & 1) + (lane >> 4)) & 7)) << 4);
}

template <int H>
DI void phase1(const Args& p, LAS char* L, u32 lbase, srd_t rs, srd_t sx, srd_t s1, srd_t s3, srd_t sw2, srd_t scr, srd_t sb, int g, int j, int rot, int r7, int w,
               int lane, u32 stampv) {
  constexpr int NT = H == 0 ? 5 : 4;
  u32 vo[5], vs;
  stage_offsets(lane, w, vo, vs);
  Sa sa;
  v4u wf[7][4];
  const int tg = w & 3, c = lane & 15, q = lane >> 4, r4 = c >> 2, pp = lane & 3;
  const u32 ld0 = lbase + w * 4096, ld4 = lbase + G_OFF + w * 896, ld5 = ld4 + G3D, lds_s = lbase + SM_OFF + w * 1024;
  auto op = [&](int i, u32 kk, u32 slot) {
    if (i == 0) glds<0>(vo[0], sx, kk * 128, ld0, slot);
    else if (i == 1) glds<1024>(vo[1], sx, kk * 128, ld0, slot);
    else if (i == 2) glds<2048>(vo[2], sx, kk * 128, ld0, slot);
    else if (i == 3) glds<3072>(vo[3], sx, kk * 128, ld0, slot);
    else if (i == 4) glds56(vo[4], s1, kk * (64 * 3584), ld4, slot);
    else if (i == 5) glds56(vo[4], s3, kk * (64 * 3584), ld5, slot);
    else if (H == 0) glds<0>(vs, sb, kk * 128, lds_s, slot);
  };
  v2u ep;
  u32 pl0 = 0, pl1 = 0, pl2 = 0;
  v4u xr0 = {0, 0, 0, 0}, xr1 = {0, 0, 0, 0};
  {
    const u32 zero = 0;
    asm volatile("global_load_dwordx2 %0, %1, %2 sc1" : "=v"(ep) : "v"(zero), "s"(p.ws + WS_EP) : "memory");
    if (H == 0) {
      const u32 o = (16 * j + c) * 2;
      asm volatile("global_load_ushort %0, %1, %2" : "=v"(pl0) : "v"(o), "s"(p.lre) : "memory");
      asm volatile("global_load_ushort %0, %1, %2" : "=v"(pl1) : "v"(o), "s"(p.lim) : "memory");
      asm volatile("global_load_ushort %0, %1, %2" : "=v"(pl2) : "v"(o), "s"(p.ldt) : "memory");
      const u32 ro = (8 * j + 2 * tg + (lane >> 5)) * 1024 + (lane & 31) * 16;
      asm volatile("buffer_load_dwordx4 %0, %1, %2, 0 offen" : "=v"(xr0) : "v"(ro), "s"(sx) : "memory");
      asm volatile("buffer_load_dwordx4 %0, %1, %2, 0 offen offset:512" : "=v"(xr1) : "v"(ro), "s"(sx) : "memory");
    }
  }
  u32 tagLo = 0, tagHi = 0;
  if (H == 0) {
    asm volatile("s_waitcnt vmcnt(0)" : "+v"(ep), "+v"(pl0), "+v"(pl1), "+v"(pl2), "+v"(xr0), "+v"(xr1)::"memory");
    PSTAMP(6);
    const u64 tg64 = (((u64)rfl(ep.y) << 32) | rfl(ep.x)) + 1;
    tagLo = (u32)tg64; tagHi = (u32)(tg64 >> 32);
    f2 v[8];
#pragma unroll
    for (int k = 0; k < 4; k++) { v[k] = f2{bf2f(xr0[k]), bfhi(xr0[k])}; v[4 + k] = f2{bf2f(xr1[k]), bfhi(xr1[k])}; }
    const f2 sm = ((v[0] + v[1]) + (v[2] + v[3])) + ((v[4] + v[5]) + (v[6] + v[7]));
    const float m = sumx16(sum16(sm.x + sm.y)) * (1.f / 512.f);
    f2 s2 = {0.f, 0.f};
#pragma unroll
    for (int k = 0; k < 8; k++) { const f2 d = v[k] - sp(m); s2 = fma2(d, d, s2); }
    const float var = sumx16(sum16(s2.x + s2.y)) * (1.f / 512.f);
    const float sd = rbf(__builtin_amdgcn_sqrtf(rbf(rbf(var) + 1e-5f)));
    const v4u o = {f2u(rbf(m)), tagLo, f2u(rcp(sd)), tagHi};
    __builtin_amdgcn_raw_buffer_store_b128(o, rs, (lane & 31) ? 0x7FFFFFF0 : WS_SG + (g * 256 + 8 * j + 2 * tg + (lane >> 5)) * 16, 0, AUX_SC1);
    SCHED();
  }
  PSTAMP(8);
  op(6, (rot + 1) & 7, SLOT);
#pragma unroll
  for (int i = 0; i < 6; i++) op(i, (rot + 1) & 7, SLOT);
  if (H == 0) {
    const float lamr = bf2f(pl0), lami = bf2f(pl1), dt = rbf(expf(bf2f(pl2)));
    float sn, cn;
    sincosf(dt * lami, &sn, &cn);
    const float e = expf(dt * lamr);
    sa.a1 = cf{e * cn, e * sn};
    sa.a8 = csq(csq(csq(sa.a1)));
    sa.a16 = csq(sa.a8);
    sa.a64 = csq(csq(sa.a16));
    const float nr = sa.a1.r - 1.f, ni = sa.a1.i, den = 2.f / (lamr * lamr + lami * lami);
    sa.c2 = cf{(nr * lamr + ni * lami) * den, (ni * lamr - nr * lami) * den};
    asm volatile("" : "+v"(sa.a1.r), "+v"(sa.a1.i), "+v"(sa.a8.r), "+v"(sa.a8.i), "+v"(sa.a16.r), "+v"(sa.a16.i), "+v"(sa.a64.r), "+v"(sa.a64.i),
                 "+v"(sa.c2.r), "+v"(sa.c2.i));
    SCHED();
  }
  const u32 xa0 = (64 * tg + 16 * r4 + 8 * (c & 1) + ((c >> 1) & 1)) * 128 + ((q ^ (2 * r4 + (c & 1))) << 4);
  const u32 sa0 = SM_OFF + c * 128 + ((q ^ ((c >> 1) & 7)) << 4);
  const u32 ga0 = G_OFF + q * 896 + r4 * 224 + 8 * pp;
  const u32 gm0 = ga0 + 96 + (pp >= 2 ? G3D - 16 : 0);
  auto ra = [&](u32 slot, int ks) { return Ra{slot + (xa0 ^ (ks * 64)), slot + (sa0 ^ (ks * 64)), slot + ga0 + ks * 3584, slot + gm0 + ks * 3584}; };
  f4 acc[NT][4];
#pragma unroll
  for (int t = 0; t < NT; t++)
#pragma unroll
    for (int i = 0; i < 4; i++) acc[t][i] = f4{0, 0, 0, 0};
  PSTAMP(10);
  if (H == 1) WAITV(7);
  PSTAMP(11);
  BAR();
  STAMP(2);
  CSTAMP(14);
  Fr<H> F0, F1;
  {
    const Ra a = ra(0, 0);
#pragma unroll
    for (int t = 0; t < NT; t++) ldpiece<H>(F0, L, a, t);
    SCHED();
  }
  half<H>(F0, F1, L, ra(0, 1), acc, [&](int t) {
    if (t == 0) op(6, (rot + 2) & 7, 2 * SLOT);
    if (t < 3) { op(2 * t, (rot + 2) & 7, 2 * SLOT); op(2 * t + 1, (rot + 2) & 7, 2 * SLOT); }
  });
  if (H == 1) {
    asm volatile("s_waitcnt vmcnt(12)" : "+v"(ep)::"memory");
    const u64 tg64 = (((u64)rfl(ep.y) << 32) | rfl(ep.x)) + 1;
    tagLo = (u32)tg64; tagHi = (u32)(tg64 >> 32);
  }
  if (H == 0 && tg < 3) {
    const int b = g * 32 + j;
    const char* src = tg == 0 ? (const char*)p.w2 + b * 7168 : (const char*)(tg == 1 ? p.cre : p.cim) + b * 2048;
    const u32 off = (tg == 0 ? (lane < 56 ? lane : 0) : (lane & 15)) * 128;
    asm volatile("s_mov_b32 m0, %2\n\ts_nop 0\n\tglobal_load_lds_dword %0, %1" ::"v"(off), "s"(src), "s"(lbase + DUMMY) : "memory", "m0");
  }
  const u64 tag = ((u64)tagHi << 32) | tagLo;
  u32 cur = 0, nxt = SLOT;
  SCHED();
#pragma unroll 1
  for (int s = 0; s < 5; s++) {
    const Ra an0 = ra(nxt, 0);
    const u32 kk = (rot + s + 3) & 7;
    WAITL0();
    DSTAMP(16 + 3 * s);
    WAITV(6);
    DSTAMP(17 + 3 * s);
    BAR();
    DSTAMP(18 + 3 * s);
    SCHED();
    half<H>(F1, F0, L, an0, acc, [&](int t) {
      if (H == 0) {
        if (t == 0) op(6, kk, cur);
        else if (t == 1) op(0, kk, cur);
        else if (t == 3) op(4, kk, cur);
        else if (t == 4) op(1, kk, cur);
      } else {
        if (t == 0) op(4, kk, cur);
        else if (t == 2) op(0, kk, cur);
        else if (t == 3) op(1, kk, cur);
      }
    });
    half<H>(F0, F1, L, ra(nxt, 1), acc, [&](int t) {
      if (H == 0) {
        if (t == 0) op(2, kk, cur);
        else if (t == 2) op(5, kk, cur);
        else if (t == 3) op(3, kk, cur);
      } else {
        if (t == 0) op(5, kk, cur);
        else if (t == 1) op(2, kk, cur);
        else if (t == 2) op(3, kk, cur);
      }
    });
    if (H == 0) {
      if (s == (g >> 2)) {
        const v4u ones = {0x3F803F80u, 0x3F803F80u, 0x3F803F80u, 0x3F803F80u};
        const u32 a1 = nxt + (tg < 3 ? ga0 + 32 * tg : gm0), a2 = tg < 3 ? a1 + G3D : a1, a3 = nxt + (tg & 1) * 2048;
        const v4u f0 = tr2(L, a1, 112), f1 = tr2(L, a1 + 3584, 112), f2_ = tr2(L, a2, 112), f3 = tr2(L, a2 + 3584, 112);
        const v4u f4_ = lds128(L, a3 + sa0), f5 = lds128(L, a3 + (sa0 ^ 64));
        const f4 z4 = {0, 0, 0, 0};
        const f4 r1 = mfma(ones, f1, mfma(ones, f0, z4)), r2 = mfma(ones, f3, mfma(ones, f2_, z4)), r3 = mfma(ones, f5, mfma(ones, f4_, z4));
        const int t1 = tg == 0 ? 2 : tg == 3 ? 4 : 3 + 2 * tg;
        const int T = q == 0 ? t1 : q == 1 ? t1 + 1 : (tg & 1);
        const bool on = q == 0 || (q == 1 && tg < 3) || (q == 2 && tg < 2);
        const float v = q == 0 ? r1[0] : q == 1 ? r2[0] : r3[0];
        __builtin_amdgcn_raw_buffer_store_b64(v2u{f2u(v), tagLo}, rs, on ? WS_CS + ((j * 144 + T * 16 + c) * 8 + g) * 8 : 0x7FFFFFF0, 0, AUX_SC1);
      }
      SCHED();
    }
    cur = nxt;
    nxt = nxt == 2 * SLOT ? 0 : nxt + SLOT;
  }
  const Pw z = pw_setup(lane, w, g & 3);
  const u32 lw2 = lbase + w * PW;
  v4u gr, gc[3];
  const u32 gso = WS_SG + (g * 256 + 64 * tg) * 16, cso = WS_CS + (j * 9 + tg) * 1024, cso8 = WS_CS + (j * 9 + 8) * 1024;
#define GATHER(PRE_, POST_)                                                                                                              \
  asm volatile(PRE_ "buffer_load_dwordx4 %0, %4, %5, %6 offen sc1\n\tbuffer_load_dwordx4 %1, %4, %5, %7 offen sc1\n\t"                    \
                    "buffer_load_dwordx4 %2, %4, %5, %8 offen sc1\n\tbuffer_load_dwordx4 %3, %4, %5, %9 offen sc1" POST_                  \
               : "=&v"(gr), "=&v"(gc[0]), "=&v"(gc[1]), "=&v"(gc[2])                                                                     \
               : "v"(lane * 16), "s"(rs), "s"(gso), "s"(cso), "s"(cso + 4096), "s"(cso8)                                                 \
               : "memory")
  float mcol[4], rcol[4];
  f4 mrow[4], rrow[4], cs[NT];
  const u32 gra = GR + tg * 256 + (16 * r4 + (c & 3)) * 4, grr = GR + tg * 256 + 64 * q;
  auto w2p = [&](auto P, auto U) { glds2<unitb(U.value)>(z.vwe, z.vwo, sw2, p2_pair(r7, P.value), lw2); };
#define W2P(p_, u_) w2p(IC<p_>{}, IC<u_>{})
  WAITL0();
  DSTAMP(31);
  WAITV(6);
  DSTAMP(32);
  BAR();
  DSTAMP(33);
  SCHED();
  half<H>(F1, F0, L, ra(0, 0), acc, [&](int t) {
    if (H == 0 && t == 0) GATHER("", "");
    if (t == 1) W2P(0, 0);
  });
  half<H>(F0, F1, L, ra(0, 1), acc, [&](int t) {
    if (t == 0) W2P(1, 1);
    if (t == 2) W2P(2, 2);
  });
  if (H == 0) {
    asm volatile("s_waitcnt vmcnt(6)" : "+v"(gr), "+v"(gc[0]), "+v"(gc[1]), "+v"(gc[2])::"memory");
#pragma unroll 1
    for (u32 spins = 0; spins < SPIN_CAP; spins++) {
      const u32 bad = (gr.y ^ tagLo) | (gr.w ^ tagHi) | (gc[0].y ^ tagLo) | (gc[0].w ^ tagLo) | (gc[1].y ^ tagLo) | (gc[1].w ^ tagLo) |
                      (gc[2].y ^ tagLo) | (gc[2].w ^ tagLo);
      if (allok(bad == 0)) break;
      GATHER("s_sleep 1\n\t", "\n\ts_waitcnt vmcnt(0)");
    }
    const u32 pos = GR + tg * 256 + ((lane & 48) + 4 * ((lane >> 1) & 3) + 2 * (lane & 1) + ((lane >> 3) & 1)) * 4;
    *(LAS u32*)(L + pos) = gr.x;
    *(LAS u32*)(L + pos + 1024) = gr.z;
    SCHED();
  }
  WAITL0();
  DSTAMP(34);
  WAITV(6);
  DSTAMP(35);
  BAR();
  DSTAMP(36);
  SCHED();
  half<H>(F1, F0, L, ra(SLOT, 0), acc, [&](int t) {
    if (t == 0) W2P(3, 3);
    if (t == 2) W2P(4, 4);
  });
  half<H>(F0, F1, L, ra(SLOT, 1), acc, [&](int t) {
    if (t == 0) W2P(5, 5);
    if (t == 3) { WAITV(8); rdw(wf[0], L, z, q, 0, 1); }
  });
  if (H == 0) {
#pragma unroll
    for (int k = 0; k < 3; k++) {
      float v = u2f(gc[k].x) + u2f(gc[k].z);
      v += dpp<0xB1>(v);
      v += dpp<0x4E>(v);
      *(LAS float*)(L + CST + (k < 2 ? tg + 4 * k : 8) * 64 + (lane >> 2) * 4) = v;
    }
    SCHED();
  }
  WAITL0();
  DSTAMP(37);
  BAR();
  DSTAMP(39);
#pragma unroll
  for (int t = 0; t < NT; t++) {
    if (H == 0 && t < 2) cs[t][0] = ldsf(L, CST + 64 * t + c * 4);
    else cs[t] = ldsf4(L, CST + (H == 0 ? 0 : 320) + 64 * t + 16 * q);
    if (t < 4) {
      mcol[t] = ldsf(L, gra + 16 * t);
      rcol[t] = ldsf(L, gra + 16 * t + 1024);
      if (H == 0) { mrow[t] = ldsf4(L, grr + 16 * t); rrow[t] = ldsf4(L, grr + 16 * t + 1024); }
    }
    mmtile<H>(F1, acc[t], t);
#pragma unroll
    for (int k = 0; k < 4; k++) { SGB(0x8, 1); SGB(0x100, 2); }
    PIN4(acc[t]);
    SCHED();
    if (t == 0) { W2P(6, 0); W2P(7, 1); }
    if (t == 1) { W2P(8, 6); W2P(9, 7); }
    if (t == 2) W2P(10, 8);
    if (t == NT - 1) { WAITV(10); rdw(wf[1], L, z, q, 2, 3); rdw(wf[2], L, z, q, 4, 5); }
    SCHED();
  }
  STAMP(3);
  CSTAMP(15);
  __builtin_amdgcn_s_setprio(3);
  const int oct0 = 44 * (j >> 2) + 16 + 7 * (j & 3);
  const u32 hbs = WS_HB + (g * 4 + tg) * 4 * HB_TILE + oct0 * 256;
  const int u = j >> 3, n = (j + NSH) & 7, xn = g & 3;
  const u32 wo = w * PW;
  const srd_t sci = mksrd((const char*)p.cim + (size_t)n * (64 * 1024) + w * 128, 64 * 1024 - w * 128);
  const u32 fso = WS_FL + ((g * 4 + u) * 32 + 4 * w) * 32, fmine = WS_FL + ((g * 4 + tg) * 8 + (j >> 2)) * 128;
  u32 l16 = lane * 16;
  auto cp = [&](auto K) { glds2<unitb(cunit(K.value >> 2, K.value & 3))>(z.vce, z.vco, K.value < 4 ? scr : sci, ((K.value & 3) ^ xn) * 16384, lw2); };
#define CP(k_) cp(IC<k_>{})
  auto setflag = [&](int k) { __builtin_amdgcn_raw_buffer_store_b64(v2u{tagLo, tagHi}, rs, l16 ? 0x7FFFFFF0 : fmine + (k < 2 ? 2 * (j & 3) + k : 8 + (j & 3)) * 8, 0, AUX_SC1); };
  v2u xv[2], dk;
  auto ldx = [&]() {
    const int dcol = 64 * n + 16 * (r4 ^ xn) + 4 * pp;
    const u32 oo = (64 * u + 16 * tg + 8 * (q & 1) + 2 * H + (q >> 1)) * 512 + dcol;
    asm volatile("buffer_load_dwordx2 %0, %3, %4, 0 offen\n\tbuffer_load_dwordx2 %1, %3, %4, %7 offen\n\tglobal_load_dwordx2 %2, %5, %6"
                 : "=&v"(xv[0]), "=&v"(xv[1]), "=&v"(dk)
                 : "v"(oo * 2), "s"(sx), "v"(dcol * 2), "s"(p.dsk), "s"(4096)
                 : "memory");
  };
  if (H == 1) {
    const u32 hv = c * 16 + (2 + (q >> 1) + 2 * (q & 1)) * 256 + hbs;
    v4u o[4];
    WAITL0();
    W2P(11, 2); W2P(12, 3);
    SCHED();
#pragma unroll
    for (int i = 0; i < 4; i++) {
      const f2 nm = sp(-mcol[i]);
      const float nrl = rcol[i] * NL2E, ir2 = rcp(rcol[i] * rcol[i]);
      u32 v[4];
#pragma unroll
      for (int k = 0; k < 4; k++) {
        const int t = 2 * (k >> 1), h = k & 1;
        v[k] = swiglu2(fma2(nm, half2(cs[t], h), half2(acc[t][i], h)), fma2(nm, half2(cs[t + 1], h), half2(acc[t + 1][i], h)), nrl, ir2);
      }
      o[i] = swap16x2(v[0], v[1], v[2], v[3]);
      SCHED();
      if (i == 0) {
        CSTAMP(18);
        BAR();
        CSTAMP(19);
        W2P(13, 4); CP(0);
      } else {
        if (i == 1) __builtin_amdgcn_raw_buffer_store_b128(o[0], rs, hv, 0, AUX_SC1);
        __builtin_amdgcn_raw_buffer_store_b128(o[i], rs, hv + i * HB_TILE, 0, AUX_SC1);
      }
      if (i == 1) {
        WAITV(12);
        rdw(wf[3], L, z, q, 0, 1); rdw(wf[4], L, z, q, 6, 7);
      }
      if (i == 2) { WAITL0(); CP(1); CP(2); CP(3); CP(4); }
      SCHED();
    }
    STAMP(4);
    ldx();
    WAITV(17);
    rdw(wf[5], L, z, q, 8, 2); rdw(wf[6], L, z, q, 3, 4);
    WAITL0();
    CP(5); CP(6); CP(7);
    CSTAMP(28);
    asm volatile("s_waitcnt vmcnt(6)" : "+v"(xv[0]), "+v"(xv[1]), "+v"(dk)::"memory");
    setflag(1);
    STAMP(5);
  } else {
#pragma unroll
    for (int k = 0; k < 8; k++) {
      const f2 m2 = half2(mrow[k >> 1], k & 1), r2v = half2(rrow[k >> 1], k & 1);
      sa.u[k].r = fma2(-m2, sp(cs[0][0]), half2(acc[0][k >> 1], k & 1)) * r2v;
      sa.u[k].i = fma2(-m2, sp(cs[1][0]), half2(acc[1][k >> 1], k & 1)) * r2v;
    }
    cf2 e2 = sa.u[0];
#pragma unroll
    for (int k = 1; k < 8; k++) e2 = cfma2(sa.a1, e2, sa.u[k]);
    {
      sa.e0 = cf{e2.r.x, e2.i.x};
      const cf e = cfma(sa.a8, sa.e0, cf{e2.r.y, e2.i.y});
      float er[4], ei[4];
      bcast4(e.r, er);
      bcast4(e.i, ei);
      const cf i0{er[0], ei[0]};
      const cf i1 = cfma(sa.a16, i0, cf{er[1], ei[1]});
      const cf i2 = cfma(sa.a16, i1, cf{er[2], ei[2]});
      const cf i3 = cfma(sa.a16, i2, cf{er[3], ei[3]});
      *(LAS v2u*)(L + XCH + (c * 4 + tg) * 8) = v2u{f2u(i3.r), f2u(i3.i)};
      sa.pq = csel(q & 2, csel(q & 1, i2, i1), csel(q & 1, i0, cf{0.f, 0.f}));
    }
    SCHED();
    CSTAMP(18);
    WAITL0();
    BAR();
    CSTAMP(19);
    {
      const f4 w01 = ldsf4(L, XCH + c * 32), w23 = ldsf4(L, XCH + c * 32 + 16);
      const cf p0{w01.x, w01.y};
      const cf p1 = cfma(sa.a64, p0, cf{w01.z, w01.w});
      const cf p2 = cfma(sa.a64, p1, cf{w23.x, w23.y});
      const cf p3 = cfma(sa.a64, p2, cf{w23.z, w23.w});
      const v4u o = {f2u(p3.r), tagLo, f2u(p3.i), tagHi};
      __builtin_amdgcn_raw_buffer_store_b128(o, rs, (q | tg) ? 0x7FFFFFF0 : WS_AG + (g * 512 + 16 * j + c) * 16, 0, AUX_SC1);
      sa.lw = csel(tg & 2, csel(tg & 1, p2, p1), csel(tg & 1, p0, cf{0.f, 0.f}));
    }
    SCHED();
    STAMP(8);
    W2P(11, 2); W2P(12, 3); W2P(13, 4); CP(0);
    SCHED();
    u32 zx[2], zy[2];
#pragma unroll
    for (int ip = 0; ip < 2; ip++) {
      u32 px[2], py[2];
      f4 tm[2];
#pragma unroll
      for (int ii = 0; ii < 2; ii++) {
        const int i = 2 * ip + ii;
        const f2 nm = sp(-mcol[i]);
        const float nrl = rcol[i] * NL2E, ir2 = rcp(rcol[i] * rcol[i]);
        px[ii] = swiglu2(fma2(nm, cs[2].xy, acc[2][i].xy), fma2(nm, cs[3].xy, acc[3][i].xy), nrl, ir2);
        py[ii] = swiglu2(fma2(nm, cs[2].zw, acc[2][i].zw), fma2(nm, cs[3].zw, acc[3][i].zw), nrl, ir2);
        tm[ii].xy = fma2(nm, cs[4].xy, acc[4][i].xy);
        tm[ii].zw = fma2(nm, cs[4].zw, acc[4][i].zw);
      }
      __builtin_amdgcn_raw_buffer_store_b128(swap16x2(px[0], py[0], px[1], py[1]), rs, c * 16 + (q & 1) * HB_TILE + (q >> 1) * 256 + hbs + 2 * ip * HB_TILE, 0,
                                             AUX_SC1);
      SCHED();
      if (ip == 0) {
        WAITV(12);
        rdw(wf[3], L, z, q, 0, 1); rdw(wf[4], L, z, q, 6, 7);
        SCHED();
      }
      float rlo = rcol[2 * ip];
      asm volatile("" : "+v"(rlo));
      const float rsel = q < 2 ? rlo : rcol[2 * ip + 1];
      const float nrl = rsel * NL2E, ir2 = rcp(rsel * rsel);
      f4 gm, um;
#pragma unroll
      for (int r = 0; r < 4; r++) {
        u32 a = f2u(tm[0][r]), b = f2u(tm[1][r]);
        sw32(a, b);
        gm[r] = u2f(a);
        um[r] = u2f(b);
      }
      SWPAD2(gm, um);
      zx[ip] = swiglu2(gm.xy, um.xy, nrl, ir2);
      zy[ip] = swiglu2(gm.zw, um.zw, nrl, ir2);
    }
    __builtin_amdgcn_raw_buffer_store_b128(swap16x2(zx[0], zy[0], zx[1], zy[1]), rs, c * 16 + (2 * (q & 1) + (q >> 1)) * HB_TILE + hbs + 6 * 256, 0, AUX_SC1);
    STAMP(4);
    ldx();
    const u32 ao = WS_AG + (q * 512 + 16 * j + c) * 16;
    v4u g0, g1;
    asm volatile("buffer_load_dwordx4 %0, %2, %3, 0 offen sc1\n\tbuffer_load_dwordx4 %1, %2, %3, %4 offen sc1"
                 : "=&v"(g0), "=&v"(g1)
                 : "v"(ao), "s"(rs), "s"(4 * 512 * 16)
                 : "memory");
    const cf one{1.f, 0.f}, zero{0.f, 0.f};
    asm volatile("" : "+v"(sa.a16.r), "+v"(sa.a16.i), "+v"(sa.a64.r), "+v"(sa.a64.i));
    {
      const cf a128 = csq(sa.a64), a256 = csq(a128), a512 = csq(a256), a1024 = csq(a512);
      sa.cw = cmul(sa.c2, cmul(csel(q & 1, sa.a16, one), csel(q & 2, csq(sa.a16), one)));
      sa.wt = cmul(sa.cw, cmul(csel(tg & 1, sa.a64, one), csel(tg & 2, a128, one)));
      const int e0 = g - 1 - q, e1 = g - 5 - q;
      sa.wg1 = cmul(csel(e0 & 1, a256, one), csel(e0 & 2, a512, one));
      sa.wg0 = csel(e0 >= 0, cmul(sa.wg1, csel(e0 & 4, a1024, one)), zero);
      sa.wg1 = csel(e1 >= 0, sa.wg1, zero);
      asm volatile("" : "+v"(sa.wg0.r), "+v"(sa.wg0.i), "+v"(sa.wg1.r), "+v"(sa.wg1.i));
    }
    const cf wt8 = cmul(sa.wt, sa.a8);
    const cf k0 = cfma(sa.cw, sa.lw, cmul(sa.c2, sa.pq));
    const cf k1 = cfma(sa.a8, k0, cmul(sa.c2, sa.e0));
    cf2 h = cf2{f2{k0.r, k1.r}, f2{k0.i, k1.i}}, wt2 = cf2{f2{sa.wt.r, wt8.r}, f2{sa.wt.i, wt8.i}};
#pragma unroll
    for (int k = 0; k < 8; k++) {
      sa.u[k] = cmul2(sa.c2, sa.u[k]);
      asm volatile("" : "+v"(sa.u[k].r), "+v"(sa.u[k].i));
    }
    asm volatile("" : "+v"(h.r), "+v"(h.i), "+v"(wt2.r), "+v"(wt2.i));
    CSTAMP(25);
    WAITV(5);
    setflag(0);
    STAMP(5);
    rdw(wf[5], L, z, q, 8, 2); rdw(wf[6], L, z, q, 3, 4);
    asm volatile("s_waitcnt vmcnt(1)" : "+v"(g0), "+v"(g1), "+v"(xv[0]), "+v"(xv[1]), "+v"(dk)::"memory");
#pragma unroll 1
    for (u32 spins = 0; spins < SPIN_CAP; spins++) {
      if (allok((q >= g || (g0.y == tagLo && g0.w == tagHi)) && (q + 4 >= g || (g1.y == tagLo && g1.w == tagHi)))) break;
      asm volatile("s_sleep 1\n\tbuffer_load_dwordx4 %0, %2, %3, 0 offen sc1\n\tbuffer_load_dwordx4 %1, %2, %3, %4 offen sc1\n\ts_waitcnt vmcnt(0)"
                   : "=&v"(g0), "=&v"(g1)
                   : "v"(ao), "s"(rs), "s"(4 * 512 * 16)
                   : "memory");
    }
    {
      cf hin = cfma(sa.wg0, csel(q < g, cf{u2f(g0.x), u2f(g0.z)}, zero), cmul(sa.wg1, csel(q + 4 < g, cf{u2f(g1.x), u2f(g1.z)}, zero)));
      hin.r = sumx32(sumx16(hin.r));
      hin.i = sumx32(sumx16(hin.i));
      h = cfma2(hin, wt2, h);
    }
    STAMP(6);
    const u32 tb = TRB + tg * 1280;
    const u32 twr = tb + c * 40 + q * 8;
    const u32 trd = tb + (q >> 1) * 640 + (8 * (q & 1) + r4) * 40 + pp * 8;
    v4u o[4];
#pragma unroll
    for (int i = 0; i < 4; i++) {
      const cf2 ha = cfma2(sa.a1, h, sa.u[2 * i]);
      h = cfma2(sa.a1, ha, sa.u[2 * i + 1]);
      *(LAS v2u*)(L + twr) = v2u{pk2(ha.r), pk2(h.r)};
      *(LAS v2u*)(L + twr + 640) = v2u{pk2(-ha.i), pk2(-h.i)};
      asm volatile("" ::: "memory");
      o[i] = tr2(L, trd, 160);
      asm volatile("" ::: "memory");
    }
#pragma unroll
    for (int i = 0; i < 4; i++)
      __builtin_amdgcn_raw_buffer_store_b128(o[i], rs, lane * 16 + WS_HB + ((g * 4 + tg) * 4 + i) * HB_TILE + (11 * (j >> 2) + (j & 3)) * 1024, 0, AUX_SC1);
    CSTAMP(26);
    WAITL0();
    CP(1); CP(2); CP(3);
    WAITV(6);
    setflag(2);
    __builtin_amdgcn_s_setprio(1);
    CP(4); CP(5); CP(6); CP(7);
    CSTAMP(28);
  }
  f4 sk[2];
  float smm[2], srr[2];
#pragma unroll
  for (int e = 0; e < 2; e++) {
    const u32 sp_ = GR + u * 256 + (16 * tg + 4 * (2 * e + H) + q) * 4;
    smm[e] = ldsf(L, sp_);
    srr[e] = ldsf(L, sp_ + 1024);
  }
  asm volatile("" : "+v"(smm[0]), "+v"(smm[1]), "+v"(srr[0]), "+v"(srr[1]));
  s16u gfv;
  glu_read(gfv, p.ws + fso);
  asm volatile("" : "+v"(xv[0]), "+v"(xv[1]));
#pragma unroll
  for (int e = 0; e < 2; e++) {
    const float mm = smm[e], r = srr[e];
    const float xs[4] = {bf2f(xv[e].x), bfhi(xv[e].x), bf2f(xv[e].y), bfhi(xv[e].y)};
    const float ds[4] = {bf2f(dk.x), bfhi(dk.x), bf2f(dk.y), bfhi(dk.y)};
#pragma unroll
    for (int k = 0; k < 4; k++) sk[e][k] = xs[k] + rbf(rbf(rbf(xs[k] - mm) * r) * ds[k]);
    asm volatile("" : "+v"(sk[e]));
  }
  asm volatile("s_waitcnt lgkmcnt(0)" : "+s"(gfv)::"memory");
  STAMP(7);

  __builtin_amdgcn_s_setprio(0);
  poll_glu(gfv, p.ws + fso, rs, l16, fso, tagLo, tagHi);
  s8u sfv;
  STAMP(9);
  const u32 hs = WS_HB + (g * 4 + u) * 4 * HB_TILE + 11 * w * 1024;
  f4 ac2[4][4];
#pragma unroll
  for (int i = 0; i < 4; i++)
#pragma unroll
    for (int nn = 0; nn < 4; nn++) ac2[i][nn] = f4{0, 0, 0, 0};
  v4u hf[44];
  auto ldh = [&](int m) {
    const int i = m < 28 ? (m & 3) : ((m - 28) >> 2);
    const u32 so = hs + i * HB_TILE + (m < 28 ? 4 + rot7(m >> 2, r7) : ((m - 28) & 3)) * 1024;
    asm volatile("buffer_load_dwordx4 %0, %1, %2, %3 offen sc1" : "=v"(hf[m]) : "v"(l16), "s"(rs), "s"(so) : "memory");
  };
  u32 ub[4];
  int c2_ = 0, q2 = 0;
  auto ldc = [&](int s) {
    const u32 ca = ((2 * s + (q2 & 1)) ^ ((c2_ >> 1) & 7)) << 4;
#pragma unroll
    for (int nn = 0; nn < 4; nn++) wf[s][nn] = lds128(L, ub[nn] + ca);
  };
  auto wrt = [&](int i) {
#pragma unroll
    for (int nn = 0; nn < 4; nn++)
      *(LAS f4*)(L + wo + unitb(2 * i + (nn >> 1)) + (nn & 1) * 1024 + q2 * 256 + ((c2_ ^ (4 * nn + q2)) << 4)) = ac2[i][nn];
  };
  const srd_t so = mksrd(p.out + (size_t)g * (256 * 512), 256 * 512 * 4);
  f4 pv[8];
  auto redrd = [&](int e) {
    const u32 rda = ((c2_ >> 3) ? unitb(4 * e + 2 * H + 1) : unitb(4 * e + 2 * H)) + ((c2_ >> 2) & 1) * 1024 + (c2_ & 3) * 256 + (((4 * tg + q2) ^ c2_) << 4);
#pragma unroll
    for (int v = 0; v < 8; v++) pv[v] = ldsf4(L, v * PW + rda);
  };
  auto redst = [&](int e) {
    const f4 s = (((pv[0] + pv[1]) + (pv[2] + pv[3])) + ((pv[4] + pv[5]) + (pv[6] + pv[7]))) + sk[e];
    const u32 o = (64 * u + 16 * tg + 8 * (q2 & 1) + 2 * (2 * e + H) + (q2 >> 1)) * 512 + 64 * n + 16 * ((c2_ >> 2) ^ xn) + 4 * (c2_ & 3);
    __builtin_amdgcn_raw_buffer_store_b128(__builtin_bit_cast(v4u, s), so, o * 4, 0, AUX_SC1);
  };
  __builtin_amdgcn_s_setprio(SPRIO ? H : 0);
#pragma unroll
  for (int m = 0; m < HI0; m++) ldh(m);
  CSTAMP(29);
  static_for<0, 44>([&](auto M) {
    constexpr int m = M.value;
    constexpr int i = m < 28 ? (m & 3) : ((m - 28) >> 2), ks = m < 28 ? (m >> 2) : ((m - 28) & 3);
    constexpr int nw = issued_upto(m - 1) - pos_h(m) - 1 + (m > 40 ? 1 : 0);
    static_assert(nw >= 0 && nw < 64 && hiss(37) == 44, "vmcnt");
    asm volatile("s_waitcnt vmcnt(%1)" : "+v"(hf[m]) : "n"(nw) : "memory");
#pragma unroll
    for (int nn = 0; nn < 4; nn++) ac2[i][nn] = mfma(wf[ks][nn], hf[m], ac2[i][nn]);
    PIN4(ac2[i]);
    SCHED();
    if (m == HCHK) {
      STAMP(10);
      asm volatile("s_waitcnt lgkmcnt(0)" : "+s"(sfv)::"memory");
      poll_ssm(sfv, p.ws + fso, rs, l16, fso, tagLo, tagHi);
      STAMP(11);
    }
#pragma unroll
    for (int k = hiss(m - 1); k < hiss(m); k++) ldh(k);
    if (m == HFL) ssm_read(sfv, p.ws + fso);
    if (m == 17) {
      asm volatile("" : "+v"(l16));
      c2_ = (l16 >> 4) & 15;
      q2 = l16 >> 8;
#pragma unroll
      for (int nn = 0; nn < 4; nn++) ub[nn] = wo + c2_ * 128 + ((q2 >> 1) ? unitb(cunit(1, nn)) : unitb(cunit(0, nn)));
    }
    if (m == 19) ldc(0);
    if (m == 21) ldc(1);
    if (m == 23) ldc(2);
    if (m == 25) ldc(3);
    if (m == 32) wrt(0);
    if (m == 36) wrt(1);
    if (m == 38) { WAITL0(); BAR(); redrd(0); }
    if (m == 40) { redst(0); wrt(2); }
    if (m == 43) wrt(3);
    SCHED();
  });
  STAMP(12);
  WAITL0();
  CSTAMP(33);
  BAR();
  CSTAMP(34);
  redrd(1);
  redst(1);
  if (H == 0 && g == 7 && j == 0 && w == 0 && l16 == 0) __hip_atomic_store((GAS u64*)(p.ws + WS_EP), tag, RLX_AGENT);
  STAMP(13);
}

}

__global__ __launch_bounds__(512, 2) void s5_glu_fused(Args p) {
  __shared__ __attribute__((aligned(256))) char smem[LDS_BYTES];
  LAS char* L = (LAS char*)smem;
  const u32 lbase = (u32)(size_t)L;
  int lane = threadIdx.x & 63;
  const int w = __builtin_amdgcn_readfirstlane(threadIdx.x >> 6);
  const int b = blockIdx.x, xcd = b & 7, idx = b >> 3;
  const int g = (xcd & 1) * 4 + (idx & 3), j = (xcd >> 1) * 8 + (idx >> 2);
  const int rot = 2 * (((idx >> 2) + idx) & 3);
  u32 stampv = 0;
  STAMP(0);
  {
    u64 pc;
    asm volatile("s_getpc_b64 %0" : "=s"(pc));
    const u32 off = (w * 64 + lane) * 128;
    if (w * 8 < CODEPF && (idx & 3) == 0)
      asm volatile("s_mov_b32 m0, %2\n\ts_nop 0\n\tglobal_load_lds_dword %0, %1" ::"v"(off < CODEPF * 1024 ? off : 0), "s"(pc & ~127ull), "s"(lbase + DUMMY) : "memory", "m0");
  }
  const srd_t sx = mksrd((const char*)p.x + (size_t)g * (256 * 1024), 256 * 1024);
  PSTAMP(4);
  u32 vo[5], vs;
  stage_offsets(lane, w, vo, vs);
  const srd_t sb = mksrd((const char*)((w & 2) ? p.bim : p.bre) + (size_t)j * (16 * 1024), 16 * 1024);
  if (w < 4) glds<0>(vs, sb, rot * 128, lbase + SM_OFF + w * 1024, 0);
  glds<0>(vo[0], sx, rot * 128, lbase + w * 4096, 0);
  glds<1024>(vo[1], sx, rot * 128, lbase + w * 4096, 0);
  glds<2048>(vo[2], sx, rot * 128, lbase + w * 4096, 0);
  glds<3072>(vo[3], sx, rot * 128, lbase + w * 4096, 0);
  const srd_t s1 = mksrd((const char*)p.w1 + j * 112, 512 * 3584 - j * 112), s3 = mksrd((const char*)p.w3 + j * 112, 512 * 3584 - j * 112);
  glds56(vo[4], s1, rot * (64 * 3584), lbase + G_OFF + w * 896, 0);
  glds56(vo[4], s3, rot * (64 * 3584), lbase + G_OFF + w * 896, G3D);
  SCHED();
  PSTAMP(5);
  const int n = (j + NSH) & 7, xn = g & 3, r7 = R7M == 0 ? rot7(n == 7 ? 0 : n, 2 * xn) : R7M == 1 ? (n == 7 ? 0 : n) : 0;
  const srd_t rs = mksrd(p.ws, WS_BYTES);
  const srd_t sw2 = mksrd((const char*)p.w2 + (size_t)w * (224 * 1024) + n * 128, 224 * 1024 - n * 128);
  const srd_t scr = mksrd((const char*)p.cre + (size_t)n * (64 * 1024) + w * 128, 64 * 1024 - w * 128);
  int wb = w;
  asm volatile("" : "+s"(wb));
  if (wb >= 4) {
    asm volatile("" : "+v"(lane));
    phase1<1>(p, L, lbase, rs, sx, s1, s3, sw2, scr, sb, g, j, rot, r7, w, lane, stampv);
    __builtin_amdgcn_endpgm();
  }
  phase1<0>(p, L, lbase, rs, sx, s1, s3, sw2, scr, sb, g, j, rot, r7, w, lane, stampv);
}

extern "C" void kernel_launch(void* const* d_in, const int* in_sizes, int n_in, void* d_out, int out_size, void* d_ws, size_t ws_size,
                              hipStream_t stream) {
  Args p;
  p.x = (const u16*)d_in[0];
  p.lre = (const u16*)d_in[1];
  p.lim = (const u16*)d_in[2];
  p.bre = (const u16*)d_in[3];
  p.bim = (const u16*)d_in[4];
  p.cre = (const u16*)d_in[5];
  p.cim = (const u16*)d_in[6];
  p.ldt = (const u16*)d_in[7];
  p.dsk = (const u16*)d_in[8];
  p.w1 = (const u16*)d_in[10];
  p.w3 = (const u16*)d_in[11];
  p.w2 = (const u16*)d_in[12];
  p.out = (float*)d_out;
  p.ws = (unsigned char*)d_ws;
  s5_glu_fused<<<256, 512, 0, stream>>>(p);
}
```

```cpp
#include <hip/hip_runtime.h>
#include <stdint.h>

typedef unsigned short u16;
typedef unsigned int u32;
typedef unsigned long long u64;
typedef u32 v4u __attribute__((ext_vector_type(4)));
typedef u32 v2u __attribute__((ext_vector_type(2)));
typedef float f4 __attribute__((ext_vector_type(4)));
typedef float f2 __attribute__((ext_vector_type(2)));
typedef short v4s __attribute__((ext_vector_type(4)));
typedef __bf16 v8bf __attribute__((ext_vector_type(8)));
typedef __bf16 v2bf __attribute__((ext_vector_type(2)));
typedef __amdgpu_buffer_rsrc_t srd_t;
#define LAS __attribute__((address_space(3)))
#define GAS __attribute__((address_space(1)))
#define DI __device__ __forceinline__
#define RLX_AGENT __ATOMIC_RELAXED, __HIP_MEMORY_SCOPE_AGENT

#define STAMPS 0
#define WARM 1
#define HAUX 16
#define R7M 1
#define DST 0
#define PSLEEP "4"
#define NSH 0
#define PRE 0
#define TLBW 0
#define CODEPF 28
#define PST 0
#define SPRIO 1
#define APRIO 2
#define AW2L 0
#define LP 0
#define BSPLIT 1
#define ROT 3

namespace {
constexpr int SM_OFF = 32768, G_OFF = 36864, G3D = 7184, SLOT = 51456, AUXO = 3 * SLOT;
constexpr int GR = AUXO, CST = AUXO + 2048, TRB = AUXO + 4096, XCH = 49152, DUMMY = AUXO + 9216;
constexpr int LDS_BYTES = 163840;
constexpr int PW = 6144;
constexpr int unitb(int v) { return ((v / 3 + 2) % 3) * SLOT + (v % 3) * 2048; }
#define SH_FL 0
#define SH_AG 0
#define SH_HB 0
constexpr int WS_EP = 0, WS_FL = 4096 + SH_FL, WS_AG = 65536 + SH_AG, WS_SG = 196608, WS_CS = 262144, WS_HB = (1 << 20) + SH_HB, HB_TILE = 88 * 1024;
constexpr int WS_BYTES = WS_HB + 128 * HB_TILE;
constexpr int WS_STAMP = 32 << 20, NSTAMP = 14;
constexpr int AUX_SC1 = 16;
constexpr u32 SPIN_CAP = 1u << 17;
constexpr float NL2E = -1.4426950408889634f;

DI float bf2f(u32 b) { return __builtin_bit_cast(float, b << 16); }
DI float bfhi(u32 b) { return __builtin_bit_cast(float, b & 0xFFFF0000u); }
DI float u2f(u32 b) { return __builtin_bit_cast(float, b); }
DI u32 f2u(float f) { return __builtin_bit_cast(u32, f); }
DI float rbf(float f) { return (float)(__bf16)f; }
DI u32 pk2(f2 a) { return __builtin_bit_cast(u32, __builtin_convertvector(a, v2bf)); }
DI float rcp(float x) { return __builtin_amdgcn_rcpf(x); }
DI float ffma(float a, float b, float c) { return __builtin_fmaf(a, b, c); }
DI f2 fma2(f2 a, f2 b, f2 c) { return __builtin_elementwise_fma(a, b, c); }
DI f2 sp(float a) { return f2{a, a}; }
DI f2 half2(f4 v, int h) { return h ? v.zw : v.xy; }
DI f4 mfma(v4u a, v4u b, f4 c) {
  return __builtin_amdgcn_mfma_f32_16x16x32_bf16(__builtin_bit_cast(v8bf, a), __builtin_bit_cast(v8bf, b), c, 0, 0, 0);
}
DI v4u lds128(LAS char* L, u32 a) { return *(LAS v4u*)(L + a); }
DI f4 ldsf4(LAS char* L, u32 a) { return *(LAS f4*)(L + a); }
DI float ldsf(LAS char* L, u32 a) { return *(LAS float*)(L + a); }
DI v4u tr2(LAS char* L, u32 a, int d) {
  v2u lo = __builtin_bit_cast(v2u, __builtin_amdgcn_ds_read_tr16_b64_v4i16((LAS v4s*)(L + a)));
  v2u hi = __builtin_bit_cast(v2u, __builtin_amdgcn_ds_read_tr16_b64_v4i16((LAS v4s*)(L + a + d)));
  return v4u{lo.x, lo.y, hi.x, hi.y};
}
DI srd_t mksrd(const void* p, int bytes) { return __builtin_amdgcn_make_buffer_rsrc((void*)p, 0, bytes, 0x00020000); }
DI u32 rfl(u32 x) { return __builtin_amdgcn_readfirstlane(x); }
#pragma clang diagnostic ignored "-Winline-asm"
template <int OFF> DI void glds(u32 voff, srd_t srd, u32 soff, u32 la, u32 lb) {
  asm volatile("s_add_u32 m0, %3, %4\n\ts_nop 0\n\tbuffer_load_dwordx4 %0, %1, %2 offen offset:%5 lds" ::"v"(voff), "s"(srd), "s"(soff), "s"(la), "s"(lb),
               "n"(OFF)
               : "memory", "m0", "scc");
}
DI void glds56(u32 voff, srd_t srd, u32 soff, u32 la, u32 lb) {
  asm volatile("s_add_u32 m0, %3, %4\n\ts_mov_b32 exec_hi, 0xffffff\n\tbuffer_load_dwordx4 %0, %1, %2 offen lds\n\ts_mov_b32 exec_hi, -1" ::"v"(voff),
               "s"(srd), "s"(soff), "s"(la), "s"(lb)
               : "memory", "m0", "scc");
}
template <int LB> DI void glds2(u32 ve, u32 vo, srd_t srd, u32 soff, u32 la) {
  asm volatile("s_add_u32 m0, %4, %5\n\ts_nop 0\n\tbuffer_load_dwordx4 %0, %2, %3 offen lds\n\tbuffer_load_dwordx4 %1, %2, %3 offen offset:1024 lds" ::"v"(ve),
               "v"(vo), "s"(srd), "s"(soff), "s"(rfl(la)), "n"(LB)
               : "memory", "m0", "scc");
}
#define WAITV(n) asm volatile("s_waitcnt vmcnt(" #n ")" ::: "memory")
#define WAITL0() do { asm volatile("" ::: "memory"); __builtin_amdgcn_s_waitcnt(0xC07F); asm volatile("" ::: "memory"); } while (0)
#define BAR() __builtin_amdgcn_s_barrier()
#define SCHED() __builtin_amdgcn_sched_barrier(0)
#define SGB(m, n) __builtin_amdgcn_sched_group_barrier(m, n, 0)
#define PIN4(a) asm volatile("" : "+v"((a)[0]), "+v"((a)[1]), "+v"((a)[2]), "+v"((a)[3]))

template <int V> struct IC { static constexpr int value = V; };
template <int B, int E, class F> DI void static_for(F f) {
  if constexpr (B < E) {
    f(IC<B>{});
    static_for<B + 1, E>(f);
  }
}
DI bool allok(bool c) { return __builtin_amdgcn_ballot_w64(!c) == 0; }
struct cf { float r, i; };
DI float afma(float a, float b, float c) { float r; asm("v_fma_f32 %0, %1, %2, %3" : "=v"(r) : "v"(a), "v"(b), "v"(c)); return r; }
DI float anfma(float a, float b, float c) { float r; asm("v_fma_f32 %0, -%1, %2, %3" : "=v"(r) : "v"(a), "v"(b), "v"(c)); return r; }
DI cf cmul(cf a, cf b) { return cf{anfma(a.i, b.i, a.r * b.r), ffma(a.r, b.i, a.i * b.r)}; }
DI cf cfma(cf a, cf b, cf c) { return cf{afma(a.r, b.r, anfma(a.i, b.i, c.r)), ffma(a.r, b.i, ffma(a.i, b.r, c.i))}; }
DI cf csq(cf a) { return cf{anfma(a.i, a.i, a.r * a.r), 2.f * a.r * a.i}; }
DI cf csel(bool p, cf a, cf b) { return cf{p ? a.r : b.r, p ? a.i : b.i}; }
struct cf2 { f2 r, i; };
DI cf2 cfma2(cf a, cf2 b, cf2 c) { return cf2{fma2(sp(a.r), b.r, fma2(sp(-a.i), b.i, c.r)), fma2(sp(a.r), b.i, fma2(sp(a.i), b.r, c.i))}; }
DI cf2 cmul2(cf a, cf2 b) { return cf2{fma2(sp(a.r), b.r, sp(-a.i) * b.i), fma2(sp(a.r), b.i, sp(a.i) * b.r)}; }
template <int CTRL> DI float dpp(float x) { return u2f(__builtin_amdgcn_mov_dpp(f2u(x), CTRL, 0xf, 0xf, true)); }
DI float sum16(float v) {
  v += dpp<0xB1>(v); v += dpp<0x4E>(v); v += dpp<0x141>(v); v += dpp<0x140>(v);
  return v;
}
DI void sw16(u32& a, u32& b) { auto r = __builtin_amdgcn_permlane16_swap(a, b, false, false); a = r[0]; b = r[1]; }
DI void sw32(u32& a, u32& b) { auto r = __builtin_amdgcn_permlane32_swap(a, b, false, false); a = r[0]; b = r[1]; }
#define SWPAD2(a, b) asm volatile("s_nop 1" : "+v"(a), "+v"(b))
#define SWPAD4(a, b, c, d) asm volatile("s_nop 1" : "+v"(a), "+v"(b), "+v"(c), "+v"(d))
DI float sumx16(float v) { u32 a = f2u(v), b = a; sw16(a, b); SWPAD2(a, b); return u2f(a) + u2f(b); }
DI float sumx32(float v) { u32 a = f2u(v), b = a; sw32(a, b); SWPAD2(a, b); return u2f(a) + u2f(b); }
DI void bcast4(float v, float (&o)[4]) {
  u32 a = f2u(v), b = a;
  sw16(a, b);
  SWPAD2(a, b);
  u32 a2 = a, b2 = b;
  sw32(a, a2);
  sw32(b, b2);
  SWPAD4(a, b, a2, b2);
  o[0] = u2f(a); o[1] = u2f(b); o[2] = u2f(a2); o[3] = u2f(b2);
}
DI v4u swap16x2(u32 ax, u32 ay, u32 bx, u32 by) {
  sw16(ax, bx);
  sw16(ay, by);
  v4u o = {ax, ay, bx, by};
  asm volatile("s_nop 1" : "+v"(o));
  return o;
}

struct Args {
  const u16 *x, *lre, *lim, *bre, *bim, *cre, *cim, *ldt, *dsk, *w1, *w3, *w2;
  float* out;
  unsigned char* ws;
};

#define STAMP(k) do { } while (0)
#define CSTAMP(k) do { } while (0)
#define DSTAMP(k) do { } while (0)
#define PSTAMP(k) do { } while (0)

DI u32 swiglu2(f2 tg, f2 tu, float nrl, float ir2) {
  const f2 a = tg * sp(nrl);
  const f2 e = {__builtin_amdgcn_exp2f(a.x), __builtin_amdgcn_exp2f(a.y)};
  const f2 d = fma2(e, sp(ir2), sp(ir2));
  const f2 r = {rcp(d.x), rcp(d.y)};
  return pk2(tg * tu * r);
}

template <int H> struct Fr { v4u x[4]; v4u w[H == 0 ? 4 : 5]; };
struct Ra { u32 xa, sa, ga, gm; };

template <int H> DI void ldpiece(Fr<H>& F, LAS char* L, const Ra& a, int k) {
  if (k == 0) { F.x[0] = lds128(L, a.xa); F.x[1] = lds128(L, a.xa + 256); F.x[2] = lds128(L, a.xa + 512); F.x[3] = lds128(L, a.xa + 768); }
  if (H == 0) {
    if (k == 1) { F.w[0] = lds128(L, a.sa); F.w[1] = lds128(L, a.sa + 2048); F.w[2] = tr2(L, a.ga, 112); }
    if (k == 2) F.w[3] = tr2(L, a.ga + G3D, 112);
  } else {
    if (k == 1) { F.w[0] = tr2(L, a.ga + 32, 112); F.w[1] = tr2(L, a.ga + G3D + 32, 112); }
    if (k == 2) { F.w[2] = tr2(L, a.ga + 64, 112); F.w[3] = tr2(L, a.ga + G3D + 64, 112); }
    if (k == 3) F.w[4] = tr2(L, a.gm, 112);
  }
}
template <int H> DI void mmtile(const Fr<H>& F, f4 (&acc)[4], int t) {
#pragma unroll
  for (int i = 0; i < 4; i++) {
    if (H == 0 && t < 2) acc[i] = mfma(F.x[i], F.w[t], acc[i]);
    else acc[i] = mfma(F.w[t], F.x[i], acc[i]);
  }
}
template <int H, class I>
DI void half(const Fr<H>& Fc, Fr<H>& Fn, LAS char* L, const Ra& a, f4 (&acc)[H == 0 ? 4 : 5][4], I iss) {
  constexpr int NT = H == 0 ? 4 : 5;
#pragma unroll
  for (int t = 0; t < NT; t++) {
    if (LP == 3) { iss(t); SCHED(); }
    if (LP == 2) __builtin_amdgcn_s_setprio(1);
    ldpiece<H>(Fn, L, a, t);
    mmtile<H>(Fc, acc[t], t);
    if (LP == 4) {
      SGB(0x100, 4); SGB(0x8, 4);
    } else if (LP == 5) {
      SGB(0x8, 2); SGB(0x100, 2); SGB(0x8, 2); SGB(0x100, 2);
    } else {
#pragma unroll
      for (int k = 0; k < 4; k++) { SGB(0x8, 1); SGB(0x100, 1); }
    }
    PIN4(acc[t]);
    SCHED();
    if (LP == 2) __builtin_amdgcn_s_setprio(0);
    if (LP != 3) iss(t);
    SCHED();
  }
}

DI int rot7(int s, int r) { const int k = s + r; return k >= 7 ? k - 7 : k; }
DI u32 p2_pair(int r, int spn) { return (2 * rot7(spn >> 1, r) + (spn & 1)) * 16384; }
constexpr int cunit(int m, int nn) { return m == 0 ? (nn == 0 ? 5 : nn == 3 ? 6 : nn - 1) : (nn == 0 ? 7 : 1 + nn); }

struct Sa {
  cf2 u[8];
  cf a1, a8, a16, a64, pq, lw, e0, c2, cw, wt, wg0, wg1;
};
struct Pw {
  u32 vwe, vwo, vce, vco, tl[4];
};
DI Pw pw_setup(int lane, int w, int xn) {
  Pw z;
  const int q = lane >> 4, r4 = (lane & 15) >> 2, pp = lane & 3;
  z.vwe = (lane >> 3) * 1024 + (((lane & 7) ^ (2 * (q & 1))) << 4);
  z.vwo = (z.vwe ^ 64) + 8192 - 1024;
  z.vce = (lane >> 3) * 1024 + (((lane & 7) ^ q) << 4);
  z.vco = (z.vce ^ 64) + 8192 - 1024;
#pragma unroll
  for (int nn = 0; nn < 4; nn++)
    z.tl[nn] = w * PW + (q & 1) * 1024 + r4 * 128 + ((((2 * (nn ^ xn)) ^ (2 * (((r4 >> 1) & 1) + 2 * (q & 1)))) + (pp >> 1)) << 4) + (pp & 1) * 8;
  return z;
}
DI void rdw(v4u (&wf)[4], LAS char* L, const Pw& z, int q, int ua, int ub) {
  const u32 o = (q >> 1) ? unitb(ub) : unitb(ua);
#pragma unroll
  for (int nn = 0; nn < 4; nn++) wf[nn] = tr2(L, o + z.tl[nn], 512);
}

#define HW 8
constexpr int HI0 = HW, HCHK = 28 - HW, HFL = HCHK - 6;
#define HG 1
constexpr int hiss(int m) {
  const int n = m < HCHK ? m + 1 + HW : 28 + HG * (m - HCHK + 1);
  return n < 44 ? n : 44;
}
constexpr int ncopy(int m) { return 0; }
constexpr int nother(int m) { return ncopy(m); }
constexpr int issued_upto(int m) {
  int n = hiss(m);
  for (int k = 0; k <= m; k++) n += nother(k);
  return n;
}
constexpr int pos_h(int m) {
  int n = m;
  for (int k = 0; k < 44; k++) if (hiss(k) <= m) n += nother(k);
  return n;
}
static_assert(hiss(HCHK - 1) == 28, "schedule");

typedef u32 s16u __attribute__((ext_vector_type(16)));
typedef u32 s8u __attribute__((ext_vector_type(8)));
template <class V, int N> DI bool flags_match(V v, u32 tagLo, u32 tagHi) {
  u32 x = 0;
#pragma unroll
  for (int k = 0; k < N; k += 2) x |= (v[k] ^ tagLo) | (v[k + 1] ^ tagHi);
  return x == 0;
}
DI bool flags_vec(srd_t rs, u32 l16, u32 so, u32 tagLo, u32 tagHi, u32 amask) {
  v2u v;
  asm volatile("buffer_load_dwordx2 %0, %1, %2, %3 offen sc1\n\ts_waitcnt vmcnt(0)" : "=&v"(v) : "v"((l16 >> 1) & amask), "s"(rs), "s"(so) : "memory");
  return allok(v.x == tagLo && v.y == tagHi);
}
DI void glu_read(s16u& v, const unsigned char* fp) { asm volatile("s_load_dwordx16 %0, %1, 0x0 glc" : "=&s"(v) : "s"(fp) : "memory"); }
DI void poll_glu(s16u v, const unsigned char* fp, srd_t rs, u32 l16, u32 so, u32 tagLo, u32 tagHi) {
#pragma unroll 1
  for (u32 spins = 1; spins < SPIN_CAP; spins++) {
    if (flags_match<s16u, 16>(v, tagLo, tagHi)) break;
    if ((spins & 7) == 0 && flags_vec(rs, l16, so, tagLo, tagHi, 0x38)) break;
    asm volatile("s_load_dwordx16 %0, %1, 0x0 glc\n\ts_waitcnt lgkmcnt(0)" : "=&s"(v) : "s"(fp) : "memory");
  }
  asm volatile("" ::: "memory");
}
DI void ssm_read(s8u& v, const unsigned char* fp) { asm volatile("s_load_dwordx8 %0, %1, 0x40 glc" : "=&s"(v) : "s"(fp) : "memory"); }
DI void poll_ssm(s8u v, const unsigned char* fp, srd_t rs, u32 l16, u32 so, u32 tagLo, u32 tagHi) {
#pragma unroll 1
  for (u32 spins = 1; spins < SPIN_CAP; spins++) {
    if (flags_match<s8u, 8>(v, tagLo, tagHi)) break;
    if ((spins & 7) == 0 && flags_vec(rs, l16, so + 64, tagLo, tagHi, 0x18)) break;
    __builtin_amdgcn_s_sleep(2);
    asm volatile("s_load_dwordx8 %0, %1, 0x40 glc\n\ts_waitcnt lgkmcnt(0)" : "=&s"(v) : "s"(fp) : "memory");
  }
  asm volatile("" ::: "memory");
}

DI void stage_offsets(int lane, int w, u32 (&vo)[5], u32& vs) {
#pragma unroll
  for (int e = 0; e < 4; e++) vo[e] = (32 * w + 8 * e + (lane >> 3)) * 1024 + (((lane & 7) ^ ((4 * w + e) & 7)) << 4) - e * 1024;
  const int gpos = (lane * 37) >> 8, gch = lane - gpos * 7;
  vo[4] = (8 * w + 4 * (gpos & 1) + (gpos >> 1)) * 3584 + gch * 16;
  vs = (8 * (w & 1) + (lane >> 3)) * 1024 + (((lane & 7) ^ ((4 * (w & 1) + (lane >> 4)) & 7)) << 4);
}

template <int H>
DI void phase1(const Args& p, LAS char* L, u32 lbase, srd_t rs, srd_t sx, srd_t s1, srd_t s3, srd_t sw2, srd_t scr, srd_t sb, int g, int j, int rot, int r7, int w,
               int lane, u32 stampv) {
  constexpr int NT = H == 0 ? 4 : 5;
  u32 vo[5], vs;
  stage_offsets(lane, w, vo, vs);
  Sa sa;
  v4u wf[7][4];
  const int tg = w & 3, c = lane & 15, q = lane >> 4, r4 = c >> 2, pp = lane & 3;
  const u32 ld0 = lbase + w * 4096, ld4 = lbase + G_OFF + w * 896, ld5 = ld4 + G3D, lds_s = lbase + SM_OFF + w * 1024;
  auto op = [&](int i, u32 kk, u32 slot) {
    if (i == 0) glds<0>(vo[0], sx, kk * 128, ld0, slot);
    else if (i == 1) glds<1024>(vo[1], sx, kk * 128, ld0, slot);
    else if (i == 2) glds<2048>(vo[2], sx, kk * 128, ld0, slot);
    else if (i == 3) glds<3072>(vo[3], sx, kk * 128, ld0, slot);
    else if (i == 4) glds56(vo[4], s1, kk * (64 * 3584), ld4, slot);
    else if (i == 5) glds56(vo[4], s3, kk * (64 * 3584), ld5, slot);
    else if (H == 0) glds<0>(vs, sb, kk * 128, lds_s, slot);
  };
  v2u ep;
  u32 pl0 = 0, pl1 = 0, pl2 = 0;
  v4u xr0 = {0, 0, 0, 0}, xr1 = {0, 0, 0, 0};
  {
    const u32 zero = 0;
    asm volatile("global_load_dwordx2 %0, %1, %2 sc1" : "=v"(ep) : "v"(zero), "s"(p.ws + WS_EP) : "memory");
    if (H == 0) {
      const u32 o = (16 * j + c) * 2;
      asm volatile("global_load_ushort %0, %1, %2" : "=v"(pl0) : "v"(o), "s"(p.lre) : "memory");
      asm volatile("global_load_ushort %0, %1, %2" : "=v"(pl1) : "v"(o), "s"(p.lim) : "memory");
      asm volatile("global_load_ushort %0, %1, %2" : "=v"(pl2) : "v"(o), "s"(p.ldt) : "memory");
      const u32 ro = (8 * j + 2 * tg + (lane >> 5)) * 1024 + (lane & 31) * 16;
      asm volatile("buffer_load_dwordx4 %0, %1, %2, 0 offen" : "=v"(xr0) : "v"(ro), "s"(sx) : "memory");
      asm volatile("buffer_load_dwordx4 %0, %1, %2, 0 offen offset:512" : "=v"(xr1) : "v"(ro), "s"(sx) : "memory");
    }
  }
  u32 tagLo = 0, tagHi = 0;
  if (H == 0) {
    asm volatile("s_waitcnt vmcnt(0)" : "+v"(ep), "+v"(pl0), "+v"(pl1), "+v"(pl2), "+v"(xr0), "+v"(xr1)::"memory");
    PSTAMP(6);
    const u64 tg64 = (((u64)rfl(ep.y) << 32) | rfl(ep.x)) + 1;
    tagLo = (u32)tg64; tagHi = (u32)(tg64 >> 32);
    f2 v[8];
#pragma unroll
    for (int k = 0; k < 4; k++) { v[k] = f2{bf2f(xr0[k]), bfhi(xr0[k])}; v[4 + k] = f2{bf2f(xr1[k]), bfhi(xr1[k])}; }
    const f2 sm = ((v[0] + v[1]) + (v[2] + v[3])) + ((v[4] + v[5]) + (v[6] + v[7]));
    const float m = sumx16(sum16(sm.x + sm.y)) * (1.f / 512.f);
    f2 s2 = {0.f, 0.f};
#pragma unroll
    for (int k = 0; k < 8; k++) { const f2 d = v[k] - sp(m); s2 = fma2(d, d, s2); }
    const float var = sumx16(sum16(s2.x + s2.y)) * (1.f / 512.f);
    const float sd = rbf(__builtin_amdgcn_sqrtf(rbf(rbf(var) + 1e-5f)));
    const v4u o = {f2u(rbf(m)), tagLo, f2u(rcp(sd)), tagHi};
    __builtin_amdgcn_raw_buffer_store_b128(o, rs, (lane & 31) ? 0x7FFFFFF0 : WS_SG + (g * 256 + 8 * j + 2 * tg + (lane >> 5)) * 16, 0, AUX_SC1);
    SCHED();
  }
  PSTAMP(8);
  op(6, (rot + 1) & 7, SLOT);
#pragma unroll
  for (int i = 0; i < 6; i++) op(i, (rot + 1) & 7, SLOT);
  if (H == 0) {
    const float lamr = bf2f(pl0), lami = bf2f(pl1), dt = rbf(expf(bf2f(pl2)));
    float sn, cn;
    sincosf(dt * lami, &sn, &cn);
    const float e = expf(dt * lamr);
    sa.a1 = cf{e * cn, e * sn};
    sa.a8 = csq(csq(csq(sa.a1)));
    sa.a16 = csq(sa.a8);
    sa.a64 = csq(csq(sa.a16));
    const float nr = sa.a1.r - 1.f, ni = sa.a1.i, den = 2.f / (lamr * lamr + lami * lami);
    sa.c2 = cf{(nr * lamr + ni * lami) * den, (ni * lamr - nr * lami) * den};
    asm volatile("" : "+v"(sa.a1.r), "+v"(sa.a1.i), "+v"(sa.a8.r), "+v"(sa.a8.i), "+v"(sa.a16.r), "+v"(sa.a16.i), "+v"(sa.a64.r), "+v"(sa.a64.i),
                 "+v"(sa.c2.r), "+v"(sa.c2.i));
    SCHED();
  }
  const u32 xa0 = (64 * tg + 16 * r4 + 8 * (c & 1) + ((c >> 1) & 1)) * 128 + ((q ^ (2 * r4 + (c & 1))) << 4);
  const u32 sa0 = SM_OFF + c * 128 + ((q ^ ((c >> 1) & 7)) << 4);
  const u32 ga0 = G_OFF + q * 896 + r4 * 224 + 8 * pp;
  const u32 gm0 = ga0 + 96 + (pp >= 2 ? G3D - 16 : 0);
  auto ra = [&](u32 slot, int ks) { return Ra{slot + (xa0 ^ (ks * 64)), slot + (sa0 ^ (ks * 64)), slot + ga0 + ks * 3584, slot + gm0 + ks * 3584}; };
  f4 acc[NT][4];
#pragma unroll
  for (int t = 0; t < NT; t++)
#pragma unroll
    for (int i = 0; i < 4; i++) acc[t][i] = f4{0, 0, 0, 0};
  PSTAMP(10);
  if (H == 1) WAITV(7);
  PSTAMP(11);
  BAR();
  STAMP(2);
  CSTAMP(14);
  Fr<H> F0, F1;
  {
    const Ra a = ra(0, 0);
#pragma unroll
    for (int t = 0; t < NT; t++) ldpiece<H>(F0, L, a, t);
    SCHED();
  }
  half<H>(F0, F1, L, ra(0, 1), acc, [&](int t) {
    if (t == 0) op(6, (rot + 2) & 7, 2 * SLOT);
    if (t < 3) { op(2 * t, (rot + 2) & 7, 2 * SLOT); op(2 * t + 1, (rot + 2) & 7, 2 * SLOT); }
  });
  if (H == 1) {
    asm volatile("s_waitcnt vmcnt(12)" : "+v"(ep)::"memory");
    const u64 tg64 = (((u64)rfl(ep.y) << 32) | rfl(ep.x)) + 1;
    tagLo = (u32)tg64; tagHi = (u32)(tg64 >> 32);
  }
  if (H == 0 && tg < 3) {
    const int b = g * 32 + j;
    const char* src = tg == 0 ? (const char*)p.w2 + b * 7168 : (const char*)(tg == 1 ? p.cre : p.cim) + b * 2048;
    const u32 off = (tg == 0 ? (lane < 56 ? lane : 0) : (lane & 15)) * 128;
    asm volatile("s_mov_b32 m0, %2\n\ts_nop 0\n\tglobal_load_lds_dword %0, %1" ::"v"(off), "s"(src), "s"(lbase + DUMMY) : "memory", "m0");
  }
  const u64 tag = ((u64)tagHi << 32) | tagLo;
  if (LP == 1 && H == 1) __builtin_amdgcn_s_setprio(1);
  u32 cur = 0, nxt = SLOT;
  SCHED();
#pragma unroll 1
  for (int s = 0; s < 5; s++) {
    const Ra an0 = ra(nxt, 0);
    const u32 kk = (rot + s + 3) & 7;
    WAITL0();
    DSTAMP(16 + 3 * s);
    WAITV(6);
    DSTAMP(17 + 3 * s);
    BAR();
    DSTAMP(18 + 3 * s);
    SCHED();
    half<H>(F1, F0, L, an0, acc, [&](int t) {
      if (H == 0) {
        if (t == 0) op(6, kk, cur);
        else if (t == 1) op(0, kk, cur);
        else if (t == 2) op(4, kk, cur);
        else if (t == 3) op(1, kk, cur);
      } else {
        if (t == 0) op(4, kk, cur);
        else if (t == 2) op(0, kk, cur);
        else if (t == 4) op(1, kk, cur);
      }
    });
    half<H>(F0, F1, L, ra(nxt, 1), acc, [&](int t) {
      if (H == 0) {
        if (t == 0) op(2, kk, cur);
        else if (t == 1) op(5, kk, cur);
        else if (t == 2) op(3, kk, cur);
      } else {
        if (t == 0) op(5, kk, cur);
        else if (t == 2) op(2, kk, cur);
        else if (t == 3) op(3, kk, cur);
      }
    });
    if (H == 0) {
      if (s == (g >> 2)) {
        const v4u ones = {0x3F803F80u, 0x3F803F80u, 0x3F803F80u, 0x3F803F80u};
        const u32 a1 = nxt + (tg < 3 ? ga0 + 32 * tg : gm0), a2 = tg < 3 ? a1 + G3D : a1, a3 = nxt + (tg & 1) * 2048;
        const v4u f0 = tr2(L, a1, 112), f1 = tr2(L, a1 + 3584, 112), f2_ = tr2(L, a2, 112), f3 = tr2(L, a2 + 3584, 112);
        const v4u f4_ = lds128(L, a3 + sa0), f5 = lds128(L, a3 + (sa0 ^ 64));
        const f4 z4 = {0, 0, 0, 0};
        const f4 r1 = mfma(ones, f1, mfma(ones, f0, z4)), r2 = mfma(ones, f3, mfma(ones, f2_, z4)), r3 = mfma(ones, f5, mfma(ones, f4_, z4));
        const int t1 = tg == 0 ? 2 : tg == 3 ? 4 : 3 + 2 * tg;
        const int T = q == 0 ? t1 : q == 1 ? t1 + 1 : (tg & 1);
        const bool on = q == 0 || (q == 1 && tg < 3) || (q == 2 && tg < 2);
        const float v = q == 0 ? r1[0] : q == 1 ? r2[0] : r3[0];
        __builtin_amdgcn_raw_buffer_store_b64(v2u{f2u(v), tagLo}, rs, on ? WS_CS + ((j * 144 + T * 16 + c) * 8 + g) * 8 : 0x7FFFFFF0, 0, AUX_SC1);
      }
      SCHED();
    }
    cur = nxt;
    nxt = nxt == 2 * SLOT ? 0 : nxt + SLOT;
  }
  const Pw z = pw_setup(lane, w, g & 3);
  const u32 lw2 = lbase + w * PW;
  v4u gr, gc[3];
  const u32 gso = WS_SG + (g * 256 + 64 * tg) * 16, cso = WS_CS + (j * 9 + tg) * 1024, cso8 = WS_CS + (j * 9 + 8) * 1024;
#define GATHER(PRE_, POST_)                                                                                                              \
  asm volatile(PRE_ "buffer_load_dwordx4 %0, %4, %5, %6 offen sc1\n\tbuffer_load_dwordx4 %1, %4, %5, %7 offen sc1\n\t"                    \
                    "buffer_load_dwordx4 %2, %4, %5, %8 offen sc1\n\tbuffer_load_dwordx4 %3, %4, %5, %9 offen sc1" POST_                  \
               : "=&v"(gr), "=&v"(gc[0]), "=&v"(gc[1]), "=&v"(gc[2])                                                                     \
               : "v"(lane * 16), "s"(rs), "s"(gso), "s"(cso), "s"(cso + 4096), "s"(cso8)                                                 \
               : "memory")
  float mcol[4], rcol[4];
  f4 mrow[4], rrow[4], cs[NT];
  const u32 gra = GR + tg * 256 + (16 * r4 + (c & 3)) * 4, grr = GR + tg * 256 + 64 * q;
  auto w2p = [&](auto P, auto U) { glds2<unitb(U.value)>(z.vwe, z.vwo, sw2, p2_pair(r7, P.value), lw2); };
#define W2P(p_, u_) w2p(IC<p_>{}, IC<u_>{})
  WAITL0();
  DSTAMP(31);
  WAITV(6);
  DSTAMP(32);
  BAR();
  DSTAMP(33);
  SCHED();
  half<H>(F1, F0, L, ra(0, 0), acc, [&](int t) {
    if (H == 0 && t == 0) GATHER("", "");
    if (t == 1) W2P(0, 0);
  });
  half<H>(F0, F1, L, ra(0, 1), acc, [&](int t) {
    if (t == 0) W2P(1, 1);
    if (t == 2) W2P(2, 2);
  });
  if (H == 0) {
    asm volatile("s_waitcnt vmcnt(6)" : "+v"(gr), "+v"(gc[0]), "+v"(gc[1]), "+v"(gc[2])::"memory");
#pragma unroll 1
    for (u32 spins = 0; spins < SPIN_CAP; spins++) {
      const u32 bad = (gr.y ^ tagLo) | (gr.w ^ tagHi) | (gc[0].y ^ tagLo) | (gc[0].w ^ tagLo) | (gc[1].y ^ tagLo) | (gc[1].w ^ tagLo) |
                      (gc[2].y ^ tagLo) | (gc[2].w ^ tagLo);
      if (allok(bad == 0)) break;
      GATHER("s_sleep 1\n\t", "\n\ts_waitcnt vmcnt(0)");
    }
    const u32 pos = GR + tg * 256 + ((lane & 48) + 4 * ((lane >> 1) & 3) + 2 * (lane & 1) + ((lane >> 3) & 1)) * 4;
    *(LAS u32*)(L + pos) = gr.x;
    *(LAS u32*)(L + pos + 1024) = gr.z;
    SCHED();
  }
  WAITL0();
  DSTAMP(34);
  WAITV(6);
  DSTAMP(35);
  BAR();
  DSTAMP(36);
  SCHED();
  half<H>(F1, F0, L, ra(SLOT, 0), acc, [&](int t) {
    if (t == 0) W2P(3, 3);
    if (t == 2) W2P(4, 4);
  });
  half<H>(F0, F1, L, ra(SLOT, 1), acc, [&](int t) {
    if (t == 0) W2P(5, 5);
    if (t == 3) { WAITV(8); rdw(wf[0], L, z, q, 0, 1); }
  });
  if (H == 0) {
#pragma unroll
    for (int k = 0; k < 3; k++) {
      float v = u2f(gc[k].x) + u2f(gc[k].z);
      v += dpp<0xB1>(v);
      v += dpp<0x4E>(v);
      *(LAS float*)(L + CST + (k < 2 ? tg + 4 * k : 8) * 64 + (lane >> 2) * 4) = v;
    }
    SCHED();
  }
  WAITL0();
  DSTAMP(37);
  BAR();
  DSTAMP(39);
#pragma unroll
  for (int t = 0; t < NT; t++) {
    if (H == 0 && t < 2) cs[t][0] = ldsf(L, CST + 64 * t + c * 4);
    else cs[t] = ldsf4(L, CST + ((H == 0 || t == 4) ? 0 : 320) + 64 * t + 16 * q);
    if (t < 4) {
      mcol[t] = ldsf(L, gra + 16 * t);
      rcol[t] = ldsf(L, gra + 16 * t + 1024);
      if (H == 0) { mrow[t] = ldsf4(L, grr + 16 * t); rrow[t] = ldsf4(L, grr + 16 * t + 1024); }
    }
    mmtile<H>(F1, acc[t], t);
#pragma unroll
    for (int k = 0; k < 4; k++) { SGB(0x8, 1); SGB(0x100, 2); }
    PIN4(acc[t]);
    SCHED();
    if (t == 0) { W2P(6, 0); W2P(7, 1); }
    if (t == 1) { W2P(8, 6); W2P(9, 7); }
    if (t == 2) W2P(10, 8);
    if (t == NT - 1) { WAITV(10); rdw(wf[1], L, z, q, 2, 3); rdw(wf[2], L, z, q, 4, 5); }
    SCHED();
  }
  STAMP(3);
  CSTAMP(15);
  __builtin_amdgcn_s_setprio(H ? 3 : APRIO);
  const int oct0 = 44 * (j >> 2) + 16 + 7 * (j & 3);
  const u32 hbs = WS_HB + (g * 4 + tg) * 4 * HB_TILE + oct0 * 256;
  const int u = j >> 3, n = (j + NSH) & 7, xn = g & 3;
  const u32 wo = w * PW;
  const srd_t sci = mksrd((const char*)p.cim + (size_t)n * (64 * 1024) + w * 128, 64 * 1024 - w * 128);
  const u32 fso = WS_FL + ((g * 4 + u) * 32 + 4 * w) * 32, fmine = WS_FL + ((g * 4 + tg) * 8 + (j >> 2)) * 128;
  u32 l16 = lane * 16;
  auto cp = [&](auto K) { glds2<unitb(cunit(K.value >> 2, K.value & 3))>(z.vce, z.vco, K.value < 4 ? scr : sci, ((K.value & 3) ^ xn) * 16384, lw2); };
#define CP(k_) cp(IC<k_>{})
  auto setflag = [&](int k) { __builtin_amdgcn_raw_buffer_store_b64(v2u{tagLo, tagHi}, rs, l16 ? 0x7FFFFFF0 : fmine + (k < 2 ? 2 * (j & 3) + k : 8 + (j & 3)) * 8, 0, AUX_SC1); };
  v2u xv[2], dk;
  auto ldx = [&]() {
    const int dcol = 64 * n + 16 * (r4 ^ xn) + 4 * pp;
    const u32 oo = (64 * u + 16 * tg + 8 * (q & 1) + 2 * H + (q >> 1)) * 512 + dcol;
    asm volatile("buffer_load_dwordx2 %0, %3, %4, 0 offen\n\tbuffer_load_dwordx2 %1, %3, %4, %7 offen\n\tglobal_load_dwordx2 %2, %5, %6"
                 : "=&v"(xv[0]), "=&v"(xv[1]), "=&v"(dk)
                 : "v"(oo * 2), "s"(sx), "v"(dcol * 2), "s"(p.dsk), "s"(4096)
                 : "memory");
  };
  if (H == 1) {
    const u32 hv = c * 16 + (2 + (q >> 1) + 2 * (q & 1)) * 256 + hbs;
#pragma unroll
    for (int i = 0; i < 4; i++) {
      const f2 nm = sp(-mcol[i]);
      const float nrl = rcol[i] * NL2E, ir2 = rcp(rcol[i] * rcol[i]);
      u32 v[4];
#pragma unroll
      for (int k = 0; k < 4; k++) {
        const int t = 2 * (k >> 1), h = k & 1;
        v[k] = swiglu2(fma2(nm, half2(cs[t], h), half2(acc[t][i], h)), fma2(nm, half2(cs[t + 1], h), half2(acc[t + 1][i], h)), nrl, ir2);
      }
      __builtin_amdgcn_raw_buffer_store_b128(swap16x2(v[0], v[1], v[2], v[3]), rs, hv + i * HB_TILE, 0, AUX_SC1);
      SCHED();
      if (i == 0) { CSTAMP(18); BAR(); CSTAMP(19); }
      if (i == 3) { WAITV(6); rdw(wf[3], L, z, q, 0, 1); rdw(wf[4], L, z, q, 6, 7); SCHED(); }
    }
    {
      u32 zx[2], zy[2];
#pragma unroll
      for (int ip = 0; ip < 2; ip++) {
        float rlo = rcol[2 * ip];
        asm volatile("" : "+v"(rlo));
        const float rsel = q < 2 ? rlo : rcol[2 * ip + 1];
        const float nrl = rsel * NL2E, ir2 = rcp(rsel * rsel);
        f4 gm, um;
#pragma unroll
        for (int r = 0; r < 4; r++) {
          u32 a = f2u(ffma(-mcol[2 * ip], cs[4][r], acc[4][2 * ip][r])), b = f2u(ffma(-mcol[2 * ip + 1], cs[4][r], acc[4][2 * ip + 1][r]));
          sw32(a, b);
          gm[r] = u2f(a);
          um[r] = u2f(b);
        }
        SWPAD2(gm, um);
        zx[ip] = swiglu2(gm.xy, um.xy, nrl, ir2);
        zy[ip] = swiglu2(gm.zw, um.zw, nrl, ir2);
      }
      __builtin_amdgcn_raw_buffer_store_b128(swap16x2(zx[0], zy[0], zx[1], zy[1]), rs, c * 16 + (2 * (q & 1) + (q >> 1)) * HB_TILE + hbs + 6 * 256, 0, AUX_SC1);
      SCHED();
    }
    STAMP(4);
    __builtin_amdgcn_s_setprio(1);
    ldx();
    WAITL0();
    W2P(11, 2); W2P(12, 3); W2P(13, 4);
    WAITV(9);
    setflag(1);
    STAMP(5);
    CP(0); CP(1); CP(2); CP(3); CP(4);
    asm volatile("s_waitcnt vmcnt(11)" : "+v"(xv[0]), "+v"(xv[1]), "+v"(dk)::"memory");
    rdw(wf[5], L, z, q, 8, 2); rdw(wf[6], L, z, q, 3, 4);
    WAITL0();
    CP(5); CP(6); CP(7);
    CSTAMP(28);
  } else {
#pragma unroll
    for (int k = 0; k < 8; k++) {
      const f2 m2 = half2(mrow[k >> 1], k & 1), r2v = half2(rrow[k >> 1], k & 1);
      sa.u[k].r = fma2(-m2, sp(cs[0][0]), half2(acc[0][k >> 1], k & 1)) * r2v;
      sa.u[k].i = fma2(-m2, sp(cs[1][0]), half2(acc[1][k >> 1], k & 1)) * r2v;
    }
    cf2 e2 = sa.u[0];
#pragma unroll
    for (int k = 1; k < 8; k++) e2 = cfma2(sa.a1, e2, sa.u[k]);
    {
      sa.e0 = cf{e2.r.x, e2.i.x};
      const cf e = cfma(sa.a8, sa.e0, cf{e2.r.y, e2.i.y});
      float er[4], ei[4];
      bcast4(e.r, er);
      bcast4(e.i, ei);
      const cf i0{er[0], ei[0]};
      const cf i1 = cfma(sa.a16, i0, cf{er[1], ei[1]});
      const cf i2 = cfma(sa.a16, i1, cf{er[2], ei[2]});
      const cf i3 = cfma(sa.a16, i2, cf{er[3], ei[3]});
      *(LAS v2u*)(L + XCH + (c * 4 + tg) * 8) = v2u{f2u(i3.r), f2u(i3.i)};
      sa.pq = csel(q & 2, csel(q & 1, i2, i1), csel(q & 1, i0, cf{0.f, 0.f}));
    }
    SCHED();
    CSTAMP(18);
    WAITL0();
    BAR();
    CSTAMP(19);
    {
      const f4 w01 = ldsf4(L, XCH + c * 32), w23 = ldsf4(L, XCH + c * 32 + 16);
      const cf p0{w01.x, w01.y};
      const cf p1 = cfma(sa.a64, p0, cf{w01.z, w01.w});
      const cf p2 = cfma(sa.a64, p1, cf{w23.x, w23.y});
      const cf p3 = cfma(sa.a64, p2, cf{w23.z, w23.w});
      const v4u o = {f2u(p3.r), tagLo, f2u(p3.i), tagHi};
      __builtin_amdgcn_raw_buffer_store_b128(o, rs, (q | tg) ? 0x7FFFFFF0 : WS_AG + (g * 512 + 16 * j + c) * 16, 0, AUX_SC1);
      sa.lw = csel(tg & 2, csel(tg & 1, p2, p1), csel(tg & 1, p0, cf{0.f, 0.f}));
    }
    SCHED();
    STAMP(8);
    if (!AW2L) { W2P(11, 2); W2P(12, 3); W2P(13, 4); CP(0); }
    SCHED();
#pragma unroll
    for (int ip = 0; ip < 2; ip++) {
      u32 px[2], py[2];
#pragma unroll
      for (int ii = 0; ii < 2; ii++) {
        const int i = 2 * ip + ii;
        const f2 nm = sp(-mcol[i]);
        const float nrl = rcol[i] * NL2E, ir2 = rcp(rcol[i] * rcol[i]);
        px[ii] = swiglu2(fma2(nm, cs[2].xy, acc[2][i].xy), fma2(nm, cs[3].xy, acc[3][i].xy), nrl, ir2);
        py[ii] = swiglu2(fma2(nm, cs[2].zw, acc[2][i].zw), fma2(nm, cs[3].zw, acc[3][i].zw), nrl, ir2);
      }
      __builtin_amdgcn_raw_buffer_store_b128(swap16x2(px[0], py[0], px[1], py[1]), rs, c * 16 + (q & 1) * HB_TILE + (q >> 1) * 256 + hbs + 2 * ip * HB_TILE, 0,
                                             AUX_SC1);
      SCHED();
      if (ip == 0) {
        if (AW2L) WAITV(4); else WAITV(12);
        rdw(wf[3], L, z, q, 0, 1); rdw(wf[4], L, z, q, 6, 7);
        SCHED();
      }
    }
    STAMP(4);
    ldx();
    const u32 ao = WS_AG + (q * 512 + 16 * j + c) * 16;
    v4u g0, g1;
    asm volatile("buffer_load_dwordx4 %0, %2, %3, 0 offen sc1\n\tbuffer_load_dwordx4 %1, %2, %3, %4 offen sc1"
                 : "=&v"(g0), "=&v"(g1)
                 : "v"(ao), "s"(rs), "s"(4 * 512 * 16)
                 : "memory");
    if (AW2L) { W2P(11, 2); W2P(12, 3); W2P(13, 4); CP(0); }
    const cf one{1.f, 0.f}, zero{0.f, 0.f};
    asm volatile("" : "+v"(sa.a16.r), "+v"(sa.a16.i), "+v"(sa.a64.r), "+v"(sa.a64.i));
    {
      const cf a128 = csq(sa.a64), a256 = csq(a128), a512 = csq(a256), a1024 = csq(a512);
      sa.cw = cmul(sa.c2, cmul(csel(q & 1, sa.a16, one), csel(q & 2, csq(sa.a16), one)));
      sa.wt = cmul(sa.cw, cmul(csel(tg & 1, sa.a64, one), csel(tg & 2, a128, one)));
      const int e0 = g - 1 - q, e1 = g - 5 - q;
      sa.wg1 = cmul(csel(e0 & 1, a256, one), csel(e0 & 2, a512, one));
      sa.wg0 = csel(e0 >= 0, cmul(sa.wg1, csel(e0 & 4, a1024, one)), zero);
      sa.wg1 = csel(e1 >= 0, sa.wg1, zero);
      asm volatile("" : "+v"(sa.wg0.r), "+v"(sa.wg0.i), "+v"(sa.wg1.r), "+v"(sa.wg1.i));
    }
    const cf wt8 = cmul(sa.wt, sa.a8);
    const cf k0 = cfma(sa.cw, sa.lw, cmul(sa.c2, sa.pq));
    const cf k1 = cfma(sa.a8, k0, cmul(sa.c2, sa.e0));
    cf2 h = cf2{f2{k0.r, k1.r}, f2{k0.i, k1.i}}, wt2 = cf2{f2{sa.wt.r, wt8.r}, f2{sa.wt.i, wt8.i}};
#pragma unroll
    for (int k = 0; k < 8; k++) {
      sa.u[k] = cmul2(sa.c2, sa.u[k]);
      asm volatile("" : "+v"(sa.u[k].r), "+v"(sa.u[k].i));
    }
    asm volatile("" : "+v"(h.r), "+v"(h.i), "+v"(wt2.r), "+v"(wt2.i));
    CSTAMP(25);
    if (AW2L) WAITV(13); else WAITV(5);
    setflag(0);
    STAMP(5);
    if (AW2L) WAITV(3);
    rdw(wf[5], L, z, q, 8, 2); rdw(wf[6], L, z, q, 3, 4);
    asm volatile("s_waitcnt vmcnt(1)" : "+v"(g0), "+v"(g1), "+v"(xv[0]), "+v"(xv[1]), "+v"(dk)::"memory");
#pragma unroll 1
    for (u32 spins = 0; spins < SPIN_CAP; spins++) {
      if (allok((q >= g || (g0.y == tagLo && g0.w == tagHi)) && (q + 4 >= g || (g1.y == tagLo && g1.w == tagHi)))) break;
      asm volatile("s_sleep 1\n\tbuffer_load_dwordx4 %0, %2, %3, 0 offen sc1\n\tbuffer_load_dwordx4 %1, %2, %3, %4 offen sc1\n\ts_waitcnt vmcnt(0)"
                   : "=&v"(g0), "=&v"(g1)
                   : "v"(ao), "s"(rs), "s"(4 * 512 * 16)
                   : "memory");
    }
    {
      cf hin = cfma(sa.wg0, csel(q < g, cf{u2f(g0.x), u2f(g0.z)}, zero), cmul(sa.wg1, csel(q + 4 < g, cf{u2f(g1.x), u2f(g1.z)}, zero)));
      hin.r = sumx32(sumx16(hin.r));
      hin.i = sumx32(sumx16(hin.i));
      h = cfma2(hin, wt2, h);
    }
    STAMP(6);
    const u32 tb = TRB + tg * 1280;
    const u32 twr = tb + c * 40 + q * 8;
    const u32 trd = tb + (q >> 1) * 640 + (8 * (q & 1) + r4) * 40 + pp * 8;
    v4u o[4];
#pragma unroll
    for (int i = 0; i < 4; i++) {
      const cf2 ha = cfma2(sa.a1, h, sa.u[2 * i]);
      h = cfma2(sa.a1, ha, sa.u[2 * i + 1]);
      *(LAS v2u*)(L + twr) = v2u{pk2(ha.r), pk2(h.r)};
      *(LAS v2u*)(L + twr + 640) = v2u{pk2(-ha.i), pk2(-h.i)};
      asm volatile("" ::: "memory");
      o[i] = tr2(L, trd, 160);
      asm volatile("" ::: "memory");
    }
#pragma unroll
    for (int i = 0; i < 4; i++)
      __builtin_amdgcn_raw_buffer_store_b128(o[i], rs, lane * 16 + WS_HB + ((g * 4 + tg) * 4 + i) * HB_TILE + (11 * (j >> 2) + (j & 3)) * 1024, 0, AUX_SC1);
    CSTAMP(26);
    WAITL0();
    CP(1); CP(2); CP(3);
    WAITV(6);
    setflag(2);
    __builtin_amdgcn_s_setprio(1);
    CP(4); CP(5); CP(6); CP(7);
    CSTAMP(28);
  }
  f4 sk[2];
  float smm[2], srr[2];
#pragma unroll
  for (int e = 0; e < 2; e++) {
    const u32 sp_ = GR + u * 256 + (16 * tg + 4 * (2 * e + H) + q) * 4;
    smm[e] = ldsf(L, sp_);
    srr[e] = ldsf(L, sp_ + 1024);
  }
  asm volatile("" : "+v"(smm[0]), "+v"(smm[1]), "+v"(srr[0]), "+v"(srr[1]));
  s16u gfv;
  glu_read(gfv, p.ws + fso);
  asm volatile("" : "+v"(xv[0]), "+v"(xv[1]));
#pragma unroll
  for (int e = 0; e < 2; e++) {
    const float mm = smm[e], r = srr[e];
    const float xs[4] = {bf2f(xv[e].x), bfhi(xv[e].x), bf2f(xv[e].y), bfhi(xv[e].y)};
    const float ds[4] = {bf2f(dk.x), bfhi(dk.x), bf2f(dk.y), bfhi(dk.y)};
#pragma unroll
    for (int k = 0; k < 4; k++) sk[e][k] = xs[k] + rbf(rbf(rbf(xs[k] - mm) * r) * ds[k]);
    asm volatile("" : "+v"(sk[e]));
  }
  asm volatile("s_waitcnt lgkmcnt(0)" : "+s"(gfv)::"memory");
  STAMP(7);

  __builtin_amdgcn_s_setprio(0);
  poll_glu(gfv, p.ws + fso, rs, l16, fso, tagLo, tagHi);
  s8u sfv;
  STAMP(9);
  const u32 hs = WS_HB + (g * 4 + u) * 4 * HB_TILE + 11 * w * 1024;
  f4 ac2[4][4];
#pragma unroll
  for (int i = 0; i < 4; i++)
#pragma unroll
    for (int nn = 0; nn < 4; nn++) ac2[i][nn] = f4{0, 0, 0, 0};
  v4u hf[44];
  auto ldh = [&](int m) {
    const int i = m < 28 ? (m & 3) : ((m - 28) >> 2);
    const u32 so = hs + i * HB_TILE + (m < 28 ? 4 + rot7(m >> 2, r7) : ((m - 28) & 3)) * 1024;
    asm volatile("buffer_load_dwordx4 %0, %1, %2, %3 offen sc1" : "=v"(hf[m]) : "v"(l16), "s"(rs), "s"(so) : "memory");
  };
  u32 ub[4];
  int c2_ = 0, q2 = 0;
  auto ldc = [&](int s) {
    const u32 ca = ((2 * s + (q2 & 1)) ^ ((c2_ >> 1) & 7)) << 4;
#pragma unroll
    for (int nn = 0; nn < 4; nn++) wf[s][nn] = lds128(L, ub[nn] + ca);
  };
  auto wrt = [&](int i) {
#pragma unroll
    for (int nn = 0; nn < 4; nn++)
      *(LAS f4*)(L + wo + unitb(2 * i + (nn >> 1)) + (nn & 1) * 1024 + q2 * 256 + ((c2_ ^ (4 * nn + q2)) << 4)) = ac2[i][nn];
  };
  const srd_t so = mksrd(p.out + (size_t)g * (256 * 512), 256 * 512 * 4);
  f4 pv[8];
  auto redrd = [&](int e) {
    const u32 rda = ((c2_ >> 3) ? unitb(4 * e + 2 * H + 1) : unitb(4 * e + 2 * H)) + ((c2_ >> 2) & 1) * 1024 + (c2_ & 3) * 256 + (((4 * tg + q2) ^ c2_) << 4);
#pragma unroll
    for (int v = 0; v < 8; v++) pv[v] = ldsf4(L, v * PW + rda);
  };
  auto redst = [&](int e) {
    const f4 s = (((pv[0] + pv[1]) + (pv[2] + pv[3])) + ((pv[4] + pv[5]) + (pv[6] + pv[7]))) + sk[e];
    const u32 o = (64 * u + 16 * tg + 8 * (q2 & 1) + 2 * (2 * e + H) + (q2 >> 1)) * 512 + 64 * n + 16 * ((c2_ >> 2) ^ xn) + 4 * (c2_ & 3);
    __builtin_amdgcn_raw_buffer_store_b128(__builtin_bit_cast(v4u, s), so, o * 4, 0, AUX_SC1);
  };
  __builtin_amdgcn_s_setprio(SPRIO ? H : 0);
#pragma unroll
  for (int m = 0; m < HI0; m++) ldh(m);
  CSTAMP(29);
  static_for<0, 44>([&](auto M) {
    constexpr int m = M.value;
    constexpr int i = m < 28 ? (m & 3) : ((m - 28) >> 2), ks = m < 28 ? (m >> 2) : ((m - 28) & 3);
    constexpr int nw = issued_upto(m - 1) - pos_h(m) - 1 + (m > 40 ? 1 : 0);
    static_assert(nw >= 0 && nw < 64 && hiss(37) == 44, "vmcnt");
    asm volatile("s_waitcnt vmcnt(%1)" : "+v"(hf[m]) : "n"(nw) : "memory");
#pragma unroll
    for (int nn = 0; nn < 4; nn++) ac2[i][nn] = mfma(wf[ks][nn], hf[m], ac2[i][nn]);
    PIN4(ac2[i]);
    SCHED();
    if (m == HCHK) {
      STAMP(10);
      asm volatile("s_waitcnt lgkmcnt(0)" : "+s"(sfv)::"memory");
      poll_ssm(sfv, p.ws + fso, rs, l16, fso, tagLo, tagHi);
      STAMP(11);
    }
#pragma unroll
    for (int k = hiss(m - 1); k < hiss(m); k++) ldh(k);
    if (m == HFL) ssm_read(sfv, p.ws + fso);
    if (m == 17) {
      asm volatile("" : "+v"(l16));
      c2_ = (l16 >> 4) & 15;
      q2 = l16 >> 8;
#pragma unroll
      for (int nn = 0; nn < 4; nn++) ub[nn] = wo + c2_ * 128 + ((q2 >> 1) ? unitb(cunit(1, nn)) : unitb(cunit(0, nn)));
    }
    if (m == 19) ldc(0);
    if (m == 21) ldc(1);
    if (m == 23) ldc(2);
    if (m == 25) ldc(3);
    if (m == 32) wrt(0);
    if (m == 36) wrt(1);
    if (m == 38) { WAITL0(); BAR(); redrd(0); }
    if (m == 40) { redst(0); wrt(2); }
    if (m == 43) wrt(3);
    SCHED();
  });
  STAMP(12);
  WAITL0();
  CSTAMP(33);
  BAR();
  CSTAMP(34);
  redrd(1);
  redst(1);
  if (H == 0 && g == 7 && j == 0 && w == 0 && l16 == 0) __hip_atomic_store((GAS u64*)(p.ws + WS_EP), tag, RLX_AGENT);
  STAMP(13);
}

}

__global__ __launch_bounds__(512, 2) void s5_glu_fused(Args p) {
  __shared__ __attribute__((aligned(256))) char smem[LDS_BYTES];
  LAS char* L = (LAS char*)smem;
  const u32 lbase = (u32)(size_t)L;
  int lane = threadIdx.x & 63;
  const int w = __builtin_amdgcn_readfirstlane(threadIdx.x >> 6);
  const int b = blockIdx.x, xcd = b & 7, idx = b >> 3;
  const int g = (xcd & 1) * 4 + (idx & 3), j = (xcd >> 1) * 8 + (idx >> 2);
  const int rot = 2 * (((idx >> 2) + idx) & 3);
  u32 stampv = 0;
  STAMP(0);
  {
    u64 pc;
    asm volatile("s_getpc_b64 %0" : "=s"(pc));
    const u32 off = (w * 64 + lane) * 128;
    if (w * 8 < CODEPF && (idx & 3) == 0)
      asm volatile("s_mov_b32 m0, %2\n\ts_nop 0\n\tglobal_load_lds_dword %0, %1" ::"v"(off < CODEPF * 1024 ? off : 0), "s"(pc & ~127ull), "s"(lbase + DUMMY) : "memory", "m0");
  }
  const srd_t sx = mksrd((const char*)p.x + (size_t)g * (256 * 1024), 256 * 1024);
  PSTAMP(4);
  u32 vo[5], vs;
  stage_offsets(lane, w, vo, vs);
  const srd_t sb = mksrd((const char*)((w & 2) ? p.bim : p.bre) + (size_t)j * (16 * 1024), 16 * 1024);
  if (w < 4) glds<0>(vs, sb, rot * 128, lbase + SM_OFF + w * 1024, 0);
  glds<0>(vo[0], sx, rot * 128, lbase + w * 4096, 0);
  glds<1024>(vo[1], sx, rot * 128, lbase + w * 4096, 0);
  glds<2048>(vo[2], sx, rot * 128, lbase + w * 4096, 0);
  glds<3072>(vo[3], sx, rot * 128, lbase + w * 4096, 0);
  const srd_t s1 = mksrd((const char*)p.w1 + j * 112, 512 * 3584 - j * 112), s3 = mksrd((const char*)p.w3 + j * 112, 512 * 3584 - j * 112);
  glds56(vo[4], s1, rot * (64 * 3584), lbase + G_OFF + w * 896, 0);
  glds56(vo[4], s3, rot * (64 * 3584), lbase + G_OFF + w * 896, G3D);
  SCHED();
  PSTAMP(5);
  const int n = (j + NSH) & 7, xn = g & 3, r7 = R7M == 0 ? rot7(n == 7 ? 0 : n, 2 * xn) : R7M == 1 ? (n == 7 ? 0 : n) : 0;
  const srd_t rs = mksrd(p.ws, WS_BYTES);
  const srd_t sw2 = mksrd((const char*)p.w2 + (size_t)w * (224 * 1024) + n * 128, 224 * 1024 - n * 128);
  const srd_t scr = mksrd((const char*)p.cre + (size_t)n * (64 * 1024) + w * 128, 64 * 1024 - w * 128);
  int wb = w;
  asm volatile("" : "+s"(wb));
  if (wb >= 4) {
    asm volatile("" : "+v"(lane));
    phase1<1>(p, L, lbase, rs, sx, s1, s3, sw2, scr, sb, g, j, rot, r7, w, lane, stampv);
    __builtin_amdgcn_endpgm();
  }
  phase1<0>(p, L, lbase, rs, sx, s1, s3, sw2, scr, sb, g, j, rot, r7, w, lane, stampv);
}

extern "C" void kernel_launch(void* const* d_in, const int* in_sizes, int n_in, void* d_out, int out_size, void* d_ws, size_t ws_size,
                              hipStream_t stream) {
  Args p;
  p.x = (const u16*)d_in[0];
  p.lre = (const u16*)d_in[1];
  p.lim = (const u16*)d_in[2];
  p.bre = (const u16*)d_in[3];
  p.bim = (const u16*)d_in[4];
  p.cre = (const u16*)d_in[5];
  p.cim = (const u16*)d_in[6];
  p.ldt = (const u16*)d_in[7];
  p.dsk = (const u16*)d_in[8];
  p.w1 = (const u16*)d_in[10];
  p.w3 = (const u16*)d_in[11];
  p.w2 = (const u16*)d_in[12];
  p.out = (float*)d_out;
  p.ws = (unsigned char*)d_ws;
  s5_glu_fused<<<256, 512, 0, stream>>>(p);
}
```

```cpp
#include <hip/hip_runtime.h>
#include <stdint.h>

typedef unsigned short u16;
typedef unsigned int u32;
typedef unsigned long long u64;
typedef u32 v4u __attribute__((ext_vector_type(4)));
typedef u32 v2u __attribute__((ext_vector_type(2)));
typedef float f4 __attribute__((ext_vector_type(4)));
typedef float f2 __attribute__((ext_vector_type(2)));
typedef short v4s __attribute__((ext_vector_type(4)));
typedef __bf16 v8bf __attribute__((ext_vector_type(8)));
typedef __bf16 v2bf __attribute__((ext_vector_type(2)));
typedef __amdgpu_buffer_rsrc_t srd_t;
#define LAS __attribute__((address_space(3)))
#define GAS __attribute__((address_space(1)))
#define DI __device__ __forceinline__
#define RLX_AGENT __ATOMIC_RELAXED, __HIP_MEMORY_SCOPE_AGENT

namespace {
constexpr int SM_OFF = 32768, G_OFF = 36864, G3D = 7184, SLOT = 51456, AUXO = 3 * SLOT;
constexpr int GR = AUXO, CST = AUXO + 2048, TRB = AUXO + 4096, XCH = 49152, DUMMY = AUXO + 9216;
constexpr int LDS_BYTES = 163840;
constexpr int PW = 6144;
constexpr int unitb(int v) { return ((v / 3 + 2) % 3) * SLOT + (v % 3) * 2048; }
constexpr int WS_EP = 0, WS_FL = 4096, WS_AG = 65536, WS_SG = 196608, WS_CS = 262144, WS_HB = 1 << 20, HB_TILE = 88 * 1024;
constexpr int WS_BYTES = WS_HB + 128 * HB_TILE;
constexpr int AUX_SC1 = 16;
constexpr int CODEPF = 28;
constexpr u32 SPIN_CAP = 1u << 17;
constexpr float NL2E = -1.4426950408889634f;

DI float bf2f(u32 b) { return __builtin_bit_cast(float, b << 16); }
DI float bfhi(u32 b) { return __builtin_bit_cast(float, b & 0xFFFF0000u); }
DI float u2f(u32 b) { return __builtin_bit_cast(float, b); }
DI u32 f2u(float f) { return __builtin_bit_cast(u32, f); }
DI float rbf(float f) { return (float)(__bf16)f; }
DI u32 pk2(f2 a) { return __builtin_bit_cast(u32, __builtin_convertvector(a, v2bf)); }
DI float rcp(float x) { return __builtin_amdgcn_rcpf(x); }
DI float ffma(float a, float b, float c) { return __builtin_fmaf(a, b, c); }
DI f2 fma2(f2 a, f2 b, f2 c) { return __builtin_elementwise_fma(a, b, c); }
DI f2 sp(float a) { return f2{a, a}; }
DI f2 half2(f4 v, int h) { return h ? v.zw : v.xy; }
DI f4 mfma(v4u a, v4u b, f4 c) {
  return __builtin_amdgcn_mfma_f32_16x16x32_bf16(__builtin_bit_cast(v8bf, a), __builtin_bit_cast(v8bf, b), c, 0, 0, 0);
}
DI v4u lds128(LAS char* L, u32 a) { return *(LAS v4u*)(L + a); }
DI f4 ldsf4(LAS char* L, u32 a) { return *(LAS f4*)(L + a); }
DI float ldsf(LAS char* L, u32 a) { return *(LAS float*)(L + a); }
DI v4u tr2(LAS char* L, u32 a, int d) {
  v2u lo = __builtin_bit_cast(v2u, __builtin_amdgcn_ds_read_tr16_b64_v4i16((LAS v4s*)(L + a)));
  v2u hi = __builtin_bit_cast(v2u, __builtin_amdgcn_ds_read_tr16_b64_v4i16((LAS v4s*)(L + a + d)));
  return v4u{lo.x, lo.y, hi.x, hi.y};
}
DI srd_t mksrd(const void* p, int bytes) { return __builtin_amdgcn_make_buffer_rsrc((void*)p, 0, bytes, 0x00020000); }
DI u32 rfl(u32 x) { return __builtin_amdgcn_readfirstlane(x); }
#pragma clang diagnostic ignored "-Winline-asm"
template <int OFF> DI void glds(u32 voff, srd_t srd, u32 soff, u32 la, u32 lb) {
  asm volatile("s_add_u32 m0, %3, %4\n\ts_nop 0\n\tbuffer_load_dwordx4 %0, %1, %2 offen offset:%5 lds" ::"v"(voff), "s"(srd), "s"(soff), "s"(la), "s"(lb),
               "n"(OFF)
               : "memory", "m0", "scc");
}
DI void glds56(u32 voff, srd_t srd, u32 soff, u32 la, u32 lb) {
  asm volatile("s_add_u32 m0, %3, %4\n\ts_mov_b32 exec_hi, 0xffffff\n\tbuffer_load_dwordx4 %0, %1, %2 offen lds\n\ts_mov_b32 exec_hi, -1" ::"v"(voff),
               "s"(srd), "s"(soff), "s"(la), "s"(lb)
               : "memory", "m0", "scc");
}
template <int LB> DI void glds2(u32 ve, u32 vo, srd_t srd, u32 soff, u32 la) {
  asm volatile("s_add_u32 m0, %4, %5\n\ts_nop 0\n\tbuffer_load_dwordx4 %0, %2, %3 offen lds\n\tbuffer_load_dwordx4 %1, %2, %3 offen offset:1024 lds" ::"v"(ve),
               "v"(vo), "s"(srd), "s"(soff), "s"(rfl(la)), "n"(LB)
               : "memory", "m0", "scc");
}
#define WAITV(n) asm volatile("s_waitcnt vmcnt(" #n ")" ::: "memory")
#define WAITL0() do { asm volatile("" ::: "memory"); __builtin_amdgcn_s_waitcnt(0xC07F); asm volatile("" ::: "memory"); } while (0)
#define BAR() __builtin_amdgcn_s_barrier()
#define SCHED() __builtin_amdgcn_sched_barrier(0)
#define SGB(m, n) __builtin_amdgcn_sched_group_barrier(m, n, 0)
#define PIN4(a) asm volatile("" : "+v"((a)[0]), "+v"((a)[1]), "+v"((a)[2]), "+v"((a)[3]))

template <int V> struct IC { static constexpr int value = V; };
template <int B, int E, class F> DI void static_for(F f) {
  if constexpr (B < E) {
    f(IC<B>{});
    static_for<B + 1, E>(f);
  }
}
DI bool allok(bool c) { return __builtin_amdgcn_ballot_w64(!c) == 0; }
struct cf { float r, i; };
DI float afma(float a, float b, float c) { float r; asm("v_fma_f32 %0, %1, %2, %3" : "=v"(r) : "v"(a), "v"(b), "v"(c)); return r; }
DI float anfma(float a, float b, float c) { float r; asm("v_fma_f32 %0, -%1, %2, %3" : "=v"(r) : "v"(a), "v"(b), "v"(c)); return r; }
DI cf cmul(cf a, cf b) { return cf{anfma(a.i, b.i, a.r * b.r), ffma(a.r, b.i, a.i * b.r)}; }
DI cf cfma(cf a, cf b, cf c) { return cf{afma(a.r, b.r, anfma(a.i, b.i, c.r)), ffma(a.r, b.i, ffma(a.i, b.r, c.i))}; }
DI cf csq(cf a) { return cf{anfma(a.i, a.i, a.r * a.r), 2.f * a.r * a.i}; }
DI cf csel(bool p, cf a, cf b) { return cf{p ? a.r : b.r, p ? a.i : b.i}; }
struct cf2 { f2 r, i; };
DI cf2 cfma2(cf a, cf2 b, cf2 c) { return cf2{fma2(sp(a.r), b.r, fma2(sp(-a.i), b.i, c.r)), fma2(sp(a.r), b.i, fma2(sp(a.i), b.r, c.i))}; }
DI cf2 cmul2(cf a, cf2 b) { return cf2{fma2(sp(a.r), b.r, sp(-a.i) * b.i), fma2(sp(a.r), b.i, sp(a.i) * b.r)}; }
template <int CTRL> DI float dpp(float x) { return u2f(__builtin_amdgcn_mov_dpp(f2u(x), CTRL, 0xf, 0xf, true)); }
DI float sum16(float v) {
  v += dpp<0xB1>(v); v += dpp<0x4E>(v); v += dpp<0x141>(v); v += dpp<0x140>(v);
  return v;
}
DI void sw16(u32& a, u32& b) { auto r = __builtin_amdgcn_permlane16_swap(a, b, false, false); a = r[0]; b = r[1]; }
DI void sw32(u32& a, u32& b) { auto r = __builtin_amdgcn_permlane32_swap(a, b, false, false); a = r[0]; b = r[1]; }
#define SWPAD2(a, b) asm volatile("s_nop 1" : "+v"(a), "+v"(b))
#define SWPAD4(a, b, c, d) asm volatile("s_nop 1" : "+v"(a), "+v"(b), "+v"(c), "+v"(d))
DI float sumx16(float v) { u32 a = f2u(v), b = a; sw16(a, b); SWPAD2(a, b); return u2f(a) + u2f(b); }
DI float sumx32(float v) { u32 a = f2u(v), b = a; sw32(a, b); SWPAD2(a, b); return u2f(a) + u2f(b); }
DI void bcast4(float v, float (&o)[4]) {
  u32 a = f2u(v), b = a;
  sw16(a, b);
  SWPAD2(a, b);
  u32 a2 = a, b2 = b;
  sw32(a, a2);
  sw32(b, b2);
  SWPAD4(a, b, a2, b2);
  o[0] = u2f(a); o[1] = u2f(b); o[2] = u2f(a2); o[3] = u2f(b2);
}
DI v4u swap16x2(u32 ax, u32 ay, u32 bx, u32 by) {
  sw16(ax, bx);
  sw16(ay, by);
  v4u o = {ax, ay, bx, by};
  asm volatile("s_nop 1" : "+v"(o));
  return o;
}

struct Args {
  const u16 *x, *lre, *lim, *bre, *bim, *cre, *cim, *ldt, *dsk, *w1, *w3, *w2;
  float* out;
  unsigned char* ws;
};

DI u32 swiglu2(f2 tg, f2 tu, float nrl, float ir2) {
  const f2 a = tg * sp(nrl);
  const f2 e = {__builtin_amdgcn_exp2f(a.x), __builtin_amdgcn_exp2f(a.y)};
  const f2 d = fma2(e, sp(ir2), sp(ir2));
  const f2 r = {rcp(d.x), rcp(d.y)};
  return pk2(tg * tu * r);
}

template <int H> struct Fr { v4u x[4]; v4u w[H == 0 ? 4 : 5]; };
struct Ra { u32 xa, sa, ga, gm; };

template <int H> DI void ldpiece(Fr<H>& F, LAS char* L, const Ra& a, int k) {
  if (k == 0) { F.x[0] = lds128(L, a.xa); F.x[1] = lds128(L, a.xa + 256); F.x[2] = lds128(L, a.xa + 512); F.x[3] = lds128(L, a.xa + 768); }
  if (H == 0) {
    if (k == 1) { F.w[0] = lds128(L, a.sa); F.w[1] = lds128(L, a.sa + 2048); F.w[2] = tr2(L, a.ga, 112); }
    if (k == 2) F.w[3] = tr2(L, a.ga + G3D, 112);
  } else {
    if (k == 1) { F.w[0] = tr2(L, a.ga + 32, 112); F.w[1] = tr2(L, a.ga + G3D + 32, 112); }
    if (k == 2) { F.w[2] = tr2(L, a.ga + 64, 112); F.w[3] = tr2(L, a.ga + G3D + 64, 112); }
    if (k == 3) F.w[4] = tr2(L, a.gm, 112);
  }
}
template <int H> DI void mmtile(const Fr<H>& F, f4 (&acc)[4], int t) {
#pragma unroll
  for (int i = 0; i < 4; i++) {
    if (H == 0 && t < 2) acc[i] = mfma(F.x[i], F.w[t], acc[i]);
    else acc[i] = mfma(F.w[t], F.x[i], acc[i]);
  }
}
template <int H, class I>
DI void half(const Fr<H>& Fc, Fr<H>& Fn, LAS char* L, const Ra& a, f4 (&acc)[H == 0 ? 4 : 5][4], I iss) {
  constexpr int NT = H == 0 ? 4 : 5;
#pragma unroll
  for (int t = 0; t < NT; t++) {
    ldpiece<H>(Fn, L, a, t);
    mmtile<H>(Fc, acc[t], t);
#pragma unroll
    for (int k = 0; k < 4; k++) { SGB(0x8, 1); SGB(0x100, 1); }
    PIN4(acc[t]);
    SCHED();
    iss(t);
    SCHED();
  }
}

DI int rot7(int s, int r) { const int k = s + r; return k >= 7 ? k - 7 : k; }
DI u32 p2_pair(int r, int spn) { return (2 * rot7(spn >> 1, r) + (spn & 1)) * 16384; }
constexpr int cunit(int m, int nn) { return m == 0 ? (nn == 0 ? 5 : nn == 3 ? 6 : nn - 1) : (nn == 0 ? 7 : 1 + nn); }

struct Sa {
  cf2 u[8];
  cf a1, a8, a16, a64, pq, lw, e0, c2, cw, wt, wg0, wg1;
};
struct Pw {
  u32 vwe, vwo, vce, vco, tl[4];
};
DI Pw pw_setup(int lane, int w, int xn) {
  Pw z;
  const int q = lane >> 4, r4 = (lane & 15) >> 2, pp = lane & 3;
  z.vwe = (lane >> 3) * 1024 + (((lane & 7) ^ (2 * (q & 1))) << 4);
  z.vwo = (z.vwe ^ 64) + 8192 - 1024;
  z.vce = (lane >> 3) * 1024 + (((lane & 7) ^ q) << 4);
  z.vco = (z.vce ^ 64) + 8192 - 1024;
#pragma unroll
  for (int nn = 0; nn < 4; nn++)
    z.tl[nn] = w * PW + (q & 1) * 1024 + r4 * 128 + ((((2 * (nn ^ xn)) ^ (2 * (((r4 >> 1) & 1) + 2 * (q & 1)))) + (pp >> 1)) << 4) + (pp & 1) * 8;
  return z;
}
DI void rdw(v4u (&wf)[4], LAS char* L, const Pw& z, int q, int ua, int ub) {
  const u32 o = (q >> 1) ? unitb(ub) : unitb(ua);
#pragma unroll
  for (int nn = 0; nn < 4; nn++) wf[nn] = tr2(L, o + z.tl[nn], 512);
}

constexpr int HW = 8, HCHK = 28 - HW, HFL = HCHK - 6;
constexpr int hiss(int m) { return m + 1 + HW < 44 ? m + 1 + HW : 44; }
static_assert(hiss(HCHK - 1) == 28, "schedule");

typedef u32 s16u __attribute__((ext_vector_type(16)));
typedef u32 s8u __attribute__((ext_vector_type(8)));
template <class V, int N> DI bool flags_match(V v, u32 tagLo, u32 tagHi) {
  u32 x = 0;
#pragma unroll
  for (int k = 0; k < N; k += 2) x |= (v[k] ^ tagLo) | (v[k + 1] ^ tagHi);
  return x == 0;
}
DI bool flags_vec(srd_t rs, u32 l16, u32 so, u32 tagLo, u32 tagHi, u32 amask) {
  v2u v;
  asm volatile("buffer_load_dwordx2 %0, %1, %2, %3 offen sc1\n\ts_waitcnt vmcnt(0)" : "=&v"(v) : "v"((l16 >> 1) & amask), "s"(rs), "s"(so) : "memory");
  return allok(v.x == tagLo && v.y == tagHi);
}
DI void glu_read(s16u& v, const unsigned char* fp) { asm volatile("s_load_dwordx16 %0, %1, 0x0 glc" : "=&s"(v) : "s"(fp) : "memory"); }
DI void poll_glu(s16u v, const unsigned char* fp, srd_t rs, u32 l16, u32 so, u32 tagLo, u32 tagHi) {
#pragma unroll 1
  for (u32 spins = 1; spins < SPIN_CAP; spins++) {
    if (flags_match<s16u, 16>(v, tagLo, tagHi)) break;
    if ((spins & 7) == 0 && flags_vec(rs, l16, so, tagLo, tagHi, 0x38)) break;
    asm volatile("s_load_dwordx16 %0, %1, 0x0 glc\n\ts_waitcnt lgkmcnt(0)" : "=&s"(v) : "s"(fp) : "memory");
  }
  asm volatile("" ::: "memory");
}
DI void ssm_read(s8u& v, const unsigned char* fp) { asm volatile("s_load_dwordx8 %0, %1, 0x40 glc" : "=&s"(v) : "s"(fp) : "memory"); }
DI void poll_ssm(s8u v, const unsigned char* fp, srd_t rs, u32 l16, u32 so, u32 tagLo, u32 tagHi) {
#pragma unroll 1
  for (u32 spins = 1; spins < SPIN_CAP; spins++) {
    if (flags_match<s8u, 8>(v, tagLo, tagHi)) break;
    if ((spins & 7) == 0 && flags_vec(rs, l16, so + 64, tagLo, tagHi, 0x18)) break;
    __builtin_amdgcn_s_sleep(2);
    asm volatile("s_load_dwordx8 %0, %1, 0x40 glc\n\ts_waitcnt lgkmcnt(0)" : "=&s"(v) : "s"(fp) : "memory");
  }
  asm volatile("" ::: "memory");
}

DI void stage_offsets(int lane, int w, u32 (&vo)[5], u32& vs) {
#pragma unroll
  for (int e = 0; e < 4; e++) vo[e] = (32 * w + 8 * e + (lane >> 3)) * 1024 + (((lane & 7) ^ ((4 * w + e) & 7)) << 4) - e * 1024;
  const int gpos = (lane * 37) >> 8, gch = lane - gpos * 7;
  vo[4] = (8 * w + 4 * (gpos & 1) + (gpos >> 1)) * 3584 + gch * 16;
  vs = (8 * (w & 1) + (lane >> 3)) * 1024 + (((lane & 7) ^ ((4 * (w & 1) + (lane >> 4)) & 7)) << 4);
}

template <int H>
DI void wave_program(const Args& p, LAS char* L, u32 lbase, srd_t rs, srd_t sx, srd_t s1, srd_t s3, srd_t sw2, srd_t scr, srd_t sb, int g, int j, int rot, int r7, int w,
               int lane) {
  constexpr int NT = H == 0 ? 4 : 5;
  u32 vo[5], vs;
  stage_offsets(lane, w, vo, vs);
  Sa sa;
  v4u wf[7][4];
  const int tg = w & 3, c = lane & 15, q = lane >> 4, r4 = c >> 2, pp = lane & 3;
  const u32 ld0 = lbase + w * 4096, ld4 = lbase + G_OFF + w * 896, ld5 = ld4 + G3D, lds_s = lbase + SM_OFF + w * 1024;
  auto op = [&](int i, u32 kk, u32 slot) {
    if (i == 0) glds<0>(vo[0], sx, kk * 128, ld0, slot);
    else if (i == 1) glds<1024>(vo[1], sx, kk * 128, ld0, slot);
    else if (i == 2) glds<2048>(vo[2], sx, kk * 128, ld0, slot);
    else if (i == 3) glds<3072>(vo[3], sx, kk * 128, ld0, slot);
    else if (i == 4) glds56(vo[4], s1, kk * (64 * 3584), ld4, slot);
    else if (i == 5) glds56(vo[4], s3, kk * (64 * 3584), ld5, slot);
    else if (H == 0) glds<0>(vs, sb, kk * 128, lds_s, slot);
  };
  v2u ep;
  u32 pl0 = 0, pl1 = 0, pl2 = 0;
  v4u xr0 = {0, 0, 0, 0}, xr1 = {0, 0, 0, 0};
  {
    const u32 zero = 0;
    asm volatile("global_load_dwordx2 %0, %1, %2 sc1" : "=v"(ep) : "v"(zero), "s"(p.ws + WS_EP) : "memory");
    if (H == 0) {
      const u32 o = (16 * j + c) * 2;
      asm volatile("global_load_ushort %0, %1, %2" : "=v"(pl0) : "v"(o), "s"(p.lre) : "memory");
      asm volatile("global_load_ushort %0, %1, %2" : "=v"(pl1) : "v"(o), "s"(p.lim) : "memory");
      asm volatile("global_load_ushort %0, %1, %2" : "=v"(pl2) : "v"(o), "s"(p.ldt) : "memory");
      const u32 ro = (8 * j + 2 * tg + (lane >> 5)) * 1024 + (lane & 31) * 16;
      asm volatile("buffer_load_dwordx4 %0, %1, %2, 0 offen" : "=v"(xr0) : "v"(ro), "s"(sx) : "memory");
      asm volatile("buffer_load_dwordx4 %0, %1, %2, 0 offen offset:512" : "=v"(xr1) : "v"(ro), "s"(sx) : "memory");
    }
  }
  u32 tagLo = 0, tagHi = 0;
  if (H == 0) {
    asm volatile("s_waitcnt vmcnt(0)" : "+v"(ep), "+v"(pl0), "+v"(pl1), "+v"(pl2), "+v"(xr0), "+v"(xr1)::"memory");
    const u64 tg64 = (((u64)rfl(ep.y) << 32) | rfl(ep.x)) + 1;
    tagLo = (u32)tg64; tagHi = (u32)(tg64 >> 32);
    f2 v[8];
#pragma unroll
    for (int k = 0; k < 4; k++) { v[k] = f2{bf2f(xr0[k]), bfhi(xr0[k])}; v[4 + k] = f2{bf2f(xr1[k]), bfhi(xr1[k])}; }
    const f2 sm = ((v[0] + v[1]) + (v[2] + v[3])) + ((v[4] + v[5]) + (v[6] + v[7]));
    const float m = sumx16(sum16(sm.x + sm.y)) * (1.f / 512.f);
    f2 s2 = {0.f, 0.f};
#pragma unroll
    for (int k = 0; k < 8; k++) { const f2 d = v[k] - sp(m); s2 = fma2(d, d, s2); }
    const float var = sumx16(sum16(s2.x + s2.y)) * (1.f / 512.f);
    const float sd = rbf(__builtin_amdgcn_sqrtf(rbf(rbf(var) + 1e-5f)));
    const v4u o = {f2u(rbf(m)), tagLo, f2u(rcp(sd)), tagHi};
    __builtin_amdgcn_raw_buffer_store_b128(o, rs, (lane & 31) ? 0x7FFFFFF0 : WS_SG + (g * 256 + 8 * j + 2 * tg + (lane >> 5)) * 16, 0, AUX_SC1);
    SCHED();
  }
  op(6, (rot + 1) & 7, SLOT);
#pragma unroll
  for (int i = 0; i < 6; i++) op(i, (rot + 1) & 7, SLOT);
  if (H == 0) {
    const float lamr = bf2f(pl0), lami = bf2f(pl1), dt = rbf(expf(bf2f(pl2)));
    float sn, cn;
    sincosf(dt * lami, &sn, &cn);
    const float e = expf(dt * lamr);
    sa.a1 = cf{e * cn, e * sn};
    sa.a8 = csq(csq(csq(sa.a1)));
    sa.a16 = csq(sa.a8);
    sa.a64 = csq(csq(sa.a16));
    const float nr = sa.a1.r - 1.f, ni = sa.a1.i, den = 2.f / (lamr * lamr + lami * lami);
    sa.c2 = cf{(nr * lamr + ni * lami) * den, (ni * lamr - nr * lami) * den};
    asm volatile("" : "+v"(sa.a1.r), "+v"(sa.a1.i), "+v"(sa.a8.r), "+v"(sa.a8.i), "+v"(sa.a16.r), "+v"(sa.a16.i), "+v"(sa.a64.r), "+v"(sa.a64.i),
                 "+v"(sa.c2.r), "+v"(sa.c2.i));
    SCHED();
  }
  const u32 xa0 = (64 * tg + 16 * r4 + 8 * (c & 1) + ((c >> 1) & 1)) * 128 + ((q ^ (2 * r4 + (c & 1))) << 4);
  const u32 sa0 = SM_OFF + c * 128 + ((q ^ ((c >> 1) & 7)) << 4);
  const u32 ga0 = G_OFF + q * 896 + r4 * 224 + 8 * pp;
  const u32 gm0 = ga0 + 96 + (pp >= 2 ? G3D - 16 : 0);
  auto ra = [&](u32 slot, int ks) { return Ra{slot + (xa0 ^ (ks * 64)), slot + (sa0 ^ (ks * 64)), slot + ga0 + ks * 3584, slot + gm0 + ks * 3584}; };
  f4 acc[NT][4];
#pragma unroll
  for (int t = 0; t < NT; t++)
#pragma unroll
    for (int i = 0; i < 4; i++) acc[t][i] = f4{0, 0, 0, 0};
  if (H == 1) WAITV(7);
  BAR();
  Fr<H> F0, F1;
  {
    const Ra a = ra(0, 0);
#pragma unroll
    for (int t = 0; t < NT; t++) ldpiece<H>(F0, L, a, t);
    SCHED();
  }
  half<H>(F0, F1, L, ra(0, 1), acc, [&](int t) {
    if (t == 0) op(6, (rot + 2) & 7, 2 * SLOT);
    if (t < 3) { op(2 * t, (rot + 2) & 7, 2 * SLOT); op(2 * t + 1, (rot + 2) & 7, 2 * SLOT); }
  });
  if (H == 1) {
    asm volatile("s_waitcnt vmcnt(12)" : "+v"(ep)::"memory");
    const u64 tg64 = (((u64)rfl(ep.y) << 32) | rfl(ep.x)) + 1;
    tagLo = (u32)tg64; tagHi = (u32)(tg64 >> 32);
  }
  if (H == 0 && tg < 3) {
    const int b = g * 32 + j;
    const char* src = tg == 0 ? (const char*)p.w2 + b * 7168 : (const char*)(tg == 1 ? p.cre : p.cim) + b * 2048;
    const u32 off = (tg == 0 ? (lane < 56 ? lane : 0) : (lane & 15)) * 128;
    asm volatile("s_mov_b32 m0, %2\n\ts_nop 0\n\tglobal_load_lds_dword %0, %1" ::"v"(off), "s"(src), "s"(lbase + DUMMY) : "memory", "m0");
  }
  const u64 tag = ((u64)tagHi << 32) | tagLo;
  u32 cur = 0, nxt = SLOT;
  SCHED();
#pragma unroll 1
  for (int s = 0; s < 5; s++) {
    const Ra an0 = ra(nxt, 0);
    const u32 kk = (rot + s + 3) & 7;
    WAITL0();
    WAITV(6);
    BAR();
    SCHED();
    half<H>(F1, F0, L, an0, acc, [&](int t) {
      if (H == 0) {
        if (t == 0) op(6, kk, cur);
        else if (t == 1) op(0, kk, cur);
        else if (t == 2) op(4, kk, cur);
        else if (t == 3) op(1, kk, cur);
      } else {
        if (t == 0) op(4, kk, cur);
        else if (t == 2) op(0, kk, cur);
        else if (t == 4) op(1, kk, cur);
      }
    });
    half<H>(F0, F1, L, ra(nxt, 1), acc, [&](int t) {
      if (H == 0) {
        if (t == 0) op(2, kk, cur);
        else if (t == 1) op(5, kk, cur);
        else if (t == 2) op(3, kk, cur);
      } else {
        if (t == 0) op(5, kk, cur);
        else if (t == 2) op(2, kk, cur);
        else if (t == 3) op(3, kk, cur);
      }
    });
    if (H == 0) {
      if (s == (g >> 2)) {
        const v4u ones = {0x3F803F80u, 0x3F803F80u, 0x3F803F80u, 0x3F803F80u};
        const u32 a1 = nxt + (tg < 3 ? ga0 + 32 * tg : gm0), a2 = tg < 3 ? a1 + G3D : a1, a3 = nxt + (tg & 1) * 2048;
        const v4u f0 = tr2(L, a1, 112), f1 = tr2(L, a1 + 3584, 112), f2_ = tr2(L, a2, 112), f3 = tr2(L, a2 + 3584, 112);
        const v4u f4_ = lds128(L, a3 + sa0), f5 = lds128(L, a3 + (sa0 ^ 64));
        const f4 z4 = {0, 0, 0, 0};
        const f4 r1 = mfma(ones, f1, mfma(ones, f0, z4)), r2 = mfma(ones, f3, mfma(ones, f2_, z4)), r3 = mfma(ones, f5, mfma(ones, f4_, z4));
        const int t1 = tg == 0 ? 2 : tg == 3 ? 4 : 3 + 2 * tg;
        const int T = q == 0 ? t1 : q == 1 ? t1 + 1 : (tg & 1);
        const bool on = !(c & 1) && (q == 0 || (q == 1 && tg < 3) || (q == 2 && tg < 2));
        const float v = q == 0 ? r1[0] : q == 1 ? r2[0] : r3[0];
        const v4u o = {f2u(v), tagLo, f2u(dpp<0xB1>(v)), tagHi};
        __builtin_amdgcn_raw_buffer_store_b128(o, rs, on ? WS_CS + (j * 9 + T) * 1024 + ((c >> 1) * 8 + g) * 16 : 0x7FFFFFF0, 0, AUX_SC1);
      }
      SCHED();
    }
    cur = nxt;
    nxt = nxt == 2 * SLOT ? 0 : nxt + SLOT;
  }
  const Pw z = pw_setup(lane, w, g & 3);
  const u32 lw2 = lbase + w * PW;
  v4u gr, gc[3];
  const u32 gso = WS_SG + (g * 256 + 64 * tg) * 16, cso = WS_CS + (j * 9 + tg) * 1024, cso8 = WS_CS + (j * 9 + 8) * 1024;
#define GATHER(PRE_, POST_)                                                                                                              \
  asm volatile(PRE_ "buffer_load_dwordx4 %0, %4, %5, %6 offen sc1\n\tbuffer_load_dwordx4 %1, %4, %5, %7 offen sc1\n\t"                    \
                    "buffer_load_dwordx4 %2, %4, %5, %8 offen sc1\n\tbuffer_load_dwordx4 %3, %4, %5, %9 offen sc1" POST_                  \
               : "=&v"(gr), "=&v"(gc[0]), "=&v"(gc[1]), "=&v"(gc[2])                                                                     \
               : "v"(lane * 16), "s"(rs), "s"(gso), "s"(cso), "s"(cso + 4096), "s"(cso8)                                                 \
               : "memory")
  float mcol[4], rcol[4];
  f4 mrow[4], rrow[4], cs[NT];
  const u32 gra = GR + tg * 256 + (16 * r4 + (c & 3)) * 4, grr = GR + tg * 256 + 64 * q;
  auto w2p = [&](auto P, auto U) { glds2<unitb(U.value)>(z.vwe, z.vwo, sw2, p2_pair(r7, P.value), lw2); };
#define W2P(p_, u_) w2p(IC<p_>{}, IC<u_>{})
  WAITL0();
  WAITV(6);
  BAR();
  SCHED();
  half<H>(F1, F0, L, ra(0, 0), acc, [&](int t) {
    if (H == 0 && t == 0) GATHER("", "");
    if (t == 1) W2P(0, 0);
  });
  half<H>(F0, F1, L, ra(0, 1), acc, [&](int t) {
    if (t == 0) W2P(1, 1);
    if (t == 2) W2P(2, 2);
  });
  if (H == 0) {
    asm volatile("s_waitcnt vmcnt(6)" : "+v"(gr), "+v"(gc[0]), "+v"(gc[1]), "+v"(gc[2])::"memory");
#pragma unroll 1
    for (u32 spins = 0; spins < SPIN_CAP; spins++) {
      const u32 bad = (gr.y ^ tagLo) | (gr.w ^ tagHi) | (gc[0].y ^ tagLo) | (gc[0].w ^ tagHi) | (gc[1].y ^ tagLo) | (gc[1].w ^ tagHi) |
                      (gc[2].y ^ tagLo) | (gc[2].w ^ tagHi);
      if (allok(bad == 0)) break;
      GATHER("s_sleep 1\n\t", "\n\ts_waitcnt vmcnt(0)");
    }
    const u32 pos = GR + tg * 256 + ((lane & 48) + 4 * ((lane >> 1) & 3) + 2 * (lane & 1) + ((lane >> 3) & 1)) * 4;
    *(LAS u32*)(L + pos) = gr.x;
    *(LAS u32*)(L + pos + 1024) = gr.z;
    SCHED();
  }
  WAITL0();
  WAITV(6);
  BAR();
  SCHED();
  half<H>(F1, F0, L, ra(SLOT, 0), acc, [&](int t) {
    if (t == 0) W2P(3, 3);
    if (t == 2) W2P(4, 4);
  });
  half<H>(F0, F1, L, ra(SLOT, 1), acc, [&](int t) {
    if (t == 0) W2P(5, 5);
    if (t == 3) { WAITV(8); rdw(wf[0], L, z, q, 0, 1); }
  });
  if (H == 0) {
#pragma unroll
    for (int k = 0; k < 3; k++) {
      float v0 = u2f(gc[k].x), v1 = u2f(gc[k].z);
      v0 += dpp<0xB1>(v0); v1 += dpp<0xB1>(v1);
      v0 += dpp<0x4E>(v0); v1 += dpp<0x4E>(v1);
      v0 += dpp<0x141>(v0); v1 += dpp<0x141>(v1);
      *(LAS v2u*)(L + CST + (k < 2 ? tg + 4 * k : 8) * 64 + (lane >> 3) * 8) = v2u{f2u(v0), f2u(v1)};
    }
    SCHED();
  }
  WAITL0();
  BAR();
#pragma unroll
  for (int t = 0; t < NT; t++) {
    if (H == 0 && t < 2) cs[t][0] = ldsf(L, CST + 64 * t + c * 4);
    else cs[t] = ldsf4(L, CST + ((H == 0 || t == 4) ? 0 : 320) + 64 * t + 16 * q);
    if (t < 4) {
      mcol[t] = ldsf(L, gra + 16 * t);
      rcol[t] = ldsf(L, gra + 16 * t + 1024);
      if (H == 0) { mrow[t] = ldsf4(L, grr + 16 * t); rrow[t] = ldsf4(L, grr + 16 * t + 1024); }
    }
    mmtile<H>(F1, acc[t], t);
#pragma unroll
    for (int k = 0; k < 4; k++) { SGB(0x8, 1); SGB(0x100, 2); }
    PIN4(acc[t]);
    SCHED();
    if (t == 0) { W2P(6, 0); W2P(7, 1); }
    if (t == 1) { W2P(8, 6); W2P(9, 7); }
    if (t == 2) W2P(10, 8);
    if (t == NT - 1) { WAITV(10); rdw(wf[1], L, z, q, 2, 3); rdw(wf[2], L, z, q, 4, 5); }
    SCHED();
  }
  __builtin_amdgcn_s_setprio(H ? 3 : 2);
  const int oct0 = 44 * (j >> 2) + 16 + 7 * (j & 3);
  const u32 hbs = WS_HB + (g * 4 + tg) * 4 * HB_TILE + oct0 * 256;
  const int u = j >> 3, n = j & 7, xn = g & 3;
  const u32 wo = w * PW;
  const srd_t sci = mksrd((const char*)p.cim + (size_t)n * (64 * 1024) + w * 128, 64 * 1024 - w * 128);
  const u32 fso = WS_FL + ((g * 4 + u) * 32 + 4 * w) * 32, fmine = WS_FL + ((g * 4 + tg) * 8 + (j >> 2)) * 128;
  u32 l16 = lane * 16;
  auto cp = [&](auto K) { glds2<unitb(cunit(K.value >> 2, K.value & 3))>(z.vce, z.vco, K.value < 4 ? scr : sci, ((K.value & 3) ^ xn) * 16384, lw2); };
#define CP(k_) cp(IC<k_>{})
  auto setflag = [&](int k) { __builtin_amdgcn_raw_buffer_store_b64(v2u{tagLo, tagHi}, rs, l16 ? 0x7FFFFFF0 : fmine + (k < 2 ? 2 * (j & 3) + k : 8 + (j & 3)) * 8, 0, AUX_SC1); };
  v2u xv[2], dk;
  auto ldx = [&]() {
    const int dcol = 64 * n + 16 * (r4 ^ xn) + 4 * pp;
    const u32 oo = (64 * u + 16 * tg + 8 * (q & 1) + 2 * H + (q >> 1)) * 512 + dcol;
    asm volatile("buffer_load_dwordx2 %0, %3, %4, 0 offen\n\tbuffer_load_dwordx2 %1, %3, %4, %7 offen\n\tglobal_load_dwordx2 %2, %5, %6"
                 : "=&v"(xv[0]), "=&v"(xv[1]), "=&v"(dk)
                 : "v"(oo * 2), "s"(sx), "v"(dcol * 2), "s"(p.dsk), "s"(4096)
                 : "memory");
  };
  if (H == 1) {
    const u32 hv = c * 16 + (2 + (q >> 1) + 2 * (q & 1)) * 256 + hbs;
#pragma unroll
    for (int i = 0; i < 4; i++) {
      const f2 nm = sp(-mcol[i]);
      const float nrl = rcol[i] * NL2E, ir2 = rcp(rcol[i] * rcol[i]);
      u32 v[4];
#pragma unroll
      for (int k = 0; k < 4; k++) {
        const int t = 2 * (k >> 1), h = k & 1;
        v[k] = swiglu2(fma2(nm, half2(cs[t], h), half2(acc[t][i], h)), fma2(nm, half2(cs[t + 1], h), half2(acc[t + 1][i], h)), nrl, ir2);
      }
      __builtin_amdgcn_raw_buffer_store_b128(swap16x2(v[0], v[1], v[2], v[3]), rs, hv + i * HB_TILE, 0, AUX_SC1);
      SCHED();
      if (i == 0) { BAR(); }
      if (i == 3) { WAITV(6); rdw(wf[3], L, z, q, 0, 1); rdw(wf[4], L, z, q, 6, 7); SCHED(); }
    }
    {
      u32 zx[2], zy[2];
#pragma unroll
      for (int ip = 0; ip < 2; ip++) {
        float rlo = rcol[2 * ip];
        asm volatile("" : "+v"(rlo));
        const float rsel = q < 2 ? rlo : rcol[2 * ip + 1];
        const float nrl = rsel * NL2E, ir2 = rcp(rsel * rsel);
        f4 gm, um;
#pragma unroll
        for (int r = 0; r < 4; r++) {
          u32 a = f2u(ffma(-mcol[2 * ip], cs[4][r], acc[4][2 * ip][r])), b = f2u(ffma(-mcol[2 * ip + 1], cs[4][r], acc[4][2 * ip + 1][r]));
          sw32(a, b);
          gm[r] = u2f(a);
          um[r] = u2f(b);
        }
        SWPAD2(gm, um);
        zx[ip] = swiglu2(gm.xy, um.xy, nrl, ir2);
        zy[ip] = swiglu2(gm.zw, um.zw, nrl, ir2);
      }
      __builtin_amdgcn_raw_buffer_store_b128(swap16x2(zx[0], zy[0], zx[1], zy[1]), rs, c * 16 + (2 * (q & 1) + (q >> 1)) * HB_TILE + hbs + 6 * 256, 0, AUX_SC1);
      SCHED();
    }
    __builtin_amdgcn_s_setprio(1);
    ldx();
    WAITL0();
    W2P(11, 2); W2P(12, 3); W2P(13, 4);
    WAITV(9);
    setflag(1);
    CP(0); CP(1); CP(2); CP(3); CP(4);
    asm volatile("s_waitcnt vmcnt(11)" : "+v"(xv[0]), "+v"(xv[1]), "+v"(dk)::"memory");
    rdw(wf[5], L, z, q, 8, 2); rdw(wf[6], L, z, q, 3, 4);
    WAITL0();
    CP(5); CP(6); CP(7);
  } else {
#pragma unroll
    for (int k = 0; k < 8; k++) {
      const f2 m2 = half2(mrow[k >> 1], k & 1), r2v = half2(rrow[k >> 1], k & 1);
      sa.u[k].r = fma2(-m2, sp(cs[0][0]), half2(acc[0][k >> 1], k & 1)) * r2v;
      sa.u[k].i = fma2(-m2, sp(cs[1][0]), half2(acc[1][k >> 1], k & 1)) * r2v;
    }
    cf2 e2 = sa.u[0];
#pragma unroll
    for (int k = 1; k < 8; k++) e2 = cfma2(sa.a1, e2, sa.u[k]);
    {
      sa.e0 = cf{e2.r.x, e2.i.x};
      const cf e = cfma(sa.a8, sa.e0, cf{e2.r.y, e2.i.y});
      float er[4], ei[4];
      bcast4(e.r, er);
      bcast4(e.i, ei);
      const cf i0{er[0], ei[0]};
      const cf i1 = cfma(sa.a16, i0, cf{er[1], ei[1]});
      const cf i2 = cfma(sa.a16, i1, cf{er[2], ei[2]});
      const cf i3 = cfma(sa.a16, i2, cf{er[3], ei[3]});
      *(LAS v2u*)(L + XCH + (c * 4 + tg) * 8) = v2u{f2u(i3.r), f2u(i3.i)};
      sa.pq = csel(q & 2, csel(q & 1, i2, i1), csel(q & 1, i0, cf{0.f, 0.f}));
    }
    SCHED();
    WAITL0();
    BAR();
    {
      const f4 w01 = ldsf4(L, XCH + c * 32), w23 = ldsf4(L, XCH + c * 32 + 16);
      const cf p0{w01.x, w01.y};
      const cf p1 = cfma(sa.a64, p0, cf{w01.z, w01.w});
      const cf p2 = cfma(sa.a64, p1, cf{w23.x, w23.y});
      const cf p3 = cfma(sa.a64, p2, cf{w23.z, w23.w});
      const v4u o = {f2u(p3.r), tagLo, f2u(p3.i), tagHi};
      __builtin_amdgcn_raw_buffer_store_b128(o, rs, (q | tg) ? 0x7FFFFFF0 : WS_AG + (g * 512 + 16 * j + c) * 16, 0, AUX_SC1);
      sa.lw = csel(tg & 2, csel(tg & 1, p2, p1), csel(tg & 1, p0, cf{0.f, 0.f}));
    }
    SCHED();
    W2P(11, 2); W2P(12, 3); W2P(13, 4); CP(0);
    SCHED();
#pragma unroll
    for (int ip = 0; ip < 2; ip++) {
      u32 px[2], py[2];
#pragma unroll
      for (int ii = 0; ii < 2; ii++) {
        const int i = 2 * ip + ii;
        const f2 nm = sp(-mcol[i]);
        const float nrl = rcol[i] * NL2E, ir2 = rcp(rcol[i] * rcol[i]);
        px[ii] = swiglu2(fma2(nm, cs[2].xy, acc[2][i].xy), fma2(nm, cs[3].xy, acc[3][i].xy), nrl, ir2);
        py[ii] = swiglu2(fma2(nm, cs[2].zw, acc[2][i].zw), fma2(nm, cs[3].zw, acc[3][i].zw), nrl, ir2);
      }
      __builtin_amdgcn_raw_buffer_store_b128(swap16x2(px[0], py[0], px[1], py[1]), rs, c * 16 + (q & 1) * HB_TILE + (q >> 1) * 256 + hbs + 2 * ip * HB_TILE, 0,
                                             AUX_SC1);
      SCHED();
      if (ip == 0) {
        WAITV(12);
        rdw(wf[3], L, z, q, 0, 1); rdw(wf[4], L, z, q, 6, 7);
        SCHED();
      }
    }
    ldx();
    const u32 ao = WS_AG + (q * 512 + 16 * j + c) * 16;
    v4u g0, g1;
    asm volatile("buffer_load_dwordx4 %0, %2, %3, 0 offen sc1\n\tbuffer_load_dwordx4 %1, %2, %3, %4 offen sc1"
                 : "=&v"(g0), "=&v"(g1)
                 : "v"(ao), "s"(rs), "s"(4 * 512 * 16)
                 : "memory");
    const cf one{1.f, 0.f}, zero{0.f, 0.f};
    asm volatile("" : "+v"(sa.a16.r), "+v"(sa.a16.i), "+v"(sa.a64.r), "+v"(sa.a64.i));
    {
      const cf a128 = csq(sa.a64), a256 = csq(a128), a512 = csq(a256), a1024 = csq(a512);
      sa.cw = cmul(sa.c2, cmul(csel(q & 1, sa.a16, one), csel(q & 2, csq(sa.a16), one)));
      sa.wt = cmul(sa.cw, cmul(csel(tg & 1, sa.a64, one), csel(tg & 2, a128, one)));
      const int e0 = g - 1 - q, e1 = g - 5 - q;
      sa.wg1 = cmul(csel(e0 & 1, a256, one), csel(e0 & 2, a512, one));
      sa.wg0 = csel(e0 >= 0, cmul(sa.wg1, csel(e0 & 4, a1024, one)), zero);
      sa.wg1 = csel(e1 >= 0, sa.wg1, zero);
      asm volatile("" : "+v"(sa.wg0.r), "+v"(sa.wg0.i), "+v"(sa.wg1.r), "+v"(sa.wg1.i));
    }
    const cf wt8 = cmul(sa.wt, sa.a8);
    const cf k0 = cfma(sa.cw, sa.lw, cmul(sa.c2, sa.pq));
    const cf k1 = cfma(sa.a8, k0, cmul(sa.c2, sa.e0));
    cf2 h = cf2{f2{k0.r, k1.r}, f2{k0.i, k1.i}}, wt2 = cf2{f2{sa.wt.r, wt8.r}, f2{sa.wt.i, wt8.i}};
#pragma unroll
    for (int k = 0; k < 8; k++) {
      sa.u[k] = cmul2(sa.c2, sa.u[k]);
      asm volatile("" : "+v"(sa.u[k].r), "+v"(sa.u[k].i));
    }
    asm volatile("" : "+v"(h.r), "+v"(h.i), "+v"(wt2.r), "+v"(wt2.i));
    WAITV(5);
    setflag(0);
    rdw(wf[5], L, z, q, 8, 2); rdw(wf[6], L, z, q, 3, 4);
    asm volatile("s_waitcnt vmcnt(1)" : "+v"(g0), "+v"(g1), "+v"(xv[0]), "+v"(xv[1]), "+v"(dk)::"memory");
#pragma unroll 1
    for (u32 spins = 0; spins < SPIN_CAP; spins++) {
      if (allok((q >= g || (g0.y == tagLo && g0.w == tagHi)) && (q + 4 >= g || (g1.y == tagLo && g1.w == tagHi)))) break;
      asm volatile("s_sleep 1\n\tbuffer_load_dwordx4 %0, %2, %3, 0 offen sc1\n\tbuffer_load_dwordx4 %1, %2, %3, %4 offen sc1\n\ts_waitcnt vmcnt(0)"
                   : "=&v"(g0), "=&v"(g1)
                   : "v"(ao), "s"(rs), "s"(4 * 512 * 16)
                   : "memory");
    }
    {
      cf hin = cfma(sa.wg0, csel(q < g, cf{u2f(g0.x), u2f(g0.z)}, zero), cmul(sa.wg1, csel(q + 4 < g, cf{u2f(g1.x), u2f(g1.z)}, zero)));
      hin.r = sumx32(sumx16(hin.r));
      hin.i = sumx32(sumx16(hin.i));
      h = cfma2(hin, wt2, h);
    }
    const u32 tb = TRB + tg * 1280;
    const u32 twr = tb + c * 40 + q * 8;
    const u32 trd = tb + (q >> 1) * 640 + (8 * (q & 1) + r4) * 40 + pp * 8;
    v4u o[4];
#pragma unroll
    for (int i = 0; i < 4; i++) {
      const cf2 ha = cfma2(sa.a1, h, sa.u[2 * i]);
      h = cfma2(sa.a1, ha, sa.u[2 * i + 1]);
      *(LAS v2u*)(L + twr) = v2u{pk2(ha.r), pk2(h.r)};
      *(LAS v2u*)(L + twr + 640) = v2u{pk2(-ha.i), pk2(-h.i)};
      asm volatile("" ::: "memory");
      o[i] = tr2(L, trd, 160);
      asm volatile("" ::: "memory");
    }
#pragma unroll
    for (int i = 0; i < 4; i++)
      __builtin_amdgcn_raw_buffer_store_b128(o[i], rs, lane * 16 + WS_HB + ((g * 4 + tg) * 4 + i) * HB_TILE + (11 * (j >> 2) + (j & 3)) * 1024, 0, AUX_SC1);
    WAITL0();
    CP(1); CP(2); CP(3);
    WAITV(6);
    setflag(2);
    __builtin_amdgcn_s_setprio(1);
    CP(4); CP(5); CP(6); CP(7);
  }
  f4 sk[2];
  float smm[2], srr[2];
#pragma unroll
  for (int e = 0; e < 2; e++) {
    const u32 sp_ = GR + u * 256 + (16 * tg + 4 * (2 * e + H) + q) * 4;
    smm[e] = ldsf(L, sp_);
    srr[e] = ldsf(L, sp_ + 1024);
  }
  asm volatile("" : "+v"(smm[0]), "+v"(smm[1]), "+v"(srr[0]), "+v"(srr[1]));
  s16u gfv;
  glu_read(gfv, p.ws + fso);
  asm volatile("" : "+v"(xv[0]), "+v"(xv[1]));
#pragma unroll
  for (int e = 0; e < 2; e++) {
    const float mm = smm[e], r = srr[e];
    const float xs[4] = {bf2f(xv[e].x), bfhi(xv[e].x), bf2f(xv[e].y), bfhi(xv[e].y)};
    const float ds[4] = {bf2f(dk.x), bfhi(dk.x), bf2f(dk.y), bfhi(dk.y)};
#pragma unroll
    for (int k = 0; k < 4; k++) sk[e][k] = xs[k] + rbf(rbf(rbf(xs[k] - mm) * r) * ds[k]);
    asm volatile("" : "+v"(sk[e]));
  }
  asm volatile("s_waitcnt lgkmcnt(0)" : "+s"(gfv)::"memory");
  __builtin_amdgcn_s_setprio(0);
  poll_glu(gfv, p.ws + fso, rs, l16, fso, tagLo, tagHi);
  s8u sfv;
  const u32 hs = WS_HB + (g * 4 + u) * 4 * HB_TILE + 11 * w * 1024;
  f4 ac2[4][4];
#pragma unroll
  for (int i = 0; i < 4; i++)
#pragma unroll
    for (int nn = 0; nn < 4; nn++) ac2[i][nn] = f4{0, 0, 0, 0};
  v4u hf[44];
  auto ldh = [&](int m) {
    const int i = m < 28 ? (m & 3) : ((m - 28) >> 2);
    const u32 so = hs + i * HB_TILE + (m < 28 ? 4 + rot7(m >> 2, r7) : ((m - 28) & 3)) * 1024;
    asm volatile("buffer_load_dwordx4 %0, %1, %2, %3 offen sc1" : "=v"(hf[m]) : "v"(l16), "s"(rs), "s"(so) : "memory");
  };
  u32 ub[4];
  int c2_ = 0, q2 = 0;
  auto ldc = [&](int s) {
    const u32 ca = ((2 * s + (q2 & 1)) ^ ((c2_ >> 1) & 7)) << 4;
#pragma unroll
    for (int nn = 0; nn < 4; nn++) wf[s][nn] = lds128(L, ub[nn] + ca);
  };
  auto wrt = [&](int i) {
#pragma unroll
    for (int nn = 0; nn < 4; nn++)
      *(LAS f4*)(L + wo + unitb(2 * i + (nn >> 1)) + (nn & 1) * 1024 + q2 * 256 + ((c2_ ^ (4 * nn + q2)) << 4)) = ac2[i][nn];
  };
  const srd_t so = mksrd(p.out + (size_t)g * (256 * 512), 256 * 512 * 4);
  f4 pv[8];
  auto redrd = [&](int e) {
    const u32 rda = ((c2_ >> 3) ? unitb(4 * e + 2 * H + 1) : unitb(4 * e + 2 * H)) + ((c2_ >> 2) & 1) * 1024 + (c2_ & 3) * 256 + (((4 * tg + q2) ^ c2_) << 4);
#pragma unroll
    for (int v = 0; v < 8; v++) pv[v] = ldsf4(L, v * PW + rda);
  };
  auto redst = [&](int e) {
    const f4 s = (((pv[0] + pv[1]) + (pv[2] + pv[3])) + ((pv[4] + pv[5]) + (pv[6] + pv[7]))) + sk[e];
    const u32 o = (64 * u + 16 * tg + 8 * (q2 & 1) + 2 * (2 * e + H) + (q2 >> 1)) * 512 + 64 * n + 16 * ((c2_ >> 2) ^ xn) + 4 * (c2_ & 3);
    __builtin_amdgcn_raw_buffer_store_b128(__builtin_bit_cast(v4u, s), so, o * 4, 0, AUX_SC1);
  };
  __builtin_amdgcn_s_setprio(H);
#pragma unroll
  for (int m = 0; m < HW; m++) ldh(m);
  static_for<0, 44>([&](auto M) {
    constexpr int m = M.value;
    constexpr int i = m < 28 ? (m & 3) : ((m - 28) >> 2), ks = m < 28 ? (m >> 2) : ((m - 28) & 3);
    constexpr int nw = hiss(m - 1) - m - 1 + (m > 40 ? 1 : 0);
    static_assert(nw >= 0 && nw < 64 && hiss(37) == 44, "vmcnt");
    asm volatile("s_waitcnt vmcnt(%1)" : "+v"(hf[m]) : "n"(nw) : "memory");
#pragma unroll
    for (int nn = 0; nn < 4; nn++) ac2[i][nn] = mfma(wf[ks][nn], hf[m], ac2[i][nn]);
    PIN4(ac2[i]);
    SCHED();
    if (m == HCHK) {
      asm volatile("s_waitcnt lgkmcnt(0)" : "+s"(sfv)::"memory");
      poll_ssm(sfv, p.ws + fso, rs, l16, fso, tagLo, tagHi);
    }
#pragma unroll
    for (int k = hiss(m - 1); k < hiss(m); k++) ldh(k);
    if (m == HFL) ssm_read(sfv, p.ws + fso);
    if (m == 17) {
      asm volatile("" : "+v"(l16));
      c2_ = (l16 >> 4) & 15;
      q2 = l16 >> 8;
#pragma unroll
      for (int nn = 0; nn < 4; nn++) ub[nn] = wo + c2_ * 128 + ((q2 >> 1) ? unitb(cunit(1, nn)) : unitb(cunit(0, nn)));
    }
    if (m == 19) ldc(0);
    if (m == 21) ldc(1);
    if (m == 23) ldc(2);
    if (m == 25) ldc(3);
    if (m == 32) wrt(0);
    if (m == 36) wrt(1);
    if (m == 38) { WAITL0(); BAR(); redrd(0); }
    if (m == 40) { redst(0); wrt(2); }
    if (m == 43) wrt(3);
    SCHED();
  });
  WAITL0();
  BAR();
  redrd(1);
  redst(1);
  if (H == 0 && g == 7 && j == 0 && w == 0 && l16 == 0) __hip_atomic_store((GAS u64*)(p.ws + WS_EP), tag, RLX_AGENT);
}

}

__global__ __launch_bounds__(512, 2) void s5_glu_fused(Args p) {
  __shared__ __attribute__((aligned(256))) char smem[LDS_BYTES];
  LAS char* L = (LAS char*)smem;
  const u32 lbase = (u32)(size_t)L;
  int lane = threadIdx.x & 63;
  const int w = __builtin_amdgcn_readfirstlane(threadIdx.x >> 6);
  const int b = blockIdx.x, xcd = b & 7, idx = b >> 3;
  const int g = (xcd & 1) * 4 + (idx & 3), j = (xcd >> 1) * 8 + (idx >> 2);
  const int rot = 2 * (((idx >> 2) + idx) & 3);
  {
    u64 pc;
    asm volatile("s_getpc_b64 %0" : "=s"(pc));
    const u32 off = (w * 64 + lane) * 128;
    if (w * 8 < CODEPF && (idx & 3) == 0)
      asm volatile("s_mov_b32 m0, %2\n\ts_nop 0\n\tglobal_load_lds_dword %0, %1" ::"v"(off < CODEPF * 1024 ? off : 0), "s"(pc & ~127ull), "s"(lbase + DUMMY) : "memory", "m0");
  }
  const srd_t sx = mksrd((const char*)p.x + (size_t)g * (256 * 1024), 256 * 1024);
  u32 vo[5], vs;
  stage_offsets(lane, w, vo, vs);
  const srd_t sb = mksrd((const char*)((w & 2) ? p.bim : p.bre) + (size_t)j * (16 * 1024), 16 * 1024);
  if (w < 4) glds<0>(vs, sb, rot * 128, lbase + SM_OFF + w * 1024, 0);
  glds<0>(vo[0], sx, rot * 128, lbase + w * 4096, 0);
  glds<1024>(vo[1], sx, rot * 128, lbase + w * 4096, 0);
  glds<2048>(vo[2], sx, rot * 128, lbase + w * 4096, 0);
  glds<3072>(vo[3], sx, rot * 128, lbase + w * 4096, 0);
  const srd_t s1 = mksrd((const char*)p.w1 + j * 112, 512 * 3584 - j * 112), s3 = mksrd((const char*)p.w3 + j * 112, 512 * 3584 - j * 112);
  glds56(vo[4], s1, rot * (64 * 3584), lbase + G_OFF + w * 896, 0);
  glds56(vo[4], s3, rot * (64 * 3584), lbase + G_OFF + w * 896, G3D);
  SCHED();
  const int n = j & 7, xn = g & 3, r7 = n == 7 ? 0 : n;
  const srd_t rs = mksrd(p.ws, WS_BYTES);
  const srd_t sw2 = mksrd((const char*)p.w2 + (size_t)w * (224 * 1024) + n * 128, 224 * 1024 - n * 128);
  const srd_t scr = mksrd((const char*)p.cre + (size_t)n * (64 * 1024) + w * 128, 64 * 1024 - w * 128);
  int wb = w;
  asm volatile("" : "+s"(wb));
  if (wb >= 4) {
    asm volatile("" : "+v"(lane));
    wave_program<1>(p, L, lbase, rs, sx, s1, s3, sw2, scr, sb, g, j, rot, r7, w, lane);
    __builtin_amdgcn_endpgm();
  }
  wave_program<0>(p, L, lbase, rs, sx, s1, s3, sw2, scr, sb, g, j, rot, r7, w, lane);
}

extern "C" void kernel_launch(void* const* d_in, const int* in_sizes, int n_in, void* d_out, int out_size, void* d_ws, size_t ws_size,
                              hipStream_t stream) {
  Args p;
  p.x = (const u16*)d_in[0];
  p.lre = (const u16*)d_in[1];
  p.lim = (const u16*)d_in[2];
  p.bre = (const u16*)d_in[3];
  p.bim = (const u16*)d_in[4];
  p.cre = (const u16*)d_in[5];
  p.cim = (const u16*)d_in[6];
  p.ldt = (const u16*)d_in[7];
  p.dsk = (const u16*)d_in[8];
  p.w1 = (const u16*)d_in[10];
  p.w3 = (const u16*)d_in[11];
  p.w2 = (const u16*)d_in[12];
  p.out = (float*)d_out;
  p.ws = (unsigned char*)d_ws;
  s5_glu_fused<<<256, 512, 0, stream>>>(p);
}
```
